```python
import torch

def _softmax(x, axis=-1):
    return torch.softmax(x, dim=axis)

def _gelu(x):
    return torch.nn.functional.gelu(x, approximate='tanh')

def _silu(x):
    return torch.nn.functional.silu(x)

def _sigmoid(x):
    return torch.sigmoid(x)

def _layernorm(x, axis=-1, eps=1e-05):
    m = x.mean(dim=axis, keepdim=True)
    v = x.var(dim=axis, keepdim=True, unbiased=False)
    return (x - m) / torch.sqrt(v + eps)

def _scatter_add(tgt, idx, src, axis=0):
    return tgt.clone().index_add_(axis, idx, src)

def _segment_sum(src, seg, n):
    out = torch.zeros((n,) + src.shape[1:], dtype=src.dtype)
    return out.index_add_(0, seg, src)

def _pad(x, pw):
    flat = tuple((v for pair in reversed(pw) for v in pair))
    return torch.nn.functional.pad(x, flat)
kernel_name = 'sequential_preLN__memory_aug_recurrent_mem_tr__swiglu_ffn__prompt'

def setup_inputs():

    _w = lambda a: (lambda t: t.to(torch.bfloat16) if t.is_floating_point() else t)(torch.as_tensor(a))

    def _A_setup():
        B, S, Dm = (1, 2048, 1024)
        H, Dh = (32, 128)
        M, seg = (8, 32)
        seg = min(seg, S)
        S = S // seg * seg
        return {'x': _w((lambda *s: torch.randn(*s, dtype=torch.bfloat16) * 0.05)(B, S, Dm)), 'mem0': _w((lambda *s: torch.randn(*s, dtype=torch.bfloat16) * 0.2)(B, M, Dm)), 'Wq': _w((lambda *s: torch.randn(*s, dtype=torch.bfloat16) * 1.6)(Dm, H, Dh) * Dm ** (-0.5)), 'Wk': _w((lambda *s: torch.randn(*s, dtype=torch.bfloat16) * 1.6)(Dm, H, Dh) * Dm ** (-0.5)), 'Wv': _w((lambda *s: torch.randn(*s, dtype=torch.bfloat16) * 0.2)(Dm, H, Dh) * Dm ** (-0.5)), 'Wo': _w((lambda *s: torch.randn(*s, dtype=torch.bfloat16) * 0.2)(H, Dh, Dm) * (H * Dh) ** (-0.5)), 'seg_len': seg}
    _A_in = _A_setup()

    def _B_setup():
        B, S, D, F = (1, 2048, 1024, 3584)
        return {'x': _w((lambda *s: torch.randn(*s, dtype=torch.bfloat16) * 0.05)(B, S, D)), 'w_gate': _w((lambda *s: torch.randn(*s, dtype=torch.bfloat16) * 0.05)(D, F)), 'w_up': _w((lambda *s: torch.randn(*s, dtype=torch.bfloat16) * 0.05)(D, F)), 'w_down': _w((lambda *s: torch.randn(*s, dtype=torch.bfloat16) * 0.05)(F, D))}
    _B_in = _B_setup()
    return {'A_x': _A_in['x'], 'A_mem0': _A_in['mem0'], 'A_Wq': _A_in['Wq'], 'A_Wk': _A_in['Wk'], 'A_Wv': _A_in['Wv'], 'A_Wo': _A_in['Wo'], 'A_seg_len': _A_in['seg_len'], 'B_x': _B_in['x'], 'B_w_gate': _B_in['w_gate'], 'B_w_up': _B_in['w_up'], 'B_w_down': _B_in['w_down']}

def reference(A_x, A_mem0, A_Wq, A_Wk, A_Wv, A_Wo, A_seg_len, B_x, B_w_gate, B_w_up, B_w_down):

    def _A_ref(x, mem0, Wq, Wk, Wv, Wo, seg_len):
        B, S, Dm = x.shape
        H, Dh = (Wq.shape[1], Wq.shape[2])
        M = mem0.shape[1]
        scale = Dh ** (-0.5)
        n_seg = S // seg_len
        L = M + seg_len
        idx = torch.arange(L)
        mask = (idx[:, None] < M) | (idx[:, None] >= idx[None, :])
        mem = mem0
        outs = []
        for s in range(n_seg):
            xs = x[:, s * seg_len:(s + 1) * seg_len, :].clone()
            h = torch.concatenate([mem, xs], axis=1)
            q = torch.einsum('btd,dhe->bhte', h, Wq)
            k = torch.einsum('btd,dhe->bhte', h, Wk)
            v = torch.einsum('btd,dhe->bhte', h, Wv)
            scores = torch.einsum('bhqe,bhke->bhqk', q, k) * scale
            scores = torch.where(mask[None, None], scores, -1000000000.0)
            p = _softmax(scores, axis=-1)
            o = torch.einsum('bhqk,bhke->bhqe', p, v)
            o = torch.einsum('bhte,hed->btd', o, Wo)
            mem = o[:, :M, :].clone()
            outs.append(o[:, M:, :])
        return torch.concatenate(outs, axis=1)

    def _B_ref(x, w_gate, w_up, w_down):
        g = _silu(x @ w_gate)
        u = x @ w_up
        return g * u @ w_down
    x = A_x
    h = x + _A_ref(_layernorm(x, axis=-1), A_mem0, A_Wq, A_Wk, A_Wv, A_Wo, A_seg_len)
    h2 = h + _B_ref(_layernorm(h, axis=-1), B_w_gate, B_w_up, B_w_down)
    return h2
```

```cpp
#include <hip/hip_runtime.h>
#include <hip/hip_bf16.h>
#include <cstdlib>

typedef unsigned u32;
typedef unsigned long long u64;
typedef unsigned short u16;
typedef u32 u32x2 __attribute__((ext_vector_type(2)));
typedef u32 u32x4 __attribute__((ext_vector_type(4)));
typedef u32 u32x8 __attribute__((ext_vector_type(8)));
typedef float f32x2 __attribute__((ext_vector_type(2)));
typedef float f32x4 __attribute__((ext_vector_type(4)));
typedef __bf16 bf16x2 __attribute__((ext_vector_type(2)));
typedef __bf16 bf16x8 __attribute__((ext_vector_type(8)));
typedef short s16x4 __attribute__((ext_vector_type(4)));
#define DI __device__ __forceinline__

#define ASM_G88 \
"s_add_u32 m0, s56, 0\n" \
"s_nop 0\n" \
"buffer_load_dwordx4 v128, s[40:43], s52 offen offset:0 sc1 lds\n" \
"buffer_load_dwordx4 v129, s[40:43], s52 offen offset:1024 sc1 lds\n" \
"buffer_load_dwordx4 v130, s[40:43], s52 offen offset:2048 sc1 lds\n" \
"buffer_load_dwordx4 v131, s[40:43], s52 offen offset:3072 sc1 lds\n" \
"s_add_u32 m0, s57, 0\n" \
"s_nop 0\n" \
"buffer_load_dwordx4 v132, s[44:47], s53 offen offset:0 lds\n" \
"buffer_load_dwordx4 v133, s[48:51], s53 offen offset:1024 lds\n" \
"buffer_load_dwordx4 v134, s[44:47], s53 offen offset:2048 lds\n" \
"buffer_load_dwordx4 v135, s[48:51], s53 offen offset:3072 lds\n" \
"s_add_u32 s52, s52, 1024\n" \
"s_add_u32 s53, s53, s54\n" \
"s_add_u32 m0, s56, 16384\n" \
"s_nop 0\n" \
"buffer_load_dwordx4 v128, s[40:43], s52 offen offset:0 sc1 lds\n" \
"buffer_load_dwordx4 v129, s[40:43], s52 offen offset:1024 sc1 lds\n" \
"buffer_load_dwordx4 v130, s[40:43], s52 offen offset:2048 sc1 lds\n" \
"buffer_load_dwordx4 v131, s[40:43], s52 offen offset:3072 sc1 lds\n" \
"s_add_u32 m0, s57, 16384\n" \
"s_nop 0\n" \
"buffer_load_dwordx4 v132, s[44:47], s53 offen offset:0 lds\n" \
"buffer_load_dwordx4 v133, s[48:51], s53 offen offset:1024 lds\n" \
"buffer_load_dwordx4 v134, s[44:47], s53 offen offset:2048 lds\n" \
"buffer_load_dwordx4 v135, s[48:51], s53 offen offset:3072 lds\n" \
"s_add_u32 s52, s52, 1024\n" \
"s_add_u32 s53, s53, s54\n" \
"s_add_u32 m0, s56, 32768\n" \
"s_nop 0\n" \
"buffer_load_dwordx4 v128, s[40:43], s52 offen offset:0 sc1 lds\n" \
"buffer_load_dwordx4 v129, s[40:43], s52 offen offset:1024 sc1 lds\n" \
"buffer_load_dwordx4 v130, s[40:43], s52 offen offset:2048 sc1 lds\n" \
"buffer_load_dwordx4 v131, s[40:43], s52 offen offset:3072 sc1 lds\n" \
"s_add_u32 m0, s57, 32768\n" \
"s_nop 0\n" \
"buffer_load_dwordx4 v132, s[44:47], s53 offen offset:0 lds\n" \
"buffer_load_dwordx4 v133, s[48:51], s53 offen offset:1024 lds\n" \
"buffer_load_dwordx4 v134, s[44:47], s53 offen offset:2048 lds\n" \
"buffer_load_dwordx4 v135, s[48:51], s53 offen offset:3072 lds\n" \
"s_add_u32 s52, s52, 1024\n" \
"s_add_u32 s53, s53, s54\n" \
"s_add_u32 m0, s56, 49152\n" \
"s_nop 0\n" \
"buffer_load_dwordx4 v128, s[40:43], s52 offen offset:0 sc1 lds\n" \
"buffer_load_dwordx4 v129, s[40:43], s52 offen offset:1024 sc1 lds\n" \
"buffer_load_dwordx4 v130, s[40:43], s52 offen offset:2048 sc1 lds\n" \
"buffer_load_dwordx4 v131, s[40:43], s52 offen offset:3072 sc1 lds\n" \
"s_add_u32 m0, s57, 49152\n" \
"s_nop 0\n" \
"buffer_load_dwordx4 v132, s[44:47], s53 offen offset:0 lds\n" \
"buffer_load_dwordx4 v133, s[48:51], s53 offen offset:1024 lds\n" \
"buffer_load_dwordx4 v134, s[44:47], s53 offen offset:2048 lds\n" \
"buffer_load_dwordx4 v135, s[48:51], s53 offen offset:3072 lds\n" \
"s_add_u32 s52, s52, 1024\n" \
"s_add_u32 s53, s53, s54\n" \
"v_accvgpr_write_b32 a0, 0\n" \
"v_accvgpr_write_b32 a1, 0\n" \
"v_accvgpr_write_b32 a2, 0\n" \
"v_accvgpr_write_b32 a3, 0\n" \
"v_accvgpr_write_b32 a4, 0\n" \
"v_accvgpr_write_b32 a5, 0\n" \
"v_accvgpr_write_b32 a6, 0\n" \
"v_accvgpr_write_b32 a7, 0\n" \
"v_accvgpr_write_b32 a8, 0\n" \
"v_accvgpr_write_b32 a9, 0\n" \
"v_accvgpr_write_b32 a10, 0\n" \
"v_accvgpr_write_b32 a11, 0\n" \
"v_accvgpr_write_b32 a12, 0\n" \
"v_accvgpr_write_b32 a13, 0\n" \
"v_accvgpr_write_b32 a14, 0\n" \
"v_accvgpr_write_b32 a15, 0\n" \
"v_accvgpr_write_b32 a16, 0\n" \
"v_accvgpr_write_b32 a17, 0\n" \
"v_accvgpr_write_b32 a18, 0\n" \
"v_accvgpr_write_b32 a19, 0\n" \
"v_accvgpr_write_b32 a20, 0\n" \
"v_accvgpr_write_b32 a21, 0\n" \
"v_accvgpr_write_b32 a22, 0\n" \
"v_accvgpr_write_b32 a23, 0\n" \
"v_accvgpr_write_b32 a24, 0\n" \
"v_accvgpr_write_b32 a25, 0\n" \
"v_accvgpr_write_b32 a26, 0\n" \
"v_accvgpr_write_b32 a27, 0\n" \
"v_accvgpr_write_b32 a28, 0\n" \
"v_accvgpr_write_b32 a29, 0\n" \
"v_accvgpr_write_b32 a30, 0\n" \
"v_accvgpr_write_b32 a31, 0\n" \
"v_accvgpr_write_b32 a32, 0\n" \
"v_accvgpr_write_b32 a33, 0\n" \
"v_accvgpr_write_b32 a34, 0\n" \
"v_accvgpr_write_b32 a35, 0\n" \
"v_accvgpr_write_b32 a36, 0\n" \
"v_accvgpr_write_b32 a37, 0\n" \
"v_accvgpr_write_b32 a38, 0\n" \
"v_accvgpr_write_b32 a39, 0\n" \
"v_accvgpr_write_b32 a40, 0\n" \
"v_accvgpr_write_b32 a41, 0\n" \
"v_accvgpr_write_b32 a42, 0\n" \
"v_accvgpr_write_b32 a43, 0\n" \
"v_accvgpr_write_b32 a44, 0\n" \
"v_accvgpr_write_b32 a45, 0\n" \
"v_accvgpr_write_b32 a46, 0\n" \
"v_accvgpr_write_b32 a47, 0\n" \
"v_accvgpr_write_b32 a48, 0\n" \
"v_accvgpr_write_b32 a49, 0\n" \
"v_accvgpr_write_b32 a50, 0\n" \
"v_accvgpr_write_b32 a51, 0\n" \
"v_accvgpr_write_b32 a52, 0\n" \
"v_accvgpr_write_b32 a53, 0\n" \
"v_accvgpr_write_b32 a54, 0\n" \
"v_accvgpr_write_b32 a55, 0\n" \
"v_accvgpr_write_b32 a56, 0\n" \
"v_accvgpr_write_b32 a57, 0\n" \
"v_accvgpr_write_b32 a58, 0\n" \
"v_accvgpr_write_b32 a59, 0\n" \
"v_accvgpr_write_b32 a60, 0\n" \
"v_accvgpr_write_b32 a61, 0\n" \
"v_accvgpr_write_b32 a62, 0\n" \
"v_accvgpr_write_b32 a63, 0\n" \
"v_accvgpr_write_b32 a64, 0\n" \
"v_accvgpr_write_b32 a65, 0\n" \
"v_accvgpr_write_b32 a66, 0\n" \
"v_accvgpr_write_b32 a67, 0\n" \
"v_accvgpr_write_b32 a68, 0\n" \
"v_accvgpr_write_b32 a69, 0\n" \
"v_accvgpr_write_b32 a70, 0\n" \
"v_accvgpr_write_b32 a71, 0\n" \
"v_accvgpr_write_b32 a72, 0\n" \
"v_accvgpr_write_b32 a73, 0\n" \
"v_accvgpr_write_b32 a74, 0\n" \
"v_accvgpr_write_b32 a75, 0\n" \
"v_accvgpr_write_b32 a76, 0\n" \
"v_accvgpr_write_b32 a77, 0\n" \
"v_accvgpr_write_b32 a78, 0\n" \
"v_accvgpr_write_b32 a79, 0\n" \
"v_accvgpr_write_b32 a80, 0\n" \
"v_accvgpr_write_b32 a81, 0\n" \
"v_accvgpr_write_b32 a82, 0\n" \
"v_accvgpr_write_b32 a83, 0\n" \
"v_accvgpr_write_b32 a84, 0\n" \
"v_accvgpr_write_b32 a85, 0\n" \
"v_accvgpr_write_b32 a86, 0\n" \
"v_accvgpr_write_b32 a87, 0\n" \
"v_accvgpr_write_b32 a88, 0\n" \
"v_accvgpr_write_b32 a89, 0\n" \
"v_accvgpr_write_b32 a90, 0\n" \
"v_accvgpr_write_b32 a91, 0\n" \
"v_accvgpr_write_b32 a92, 0\n" \
"v_accvgpr_write_b32 a93, 0\n" \
"v_accvgpr_write_b32 a94, 0\n" \
"v_accvgpr_write_b32 a95, 0\n" \
"v_accvgpr_write_b32 a96, 0\n" \
"v_accvgpr_write_b32 a97, 0\n" \
"v_accvgpr_write_b32 a98, 0\n" \
"v_accvgpr_write_b32 a99, 0\n" \
"v_accvgpr_write_b32 a100, 0\n" \
"v_accvgpr_write_b32 a101, 0\n" \
"v_accvgpr_write_b32 a102, 0\n" \
"v_accvgpr_write_b32 a103, 0\n" \
"v_accvgpr_write_b32 a104, 0\n" \
"v_accvgpr_write_b32 a105, 0\n" \
"v_accvgpr_write_b32 a106, 0\n" \
"v_accvgpr_write_b32 a107, 0\n" \
"v_accvgpr_write_b32 a108, 0\n" \
"v_accvgpr_write_b32 a109, 0\n" \
"v_accvgpr_write_b32 a110, 0\n" \
"v_accvgpr_write_b32 a111, 0\n" \
"v_accvgpr_write_b32 a112, 0\n" \
"v_accvgpr_write_b32 a113, 0\n" \
"v_accvgpr_write_b32 a114, 0\n" \
"v_accvgpr_write_b32 a115, 0\n" \
"v_accvgpr_write_b32 a116, 0\n" \
"v_accvgpr_write_b32 a117, 0\n" \
"v_accvgpr_write_b32 a118, 0\n" \
"v_accvgpr_write_b32 a119, 0\n" \
"v_accvgpr_write_b32 a120, 0\n" \
"v_accvgpr_write_b32 a121, 0\n" \
"v_accvgpr_write_b32 a122, 0\n" \
"v_accvgpr_write_b32 a123, 0\n" \
"v_accvgpr_write_b32 a124, 0\n" \
"v_accvgpr_write_b32 a125, 0\n" \
"v_accvgpr_write_b32 a126, 0\n" \
"v_accvgpr_write_b32 a127, 0\n" \
"v_accvgpr_write_b32 a128, 0\n" \
"v_accvgpr_write_b32 a129, 0\n" \
"v_accvgpr_write_b32 a130, 0\n" \
"v_accvgpr_write_b32 a131, 0\n" \
"v_accvgpr_write_b32 a132, 0\n" \
"v_accvgpr_write_b32 a133, 0\n" \
"v_accvgpr_write_b32 a134, 0\n" \
"v_accvgpr_write_b32 a135, 0\n" \
"v_accvgpr_write_b32 a136, 0\n" \
"v_accvgpr_write_b32 a137, 0\n" \
"v_accvgpr_write_b32 a138, 0\n" \
"v_accvgpr_write_b32 a139, 0\n" \
"v_accvgpr_write_b32 a140, 0\n" \
"v_accvgpr_write_b32 a141, 0\n" \
"v_accvgpr_write_b32 a142, 0\n" \
"v_accvgpr_write_b32 a143, 0\n" \
"v_accvgpr_write_b32 a144, 0\n" \
"v_accvgpr_write_b32 a145, 0\n" \
"v_accvgpr_write_b32 a146, 0\n" \
"v_accvgpr_write_b32 a147, 0\n" \
"v_accvgpr_write_b32 a148, 0\n" \
"v_accvgpr_write_b32 a149, 0\n" \
"v_accvgpr_write_b32 a150, 0\n" \
"v_accvgpr_write_b32 a151, 0\n" \
"v_accvgpr_write_b32 a152, 0\n" \
"v_accvgpr_write_b32 a153, 0\n" \
"v_accvgpr_write_b32 a154, 0\n" \
"v_accvgpr_write_b32 a155, 0\n" \
"v_accvgpr_write_b32 a156, 0\n" \
"v_accvgpr_write_b32 a157, 0\n" \
"v_accvgpr_write_b32 a158, 0\n" \
"v_accvgpr_write_b32 a159, 0\n" \
"v_accvgpr_write_b32 a160, 0\n" \
"v_accvgpr_write_b32 a161, 0\n" \
"v_accvgpr_write_b32 a162, 0\n" \
"v_accvgpr_write_b32 a163, 0\n" \
"v_accvgpr_write_b32 a164, 0\n" \
"v_accvgpr_write_b32 a165, 0\n" \
"v_accvgpr_write_b32 a166, 0\n" \
"v_accvgpr_write_b32 a167, 0\n" \
"v_accvgpr_write_b32 a168, 0\n" \
"v_accvgpr_write_b32 a169, 0\n" \
"v_accvgpr_write_b32 a170, 0\n" \
"v_accvgpr_write_b32 a171, 0\n" \
"v_accvgpr_write_b32 a172, 0\n" \
"v_accvgpr_write_b32 a173, 0\n" \
"v_accvgpr_write_b32 a174, 0\n" \
"v_accvgpr_write_b32 a175, 0\n" \
"v_accvgpr_write_b32 a176, 0\n" \
"v_accvgpr_write_b32 a177, 0\n" \
"v_accvgpr_write_b32 a178, 0\n" \
"v_accvgpr_write_b32 a179, 0\n" \
"v_accvgpr_write_b32 a180, 0\n" \
"v_accvgpr_write_b32 a181, 0\n" \
"v_accvgpr_write_b32 a182, 0\n" \
"v_accvgpr_write_b32 a183, 0\n" \
"v_accvgpr_write_b32 a184, 0\n" \
"v_accvgpr_write_b32 a185, 0\n" \
"v_accvgpr_write_b32 a186, 0\n" \
"v_accvgpr_write_b32 a187, 0\n" \
"v_accvgpr_write_b32 a188, 0\n" \
"v_accvgpr_write_b32 a189, 0\n" \
"v_accvgpr_write_b32 a190, 0\n" \
"v_accvgpr_write_b32 a191, 0\n" \
"v_accvgpr_write_b32 a192, 0\n" \
"v_accvgpr_write_b32 a193, 0\n" \
"v_accvgpr_write_b32 a194, 0\n" \
"v_accvgpr_write_b32 a195, 0\n" \
"v_accvgpr_write_b32 a196, 0\n" \
"v_accvgpr_write_b32 a197, 0\n" \
"v_accvgpr_write_b32 a198, 0\n" \
"v_accvgpr_write_b32 a199, 0\n" \
"v_accvgpr_write_b32 a200, 0\n" \
"v_accvgpr_write_b32 a201, 0\n" \
"v_accvgpr_write_b32 a202, 0\n" \
"v_accvgpr_write_b32 a203, 0\n" \
"v_accvgpr_write_b32 a204, 0\n" \
"v_accvgpr_write_b32 a205, 0\n" \
"v_accvgpr_write_b32 a206, 0\n" \
"v_accvgpr_write_b32 a207, 0\n" \
"v_accvgpr_write_b32 a208, 0\n" \
"v_accvgpr_write_b32 a209, 0\n" \
"v_accvgpr_write_b32 a210, 0\n" \
"v_accvgpr_write_b32 a211, 0\n" \
"v_accvgpr_write_b32 a212, 0\n" \
"v_accvgpr_write_b32 a213, 0\n" \
"v_accvgpr_write_b32 a214, 0\n" \
"v_accvgpr_write_b32 a215, 0\n" \
"v_accvgpr_write_b32 a216, 0\n" \
"v_accvgpr_write_b32 a217, 0\n" \
"v_accvgpr_write_b32 a218, 0\n" \
"v_accvgpr_write_b32 a219, 0\n" \
"v_accvgpr_write_b32 a220, 0\n" \
"v_accvgpr_write_b32 a221, 0\n" \
"v_accvgpr_write_b32 a222, 0\n" \
"v_accvgpr_write_b32 a223, 0\n" \
"v_accvgpr_write_b32 a224, 0\n" \
"v_accvgpr_write_b32 a225, 0\n" \
"v_accvgpr_write_b32 a226, 0\n" \
"v_accvgpr_write_b32 a227, 0\n" \
"v_accvgpr_write_b32 a228, 0\n" \
"v_accvgpr_write_b32 a229, 0\n" \
"v_accvgpr_write_b32 a230, 0\n" \
"v_accvgpr_write_b32 a231, 0\n" \
"v_accvgpr_write_b32 a232, 0\n" \
"v_accvgpr_write_b32 a233, 0\n" \
"v_accvgpr_write_b32 a234, 0\n" \
"v_accvgpr_write_b32 a235, 0\n" \
"v_accvgpr_write_b32 a236, 0\n" \
"v_accvgpr_write_b32 a237, 0\n" \
"v_accvgpr_write_b32 a238, 0\n" \
"v_accvgpr_write_b32 a239, 0\n" \
"v_accvgpr_write_b32 a240, 0\n" \
"v_accvgpr_write_b32 a241, 0\n" \
"v_accvgpr_write_b32 a242, 0\n" \
"v_accvgpr_write_b32 a243, 0\n" \
"v_accvgpr_write_b32 a244, 0\n" \
"v_accvgpr_write_b32 a245, 0\n" \
"v_accvgpr_write_b32 a246, 0\n" \
"v_accvgpr_write_b32 a247, 0\n" \
"v_accvgpr_write_b32 a248, 0\n" \
"v_accvgpr_write_b32 a249, 0\n" \
"v_accvgpr_write_b32 a250, 0\n" \
"v_accvgpr_write_b32 a251, 0\n" \
"v_accvgpr_write_b32 a252, 0\n" \
"v_accvgpr_write_b32 a253, 0\n" \
"v_accvgpr_write_b32 a254, 0\n" \
"v_accvgpr_write_b32 a255, 0\n" \
"s_waitcnt vmcnt(24)\n" \
"s_barrier\n" \
"ds_read_b64_tr_b16 v[32:33], v137 offset:0\n" \
"ds_read_b64_tr_b16 v[34:35], v137 offset:8192\n" \
"ds_read_b128 v[0:3], v136 offset:0\n" \
"ds_read_b128 v[4:7], v136 offset:1024\n" \
"ds_read_b128 v[8:11], v136 offset:2048\n" \
"ds_read_b128 v[12:15], v136 offset:3072\n" \
"ds_read_b128 v[16:19], v136 offset:4096\n" \
"ds_read_b128 v[20:23], v136 offset:5120\n" \
"ds_read_b128 v[24:27], v136 offset:6144\n" \
"ds_read_b128 v[28:31], v136 offset:7168\n" \
"ds_read_b64_tr_b16 v[36:37], v138 offset:0\n" \
"ds_read_b64_tr_b16 v[38:39], v138 offset:8192\n" \
"ds_read_b64_tr_b16 v[40:41], v139 offset:0\n" \
"ds_read_b64_tr_b16 v[42:43], v139 offset:8192\n" \
"ds_read_b64_tr_b16 v[44:45], v140 offset:0\n" \
"ds_read_b64_tr_b16 v[46:47], v140 offset:8192\n" \
"ds_read_b64_tr_b16 v[48:49], v137 offset:1024\n" \
"ds_read_b64_tr_b16 v[50:51], v137 offset:9216\n" \
"ds_read_b64_tr_b16 v[52:53], v138 offset:1024\n" \
"ds_read_b64_tr_b16 v[54:55], v138 offset:9216\n" \
"ds_read_b64_tr_b16 v[56:57], v139 offset:1024\n" \
"ds_read_b64_tr_b16 v[58:59], v139 offset:9216\n" \
"ds_read_b64_tr_b16 v[60:61], v140 offset:1024\n" \
"ds_read_b64_tr_b16 v[62:63], v140 offset:9216\n" \
"s_cmp_eq_u32 s55, 0\n" \
"s_cbranch_scc1 LG88_tail_%=\n" \
"LG88_loop_%=:\n" \
"s_waitcnt vmcnt(16) lgkmcnt(0)\n" \
"s_barrier\n" \
"v_mfma_f32_16x16x32_bf16 a[0:3], v[32:35], v[0:3], a[0:3]\n" \
"ds_read_b64_tr_b16 v[96:97], v137 offset:16384\n" \
"s_add_u32 m0, s56, 0\n" \
"v_mfma_f32_16x16x32_bf16 a[4:7], v[32:35], v[4:7], a[4:7]\n" \
"buffer_load_dwordx4 v128, s[40:43], s52 offen offset:0 sc1 lds\n" \
"v_mfma_f32_16x16x32_bf16 a[8:11], v[32:35], v[8:11], a[8:11]\n" \
"ds_read_b64_tr_b16 v[98:99], v137 offset:24576\n" \
"v_mfma_f32_16x16x32_bf16 a[12:15], v[32:35], v[12:15], a[12:15]\n" \
"v_mfma_f32_16x16x32_bf16 a[16:19], v[32:35], v[16:19], a[16:19]\n" \
"ds_read_b128 v[64:67], v136 offset:16384\n" \
"v_mfma_f32_16x16x32_bf16 a[20:23], v[32:35], v[20:23], a[20:23]\n" \
"v_mfma_f32_16x16x32_bf16 a[24:27], v[32:35], v[24:27], a[24:27]\n" \
"ds_read_b128 v[68:71], v136 offset:17408\n" \
"v_mfma_f32_16x16x32_bf16 a[28:31], v[32:35], v[28:31], a[28:31]\n" \
"v_mfma_f32_16x16x32_bf16 a[32:35], v[36:39], v[0:3], a[32:35]\n" \
"ds_read_b128 v[72:75], v136 offset:18432\n" \
"v_mfma_f32_16x16x32_bf16 a[36:39], v[36:39], v[4:7], a[36:39]\n" \
"buffer_load_dwordx4 v129, s[40:43], s52 offen offset:1024 sc1 lds\n" \
"v_mfma_f32_16x16x32_bf16 a[40:43], v[36:39], v[8:11], a[40:43]\n" \
"ds_read_b128 v[76:79], v136 offset:19456\n" \
"v_mfma_f32_16x16x32_bf16 a[44:47], v[36:39], v[12:15], a[44:47]\n" \
"v_mfma_f32_16x16x32_bf16 a[48:51], v[36:39], v[16:19], a[48:51]\n" \
"ds_read_b128 v[80:83], v136 offset:20480\n" \
"v_mfma_f32_16x16x32_bf16 a[52:55], v[36:39], v[20:23], a[52:55]\n" \
"v_mfma_f32_16x16x32_bf16 a[56:59], v[36:39], v[24:27], a[56:59]\n" \
"ds_read_b128 v[84:87], v136 offset:21504\n" \
"v_mfma_f32_16x16x32_bf16 a[60:63], v[36:39], v[28:31], a[60:63]\n" \
"v_mfma_f32_16x16x32_bf16 a[64:67], v[40:43], v[0:3], a[64:67]\n" \
"ds_read_b128 v[88:91], v136 offset:22528\n" \
"v_mfma_f32_16x16x32_bf16 a[68:71], v[40:43], v[4:7], a[68:71]\n" \
"buffer_load_dwordx4 v130, s[40:43], s52 offen offset:2048 sc1 lds\n" \
"v_mfma_f32_16x16x32_bf16 a[72:75], v[40:43], v[8:11], a[72:75]\n" \
"ds_read_b128 v[92:95], v136 offset:23552\n" \
"v_mfma_f32_16x16x32_bf16 a[76:79], v[40:43], v[12:15], a[76:79]\n" \
"v_mfma_f32_16x16x32_bf16 a[80:83], v[40:43], v[16:19], a[80:83]\n" \
"ds_read_b64_tr_b16 v[100:101], v138 offset:16384\n" \
"v_mfma_f32_16x16x32_bf16 a[84:87], v[40:43], v[20:23], a[84:87]\n" \
"v_mfma_f32_16x16x32_bf16 a[88:91], v[40:43], v[24:27], a[88:91]\n" \
"ds_read_b64_tr_b16 v[102:103], v138 offset:24576\n" \
"v_mfma_f32_16x16x32_bf16 a[92:95], v[40:43], v[28:31], a[92:95]\n" \
"v_mfma_f32_16x16x32_bf16 a[96:99], v[44:47], v[0:3], a[96:99]\n" \
"ds_read_b64_tr_b16 v[104:105], v139 offset:16384\n" \
"v_mfma_f32_16x16x32_bf16 a[100:103], v[44:47], v[4:7], a[100:103]\n" \
"buffer_load_dwordx4 v131, s[40:43], s52 offen offset:3072 sc1 lds\n" \
"v_mfma_f32_16x16x32_bf16 a[104:107], v[44:47], v[8:11], a[104:107]\n" \
"ds_read_b64_tr_b16 v[106:107], v139 offset:24576\n" \
"v_mfma_f32_16x16x32_bf16 a[108:111], v[44:47], v[12:15], a[108:111]\n" \
"v_mfma_f32_16x16x32_bf16 a[112:115], v[44:47], v[16:19], a[112:115]\n" \
"ds_read_b64_tr_b16 v[108:109], v140 offset:16384\n" \
"v_mfma_f32_16x16x32_bf16 a[116:119], v[44:47], v[20:23], a[116:119]\n" \
"v_mfma_f32_16x16x32_bf16 a[120:123], v[44:47], v[24:27], a[120:123]\n" \
"ds_read_b64_tr_b16 v[110:111], v140 offset:24576\n" \
"v_mfma_f32_16x16x32_bf16 a[124:127], v[44:47], v[28:31], a[124:127]\n" \
"v_mfma_f32_16x16x32_bf16 a[128:131], v[48:51], v[0:3], a[128:131]\n" \
"ds_read_b64_tr_b16 v[112:113], v137 offset:17408\n" \
"s_add_u32 m0, s57, 0\n" \
"v_mfma_f32_16x16x32_bf16 a[132:135], v[48:51], v[4:7], a[132:135]\n" \
"buffer_load_dwordx4 v132, s[44:47], s53 offen offset:0 lds\n" \
"v_mfma_f32_16x16x32_bf16 a[136:139], v[48:51], v[8:11], a[136:139]\n" \
"ds_read_b64_tr_b16 v[114:115], v137 offset:25600\n" \
"v_mfma_f32_16x16x32_bf16 a[140:143], v[48:51], v[12:15], a[140:143]\n" \
"v_mfma_f32_16x16x32_bf16 a[144:147], v[48:51], v[16:19], a[144:147]\n" \
"ds_read_b64_tr_b16 v[116:117], v138 offset:17408\n" \
"v_mfma_f32_16x16x32_bf16 a[148:151], v[48:51], v[20:23], a[148:151]\n" \
"v_mfma_f32_16x16x32_bf16 a[152:155], v[48:51], v[24:27], a[152:155]\n" \
"ds_read_b64_tr_b16 v[118:119], v138 offset:25600\n" \
"v_mfma_f32_16x16x32_bf16 a[156:159], v[48:51], v[28:31], a[156:159]\n" \
"v_mfma_f32_16x16x32_bf16 a[160:163], v[52:55], v[0:3], a[160:163]\n" \
"ds_read_b64_tr_b16 v[120:121], v139 offset:17408\n" \
"v_mfma_f32_16x16x32_bf16 a[164:167], v[52:55], v[4:7], a[164:167]\n" \
"buffer_load_dwordx4 v133, s[48:51], s53 offen offset:1024 lds\n" \
"v_mfma_f32_16x16x32_bf16 a[168:171], v[52:55], v[8:11], a[168:171]\n" \
"ds_read_b64_tr_b16 v[122:123], v139 offset:25600\n" \
"v_mfma_f32_16x16x32_bf16 a[172:175], v[52:55], v[12:15], a[172:175]\n" \
"v_mfma_f32_16x16x32_bf16 a[176:179], v[52:55], v[16:19], a[176:179]\n" \
"ds_read_b64_tr_b16 v[124:125], v140 offset:17408\n" \
"v_mfma_f32_16x16x32_bf16 a[180:183], v[52:55], v[20:23], a[180:183]\n" \
"v_mfma_f32_16x16x32_bf16 a[184:187], v[52:55], v[24:27], a[184:187]\n" \
"ds_read_b64_tr_b16 v[126:127], v140 offset:25600\n" \
"v_mfma_f32_16x16x32_bf16 a[188:191], v[52:55], v[28:31], a[188:191]\n" \
"v_mfma_f32_16x16x32_bf16 a[192:195], v[56:59], v[0:3], a[192:195]\n" \
"v_mfma_f32_16x16x32_bf16 a[196:199], v[56:59], v[4:7], a[196:199]\n" \
"buffer_load_dwordx4 v134, s[44:47], s53 offen offset:2048 lds\n" \
"v_mfma_f32_16x16x32_bf16 a[200:203], v[56:59], v[8:11], a[200:203]\n" \
"v_mfma_f32_16x16x32_bf16 a[204:207], v[56:59], v[12:15], a[204:207]\n" \
"v_mfma_f32_16x16x32_bf16 a[208:211], v[56:59], v[16:19], a[208:211]\n" \
"v_mfma_f32_16x16x32_bf16 a[212:215], v[56:59], v[20:23], a[212:215]\n" \
"v_mfma_f32_16x16x32_bf16 a[216:219], v[56:59], v[24:27], a[216:219]\n" \
"v_mfma_f32_16x16x32_bf16 a[220:223], v[56:59], v[28:31], a[220:223]\n" \
"v_mfma_f32_16x16x32_bf16 a[224:227], v[60:63], v[0:3], a[224:227]\n" \
"v_mfma_f32_16x16x32_bf16 a[228:231], v[60:63], v[4:7], a[228:231]\n" \
"buffer_load_dwordx4 v135, s[48:51], s53 offen offset:3072 lds\n" \
"v_mfma_f32_16x16x32_bf16 a[232:235], v[60:63], v[8:11], a[232:235]\n" \
"v_mfma_f32_16x16x32_bf16 a[236:239], v[60:63], v[12:15], a[236:239]\n" \
"v_mfma_f32_16x16x32_bf16 a[240:243], v[60:63], v[16:19], a[240:243]\n" \
"v_mfma_f32_16x16x32_bf16 a[244:247], v[60:63], v[20:23], a[244:247]\n" \
"v_mfma_f32_16x16x32_bf16 a[248:251], v[60:63], v[24:27], a[248:251]\n" \
"v_mfma_f32_16x16x32_bf16 a[252:255], v[60:63], v[28:31], a[252:255]\n" \
"s_add_u32 s52, s52, 1024\n" \
"s_add_u32 s53, s53, s54\n" \
"s_waitcnt vmcnt(16) lgkmcnt(0)\n" \
"s_barrier\n" \
"v_mfma_f32_16x16x32_bf16 a[0:3], v[96:99], v[64:67], a[0:3]\n" \
"ds_read_b64_tr_b16 v[32:33], v137 offset:32768\n" \
"s_add_u32 m0, s56, 16384\n" \
"v_mfma_f32_16x16x32_bf16 a[4:7], v[96:99], v[68:71], a[4:7]\n" \
"buffer_load_dwordx4 v128, s[40:43], s52 offen offset:0 sc1 lds\n" \
"v_mfma_f32_16x16x32_bf16 a[8:11], v[96:99], v[72:75], a[8:11]\n" \
"ds_read_b64_tr_b16 v[34:35], v137 offset:40960\n" \
"v_mfma_f32_16x16x32_bf16 a[12:15], v[96:99], v[76:79], a[12:15]\n" \
"v_mfma_f32_16x16x32_bf16 a[16:19], v[96:99], v[80:83], a[16:19]\n" \
"ds_read_b128 v[0:3], v136 offset:32768\n" \
"v_mfma_f32_16x16x32_bf16 a[20:23], v[96:99], v[84:87], a[20:23]\n" \
"v_mfma_f32_16x16x32_bf16 a[24:27], v[96:99], v[88:91], a[24:27]\n" \
"ds_read_b128 v[4:7], v136 offset:33792\n" \
"v_mfma_f32_16x16x32_bf16 a[28:31], v[96:99], v[92:95], a[28:31]\n" \
"v_mfma_f32_16x16x32_bf16 a[32:35], v[100:103], v[64:67], a[32:35]\n" \
"ds_read_b128 v[8:11], v136 offset:34816\n" \
"v_mfma_f32_16x16x32_bf16 a[36:39], v[100:103], v[68:71], a[36:39]\n" \
"buffer_load_dwordx4 v129, s[40:43], s52 offen offset:1024 sc1 lds\n" \
"v_mfma_f32_16x16x32_bf16 a[40:43], v[100:103], v[72:75], a[40:43]\n" \
"ds_read_b128 v[12:15], v136 offset:35840\n" \
"v_mfma_f32_16x16x32_bf16 a[44:47], v[100:103], v[76:79], a[44:47]\n" \
"v_mfma_f32_16x16x32_bf16 a[48:51], v[100:103], v[80:83], a[48:51]\n" \
"ds_read_b128 v[16:19], v136 offset:36864\n" \
"v_mfma_f32_16x16x32_bf16 a[52:55], v[100:103], v[84:87], a[52:55]\n" \
"v_mfma_f32_16x16x32_bf16 a[56:59], v[100:103], v[88:91], a[56:59]\n" \
"ds_read_b128 v[20:23], v136 offset:37888\n" \
"v_mfma_f32_16x16x32_bf16 a[60:63], v[100:103], v[92:95], a[60:63]\n" \
"v_mfma_f32_16x16x32_bf16 a[64:67], v[104:107], v[64:67], a[64:67]\n" \
"ds_read_b128 v[24:27], v136 offset:38912\n" \
"v_mfma_f32_16x16x32_bf16 a[68:71], v[104:107], v[68:71], a[68:71]\n" \
"buffer_load_dwordx4 v130, s[40:43], s52 offen offset:2048 sc1 lds\n" \
"v_mfma_f32_16x16x32_bf16 a[72:75], v[104:107], v[72:75], a[72:75]\n" \
"ds_read_b128 v[28:31], v136 offset:39936\n" \
"v_mfma_f32_16x16x32_bf16 a[76:79], v[104:107], v[76:79], a[76:79]\n" \
"v_mfma_f32_16x16x32_bf16 a[80:83], v[104:107], v[80:83], a[80:83]\n" \
"ds_read_b64_tr_b16 v[36:37], v138 offset:32768\n" \
"v_mfma_f32_16x16x32_bf16 a[84:87], v[104:107], v[84:87], a[84:87]\n" \
"v_mfma_f32_16x16x32_bf16 a[88:91], v[104:107], v[88:91], a[88:91]\n" \
"ds_read_b64_tr_b16 v[38:39], v138 offset:40960\n" \
"v_mfma_f32_16x16x32_bf16 a[92:95], v[104:107], v[92:95], a[92:95]\n" \
"v_mfma_f32_16x16x32_bf16 a[96:99], v[108:111], v[64:67], a[96:99]\n" \
"ds_read_b64_tr_b16 v[40:41], v139 offset:32768\n" \
"v_mfma_f32_16x16x32_bf16 a[100:103], v[108:111], v[68:71], a[100:103]\n" \
"buffer_load_dwordx4 v131, s[40:43], s52 offen offset:3072 sc1 lds\n" \
"v_mfma_f32_16x16x32_bf16 a[104:107], v[108:111], v[72:75], a[104:107]\n" \
"ds_read_b64_tr_b16 v[42:43], v139 offset:40960\n" \
"v_mfma_f32_16x16x32_bf16 a[108:111], v[108:111], v[76:79], a[108:111]\n" \
"v_mfma_f32_16x16x32_bf16 a[112:115], v[108:111], v[80:83], a[112:115]\n" \
"ds_read_b64_tr_b16 v[44:45], v140 offset:32768\n" \
"v_mfma_f32_16x16x32_bf16 a[116:119], v[108:111], v[84:87], a[116:119]\n" \
"v_mfma_f32_16x16x32_bf16 a[120:123], v[108:111], v[88:91], a[120:123]\n" \
"ds_read_b64_tr_b16 v[46:47], v140 offset:40960\n" \
"v_mfma_f32_16x16x32_bf16 a[124:127], v[108:111], v[92:95], a[124:127]\n" \
"v_mfma_f32_16x16x32_bf16 a[128:131], v[112:115], v[64:67], a[128:131]\n" \
"ds_read_b64_tr_b16 v[48:49], v137 offset:33792\n" \
"s_add_u32 m0, s57, 16384\n" \
"v_mfma_f32_16x16x32_bf16 a[132:135], v[112:115], v[68:71], a[132:135]\n" \
"buffer_load_dwordx4 v132, s[44:47], s53 offen offset:0 lds\n" \
"v_mfma_f32_16x16x32_bf16 a[136:139], v[112:115], v[72:75], a[136:139]\n" \
"ds_read_b64_tr_b16 v[50:51], v137 offset:41984\n" \
"v_mfma_f32_16x16x32_bf16 a[140:143], v[112:115], v[76:79], a[140:143]\n" \
"v_mfma_f32_16x16x32_bf16 a[144:147], v[112:115], v[80:83], a[144:147]\n" \
"ds_read_b64_tr_b16 v[52:53], v138 offset:33792\n" \
"v_mfma_f32_16x16x32_bf16 a[148:151], v[112:115], v[84:87], a[148:151]\n" \
"v_mfma_f32_16x16x32_bf16 a[152:155], v[112:115], v[88:91], a[152:155]\n" \
"ds_read_b64_tr_b16 v[54:55], v138 offset:41984\n" \
"v_mfma_f32_16x16x32_bf16 a[156:159], v[112:115], v[92:95], a[156:159]\n" \
"v_mfma_f32_16x16x32_bf16 a[160:163], v[116:119], v[64:67], a[160:163]\n" \
"ds_read_b64_tr_b16 v[56:57], v139 offset:33792\n" \
"v_mfma_f32_16x16x32_bf16 a[164:167], v[116:119], v[68:71], a[164:167]\n" \
"buffer_load_dwordx4 v133, s[48:51], s53 offen offset:1024 lds\n" \
"v_mfma_f32_16x16x32_bf16 a[168:171], v[116:119], v[72:75], a[168:171]\n" \
"ds_read_b64_tr_b16 v[58:59], v139 offset:41984\n" \
"v_mfma_f32_16x16x32_bf16 a[172:175], v[116:119], v[76:79], a[172:175]\n" \
"v_mfma_f32_16x16x32_bf16 a[176:179], v[116:119], v[80:83], a[176:179]\n" \
"ds_read_b64_tr_b16 v[60:61], v140 offset:33792\n" \
"v_mfma_f32_16x16x32_bf16 a[180:183], v[116:119], v[84:87], a[180:183]\n" \
"v_mfma_f32_16x16x32_bf16 a[184:187], v[116:119], v[88:91], a[184:187]\n" \
"ds_read_b64_tr_b16 v[62:63], v140 offset:41984\n" \
"v_mfma_f32_16x16x32_bf16 a[188:191], v[116:119], v[92:95], a[188:191]\n" \
"v_mfma_f32_16x16x32_bf16 a[192:195], v[120:123], v[64:67], a[192:195]\n" \
"v_mfma_f32_16x16x32_bf16 a[196:199], v[120:123], v[68:71], a[196:199]\n" \
"buffer_load_dwordx4 v134, s[44:47], s53 offen offset:2048 lds\n" \
"v_mfma_f32_16x16x32_bf16 a[200:203], v[120:123], v[72:75], a[200:203]\n" \
"v_mfma_f32_16x16x32_bf16 a[204:207], v[120:123], v[76:79], a[204:207]\n" \
"v_mfma_f32_16x16x32_bf16 a[208:211], v[120:123], v[80:83], a[208:211]\n" \
"v_mfma_f32_16x16x32_bf16 a[212:215], v[120:123], v[84:87], a[212:215]\n" \
"v_mfma_f32_16x16x32_bf16 a[216:219], v[120:123], v[88:91], a[216:219]\n" \
"v_mfma_f32_16x16x32_bf16 a[220:223], v[120:123], v[92:95], a[220:223]\n" \
"v_mfma_f32_16x16x32_bf16 a[224:227], v[124:127], v[64:67], a[224:227]\n" \
"v_mfma_f32_16x16x32_bf16 a[228:231], v[124:127], v[68:71], a[228:231]\n" \
"buffer_load_dwordx4 v135, s[48:51], s53 offen offset:3072 lds\n" \
"v_mfma_f32_16x16x32_bf16 a[232:235], v[124:127], v[72:75], a[232:235]\n" \
"v_mfma_f32_16x16x32_bf16 a[236:239], v[124:127], v[76:79], a[236:239]\n" \
"v_mfma_f32_16x16x32_bf16 a[240:243], v[124:127], v[80:83], a[240:243]\n" \
"v_mfma_f32_16x16x32_bf16 a[244:247], v[124:127], v[84:87], a[244:247]\n" \
"v_mfma_f32_16x16x32_bf16 a[248:251], v[124:127], v[88:91], a[248:251]\n" \
"v_mfma_f32_16x16x32_bf16 a[252:255], v[124:127], v[92:95], a[252:255]\n" \
"s_add_u32 s52, s52, 1024\n" \
"s_add_u32 s53, s53, s54\n" \
"s_waitcnt vmcnt(16) lgkmcnt(0)\n" \
"s_barrier\n" \
"v_mfma_f32_16x16x32_bf16 a[0:3], v[32:35], v[0:3], a[0:3]\n" \
"ds_read_b64_tr_b16 v[96:97], v137 offset:49152\n" \
"s_add_u32 m0, s56, 32768\n" \
"v_mfma_f32_16x16x32_bf16 a[4:7], v[32:35], v[4:7], a[4:7]\n" \
"buffer_load_dwordx4 v128, s[40:43], s52 offen offset:0 sc1 lds\n" \
"v_mfma_f32_16x16x32_bf16 a[8:11], v[32:35], v[8:11], a[8:11]\n" \
"ds_read_b64_tr_b16 v[98:99], v137 offset:57344\n" \
"v_mfma_f32_16x16x32_bf16 a[12:15], v[32:35], v[12:15], a[12:15]\n" \
"v_mfma_f32_16x16x32_bf16 a[16:19], v[32:35], v[16:19], a[16:19]\n" \
"ds_read_b128 v[64:67], v136 offset:49152\n" \
"v_mfma_f32_16x16x32_bf16 a[20:23], v[32:35], v[20:23], a[20:23]\n" \
"v_mfma_f32_16x16x32_bf16 a[24:27], v[32:35], v[24:27], a[24:27]\n" \
"ds_read_b128 v[68:71], v136 offset:50176\n" \
"v_mfma_f32_16x16x32_bf16 a[28:31], v[32:35], v[28:31], a[28:31]\n" \
"v_mfma_f32_16x16x32_bf16 a[32:35], v[36:39], v[0:3], a[32:35]\n" \
"ds_read_b128 v[72:75], v136 offset:51200\n" \
"v_mfma_f32_16x16x32_bf16 a[36:39], v[36:39], v[4:7], a[36:39]\n" \
"buffer_load_dwordx4 v129, s[40:43], s52 offen offset:1024 sc1 lds\n" \
"v_mfma_f32_16x16x32_bf16 a[40:43], v[36:39], v[8:11], a[40:43]\n" \
"ds_read_b128 v[76:79], v136 offset:52224\n" \
"v_mfma_f32_16x16x32_bf16 a[44:47], v[36:39], v[12:15], a[44:47]\n" \
"v_mfma_f32_16x16x32_bf16 a[48:51], v[36:39], v[16:19], a[48:51]\n" \
"ds_read_b128 v[80:83], v136 offset:53248\n" \
"v_mfma_f32_16x16x32_bf16 a[52:55], v[36:39], v[20:23], a[52:55]\n" \
"v_mfma_f32_16x16x32_bf16 a[56:59], v[36:39], v[24:27], a[56:59]\n" \
"ds_read_b128 v[84:87], v136 offset:54272\n" \
"v_mfma_f32_16x16x32_bf16 a[60:63], v[36:39], v[28:31], a[60:63]\n" \
"v_mfma_f32_16x16x32_bf16 a[64:67], v[40:43], v[0:3], a[64:67]\n" \
"ds_read_b128 v[88:91], v136 offset:55296\n" \
"v_mfma_f32_16x16x32_bf16 a[68:71], v[40:43], v[4:7], a[68:71]\n" \
"buffer_load_dwordx4 v130, s[40:43], s52 offen offset:2048 sc1 lds\n" \
"v_mfma_f32_16x16x32_bf16 a[72:75], v[40:43], v[8:11], a[72:75]\n" \
"ds_read_b128 v[92:95], v136 offset:56320\n" \
"v_mfma_f32_16x16x32_bf16 a[76:79], v[40:43], v[12:15], a[76:79]\n" \
"v_mfma_f32_16x16x32_bf16 a[80:83], v[40:43], v[16:19], a[80:83]\n" \
"ds_read_b64_tr_b16 v[100:101], v138 offset:49152\n" \
"v_mfma_f32_16x16x32_bf16 a[84:87], v[40:43], v[20:23], a[84:87]\n" \
"v_mfma_f32_16x16x32_bf16 a[88:91], v[40:43], v[24:27], a[88:91]\n" \
"ds_read_b64_tr_b16 v[102:103], v138 offset:57344\n" \
"v_mfma_f32_16x16x32_bf16 a[92:95], v[40:43], v[28:31], a[92:95]\n" \
"v_mfma_f32_16x16x32_bf16 a[96:99], v[44:47], v[0:3], a[96:99]\n" \
"ds_read_b64_tr_b16 v[104:105], v139 offset:49152\n" \
"v_mfma_f32_16x16x32_bf16 a[100:103], v[44:47], v[4:7], a[100:103]\n" \
"buffer_load_dwordx4 v131, s[40:43], s52 offen offset:3072 sc1 lds\n" \
"v_mfma_f32_16x16x32_bf16 a[104:107], v[44:47], v[8:11], a[104:107]\n" \
"ds_read_b64_tr_b16 v[106:107], v139 offset:57344\n" \
"v_mfma_f32_16x16x32_bf16 a[108:111], v[44:47], v[12:15], a[108:111]\n" \
"v_mfma_f32_16x16x32_bf16 a[112:115], v[44:47], v[16:19], a[112:115]\n" \
"ds_read_b64_tr_b16 v[108:109], v140 offset:49152\n" \
"v_mfma_f32_16x16x32_bf16 a[116:119], v[44:47], v[20:23], a[116:119]\n" \
"v_mfma_f32_16x16x32_bf16 a[120:123], v[44:47], v[24:27], a[120:123]\n" \
"ds_read_b64_tr_b16 v[110:111], v140 offset:57344\n" \
"v_mfma_f32_16x16x32_bf16 a[124:127], v[44:47], v[28:31], a[124:127]\n" \
"v_mfma_f32_16x16x32_bf16 a[128:131], v[48:51], v[0:3], a[128:131]\n" \
"ds_read_b64_tr_b16 v[112:113], v137 offset:50176\n" \
"s_add_u32 m0, s57, 32768\n" \
"v_mfma_f32_16x16x32_bf16 a[132:135], v[48:51], v[4:7], a[132:135]\n" \
"buffer_load_dwordx4 v132, s[44:47], s53 offen offset:0 lds\n" \
"v_mfma_f32_16x16x32_bf16 a[136:139], v[48:51], v[8:11], a[136:139]\n" \
"ds_read_b64_tr_b16 v[114:115], v137 offset:58368\n" \
"v_mfma_f32_16x16x32_bf16 a[140:143], v[48:51], v[12:15], a[140:143]\n" \
"v_mfma_f32_16x16x32_bf16 a[144:147], v[48:51], v[16:19], a[144:147]\n" \
"ds_read_b64_tr_b16 v[116:117], v138 offset:50176\n" \
"v_mfma_f32_16x16x32_bf16 a[148:151], v[48:51], v[20:23], a[148:151]\n" \
"v_mfma_f32_16x16x32_bf16 a[152:155], v[48:51], v[24:27], a[152:155]\n" \
"ds_read_b64_tr_b16 v[118:119], v138 offset:58368\n" \
"v_mfma_f32_16x16x32_bf16 a[156:159], v[48:51], v[28:31], a[156:159]\n" \
"v_mfma_f32_16x16x32_bf16 a[160:163], v[52:55], v[0:3], a[160:163]\n" \
"ds_read_b64_tr_b16 v[120:121], v139 offset:50176\n" \
"v_mfma_f32_16x16x32_bf16 a[164:167], v[52:55], v[4:7], a[164:167]\n" \
"buffer_load_dwordx4 v133, s[48:51], s53 offen offset:1024 lds\n" \
"v_mfma_f32_16x16x32_bf16 a[168:171], v[52:55], v[8:11], a[168:171]\n" \
"ds_read_b64_tr_b16 v[122:123], v139 offset:58368\n" \
"v_mfma_f32_16x16x32_bf16 a[172:175], v[52:55], v[12:15], a[172:175]\n" \
"v_mfma_f32_16x16x32_bf16 a[176:179], v[52:55], v[16:19], a[176:179]\n" \
"ds_read_b64_tr_b16 v[124:125], v140 offset:50176\n" \
"v_mfma_f32_16x16x32_bf16 a[180:183], v[52:55], v[20:23], a[180:183]\n" \
"v_mfma_f32_16x16x32_bf16 a[184:187], v[52:55], v[24:27], a[184:187]\n" \
"ds_read_b64_tr_b16 v[126:127], v140 offset:58368\n" \
"v_mfma_f32_16x16x32_bf16 a[188:191], v[52:55], v[28:31], a[188:191]\n" \
"v_mfma_f32_16x16x32_bf16 a[192:195], v[56:59], v[0:3], a[192:195]\n" \
"v_mfma_f32_16x16x32_bf16 a[196:199], v[56:59], v[4:7], a[196:199]\n" \
"buffer_load_dwordx4 v134, s[44:47], s53 offen offset:2048 lds\n" \
"v_mfma_f32_16x16x32_bf16 a[200:203], v[56:59], v[8:11], a[200:203]\n" \
"v_mfma_f32_16x16x32_bf16 a[204:207], v[56:59], v[12:15], a[204:207]\n" \
"v_mfma_f32_16x16x32_bf16 a[208:211], v[56:59], v[16:19], a[208:211]\n" \
"v_mfma_f32_16x16x32_bf16 a[212:215], v[56:59], v[20:23], a[212:215]\n" \
"v_mfma_f32_16x16x32_bf16 a[216:219], v[56:59], v[24:27], a[216:219]\n" \
"v_mfma_f32_16x16x32_bf16 a[220:223], v[56:59], v[28:31], a[220:223]\n" \
"v_mfma_f32_16x16x32_bf16 a[224:227], v[60:63], v[0:3], a[224:227]\n" \
"v_mfma_f32_16x16x32_bf16 a[228:231], v[60:63], v[4:7], a[228:231]\n" \
"buffer_load_dwordx4 v135, s[48:51], s53 offen offset:3072 lds\n" \
"v_mfma_f32_16x16x32_bf16 a[232:235], v[60:63], v[8:11], a[232:235]\n" \
"v_mfma_f32_16x16x32_bf16 a[236:239], v[60:63], v[12:15], a[236:239]\n" \
"v_mfma_f32_16x16x32_bf16 a[240:243], v[60:63], v[16:19], a[240:243]\n" \
"v_mfma_f32_16x16x32_bf16 a[244:247], v[60:63], v[20:23], a[244:247]\n" \
"v_mfma_f32_16x16x32_bf16 a[248:251], v[60:63], v[24:27], a[248:251]\n" \
"v_mfma_f32_16x16x32_bf16 a[252:255], v[60:63], v[28:31], a[252:255]\n" \
"s_add_u32 s52, s52, 1024\n" \
"s_add_u32 s53, s53, s54\n" \
"s_waitcnt vmcnt(16) lgkmcnt(0)\n" \
"s_barrier\n" \
"v_mfma_f32_16x16x32_bf16 a[0:3], v[96:99], v[64:67], a[0:3]\n" \
"ds_read_b64_tr_b16 v[32:33], v137 offset:0\n" \
"s_add_u32 m0, s56, 49152\n" \
"v_mfma_f32_16x16x32_bf16 a[4:7], v[96:99], v[68:71], a[4:7]\n" \
"buffer_load_dwordx4 v128, s[40:43], s52 offen offset:0 sc1 lds\n" \
"v_mfma_f32_16x16x32_bf16 a[8:11], v[96:99], v[72:75], a[8:11]\n" \
"ds_read_b64_tr_b16 v[34:35], v137 offset:8192\n" \
"v_mfma_f32_16x16x32_bf16 a[12:15], v[96:99], v[76:79], a[12:15]\n" \
"v_mfma_f32_16x16x32_bf16 a[16:19], v[96:99], v[80:83], a[16:19]\n" \
"ds_read_b128 v[0:3], v136 offset:0\n" \
"v_mfma_f32_16x16x32_bf16 a[20:23], v[96:99], v[84:87], a[20:23]\n" \
"v_mfma_f32_16x16x32_bf16 a[24:27], v[96:99], v[88:91], a[24:27]\n" \
"ds_read_b128 v[4:7], v136 offset:1024\n" \
"v_mfma_f32_16x16x32_bf16 a[28:31], v[96:99], v[92:95], a[28:31]\n" \
"v_mfma_f32_16x16x32_bf16 a[32:35], v[100:103], v[64:67], a[32:35]\n" \
"ds_read_b128 v[8:11], v136 offset:2048\n" \
"v_mfma_f32_16x16x32_bf16 a[36:39], v[100:103], v[68:71], a[36:39]\n" \
"buffer_load_dwordx4 v129, s[40:43], s52 offen offset:1024 sc1 lds\n" \
"v_mfma_f32_16x16x32_bf16 a[40:43], v[100:103], v[72:75], a[40:43]\n" \
"ds_read_b128 v[12:15], v136 offset:3072\n" \
"v_mfma_f32_16x16x32_bf16 a[44:47], v[100:103], v[76:79], a[44:47]\n" \
"v_mfma_f32_16x16x32_bf16 a[48:51], v[100:103], v[80:83], a[48:51]\n" \
"ds_read_b128 v[16:19], v136 offset:4096\n" \
"v_mfma_f32_16x16x32_bf16 a[52:55], v[100:103], v[84:87], a[52:55]\n" \
"v_mfma_f32_16x16x32_bf16 a[56:59], v[100:103], v[88:91], a[56:59]\n" \
"ds_read_b128 v[20:23], v136 offset:5120\n" \
"v_mfma_f32_16x16x32_bf16 a[60:63], v[100:103], v[92:95], a[60:63]\n" \
"v_mfma_f32_16x16x32_bf16 a[64:67], v[104:107], v[64:67], a[64:67]\n" \
"ds_read_b128 v[24:27], v136 offset:6144\n" \
"v_mfma_f32_16x16x32_bf16 a[68:71], v[104:107], v[68:71], a[68:71]\n" \
"buffer_load_dwordx4 v130, s[40:43], s52 offen offset:2048 sc1 lds\n" \
"v_mfma_f32_16x16x32_bf16 a[72:75], v[104:107], v[72:75], a[72:75]\n" \
"ds_read_b128 v[28:31], v136 offset:7168\n" \
"v_mfma_f32_16x16x32_bf16 a[76:79], v[104:107], v[76:79], a[76:79]\n" \
"v_mfma_f32_16x16x32_bf16 a[80:83], v[104:107], v[80:83], a[80:83]\n" \
"ds_read_b64_tr_b16 v[36:37], v138 offset:0\n" \
"v_mfma_f32_16x16x32_bf16 a[84:87], v[104:107], v[84:87], a[84:87]\n" \
"v_mfma_f32_16x16x32_bf16 a[88:91], v[104:107], v[88:91], a[88:91]\n" \
"ds_read_b64_tr_b16 v[38:39], v138 offset:8192\n" \
"v_mfma_f32_16x16x32_bf16 a[92:95], v[104:107], v[92:95], a[92:95]\n" \
"v_mfma_f32_16x16x32_bf16 a[96:99], v[108:111], v[64:67], a[96:99]\n" \
"ds_read_b64_tr_b16 v[40:41], v139 offset:0\n" \
"v_mfma_f32_16x16x32_bf16 a[100:103], v[108:111], v[68:71], a[100:103]\n" \
"buffer_load_dwordx4 v131, s[40:43], s52 offen offset:3072 sc1 lds\n" \
"v_mfma_f32_16x16x32_bf16 a[104:107], v[108:111], v[72:75], a[104:107]\n" \
"ds_read_b64_tr_b16 v[42:43], v139 offset:8192\n" \
"v_mfma_f32_16x16x32_bf16 a[108:111], v[108:111], v[76:79], a[108:111]\n" \
"v_mfma_f32_16x16x32_bf16 a[112:115], v[108:111], v[80:83], a[112:115]\n" \
"ds_read_b64_tr_b16 v[44:45], v140 offset:0\n" \
"v_mfma_f32_16x16x32_bf16 a[116:119], v[108:111], v[84:87], a[116:119]\n" \
"v_mfma_f32_16x16x32_bf16 a[120:123], v[108:111], v[88:91], a[120:123]\n" \
"ds_read_b64_tr_b16 v[46:47], v140 offset:8192\n" \
"v_mfma_f32_16x16x32_bf16 a[124:127], v[108:111], v[92:95], a[124:127]\n" \
"v_mfma_f32_16x16x32_bf16 a[128:131], v[112:115], v[64:67], a[128:131]\n" \
"ds_read_b64_tr_b16 v[48:49], v137 offset:1024\n" \
"s_add_u32 m0, s57, 49152\n" \
"v_mfma_f32_16x16x32_bf16 a[132:135], v[112:115], v[68:71], a[132:135]\n" \
"buffer_load_dwordx4 v132, s[44:47], s53 offen offset:0 lds\n" \
"v_mfma_f32_16x16x32_bf16 a[136:139], v[112:115], v[72:75], a[136:139]\n" \
"ds_read_b64_tr_b16 v[50:51], v137 offset:9216\n" \
"v_mfma_f32_16x16x32_bf16 a[140:143], v[112:115], v[76:79], a[140:143]\n" \
"v_mfma_f32_16x16x32_bf16 a[144:147], v[112:115], v[80:83], a[144:147]\n" \
"ds_read_b64_tr_b16 v[52:53], v138 offset:1024\n" \
"v_mfma_f32_16x16x32_bf16 a[148:151], v[112:115], v[84:87], a[148:151]\n" \
"v_mfma_f32_16x16x32_bf16 a[152:155], v[112:115], v[88:91], a[152:155]\n" \
"ds_read_b64_tr_b16 v[54:55], v138 offset:9216\n" \
"v_mfma_f32_16x16x32_bf16 a[156:159], v[112:115], v[92:95], a[156:159]\n" \
"v_mfma_f32_16x16x32_bf16 a[160:163], v[116:119], v[64:67], a[160:163]\n" \
"ds_read_b64_tr_b16 v[56:57], v139 offset:1024\n" \
"v_mfma_f32_16x16x32_bf16 a[164:167], v[116:119], v[68:71], a[164:167]\n" \
"buffer_load_dwordx4 v133, s[48:51], s53 offen offset:1024 lds\n" \
"v_mfma_f32_16x16x32_bf16 a[168:171], v[116:119], v[72:75], a[168:171]\n" \
"ds_read_b64_tr_b16 v[58:59], v139 offset:9216\n" \
"v_mfma_f32_16x16x32_bf16 a[172:175], v[116:119], v[76:79], a[172:175]\n" \
"v_mfma_f32_16x16x32_bf16 a[176:179], v[116:119], v[80:83], a[176:179]\n" \
"ds_read_b64_tr_b16 v[60:61], v140 offset:1024\n" \
"v_mfma_f32_16x16x32_bf16 a[180:183], v[116:119], v[84:87], a[180:183]\n" \
"v_mfma_f32_16x16x32_bf16 a[184:187], v[116:119], v[88:91], a[184:187]\n" \
"ds_read_b64_tr_b16 v[62:63], v140 offset:9216\n" \
"v_mfma_f32_16x16x32_bf16 a[188:191], v[116:119], v[92:95], a[188:191]\n" \
"v_mfma_f32_16x16x32_bf16 a[192:195], v[120:123], v[64:67], a[192:195]\n" \
"v_mfma_f32_16x16x32_bf16 a[196:199], v[120:123], v[68:71], a[196:199]\n" \
"buffer_load_dwordx4 v134, s[44:47], s53 offen offset:2048 lds\n" \
"v_mfma_f32_16x16x32_bf16 a[200:203], v[120:123], v[72:75], a[200:203]\n" \
"v_mfma_f32_16x16x32_bf16 a[204:207], v[120:123], v[76:79], a[204:207]\n" \
"v_mfma_f32_16x16x32_bf16 a[208:211], v[120:123], v[80:83], a[208:211]\n" \
"v_mfma_f32_16x16x32_bf16 a[212:215], v[120:123], v[84:87], a[212:215]\n" \
"v_mfma_f32_16x16x32_bf16 a[216:219], v[120:123], v[88:91], a[216:219]\n" \
"v_mfma_f32_16x16x32_bf16 a[220:223], v[120:123], v[92:95], a[220:223]\n" \
"v_mfma_f32_16x16x32_bf16 a[224:227], v[124:127], v[64:67], a[224:227]\n" \
"v_mfma_f32_16x16x32_bf16 a[228:231], v[124:127], v[68:71], a[228:231]\n" \
"buffer_load_dwordx4 v135, s[48:51], s53 offen offset:3072 lds\n" \
"v_mfma_f32_16x16x32_bf16 a[232:235], v[124:127], v[72:75], a[232:235]\n" \
"v_mfma_f32_16x16x32_bf16 a[236:239], v[124:127], v[76:79], a[236:239]\n" \
"v_mfma_f32_16x16x32_bf16 a[240:243], v[124:127], v[80:83], a[240:243]\n" \
"v_mfma_f32_16x16x32_bf16 a[244:247], v[124:127], v[84:87], a[244:247]\n" \
"v_mfma_f32_16x16x32_bf16 a[248:251], v[124:127], v[88:91], a[248:251]\n" \
"v_mfma_f32_16x16x32_bf16 a[252:255], v[124:127], v[92:95], a[252:255]\n" \
"s_add_u32 s52, s52, 1024\n" \
"s_add_u32 s53, s53, s54\n" \
"s_sub_u32 s55, s55, 1\n" \
"s_cmp_lg_u32 s55, 0\n" \
"s_cbranch_scc1 LG88_loop_%=\n" \
"LG88_tail_%=:\n" \
"s_waitcnt vmcnt(16) lgkmcnt(0)\n" \
"s_barrier\n" \
"v_mfma_f32_16x16x32_bf16 a[0:3], v[32:35], v[0:3], a[0:3]\n" \
"ds_read_b64_tr_b16 v[96:97], v137 offset:16384\n" \
"v_mfma_f32_16x16x32_bf16 a[4:7], v[32:35], v[4:7], a[4:7]\n" \
"v_mfma_f32_16x16x32_bf16 a[8:11], v[32:35], v[8:11], a[8:11]\n" \
"ds_read_b64_tr_b16 v[98:99], v137 offset:24576\n" \
"v_mfma_f32_16x16x32_bf16 a[12:15], v[32:35], v[12:15], a[12:15]\n" \
"v_mfma_f32_16x16x32_bf16 a[16:19], v[32:35], v[16:19], a[16:19]\n" \
"ds_read_b128 v[64:67], v136 offset:16384\n" \
"v_mfma_f32_16x16x32_bf16 a[20:23], v[32:35], v[20:23], a[20:23]\n" \
"v_mfma_f32_16x16x32_bf16 a[24:27], v[32:35], v[24:27], a[24:27]\n" \
"ds_read_b128 v[68:71], v136 offset:17408\n" \
"v_mfma_f32_16x16x32_bf16 a[28:31], v[32:35], v[28:31], a[28:31]\n" \
"v_mfma_f32_16x16x32_bf16 a[32:35], v[36:39], v[0:3], a[32:35]\n" \
"ds_read_b128 v[72:75], v136 offset:18432\n" \
"v_mfma_f32_16x16x32_bf16 a[36:39], v[36:39], v[4:7], a[36:39]\n" \
"v_mfma_f32_16x16x32_bf16 a[40:43], v[36:39], v[8:11], a[40:43]\n" \
"ds_read_b128 v[76:79], v136 offset:19456\n" \
"v_mfma_f32_16x16x32_bf16 a[44:47], v[36:39], v[12:15], a[44:47]\n" \
"v_mfma_f32_16x16x32_bf16 a[48:51], v[36:39], v[16:19], a[48:51]\n" \
"ds_read_b128 v[80:83], v136 offset:20480\n" \
"v_mfma_f32_16x16x32_bf16 a[52:55], v[36:39], v[20:23], a[52:55]\n" \
"v_mfma_f32_16x16x32_bf16 a[56:59], v[36:39], v[24:27], a[56:59]\n" \
"ds_read_b128 v[84:87], v136 offset:21504\n" \
"v_mfma_f32_16x16x32_bf16 a[60:63], v[36:39], v[28:31], a[60:63]\n" \
"v_mfma_f32_16x16x32_bf16 a[64:67], v[40:43], v[0:3], a[64:67]\n" \
"ds_read_b128 v[88:91], v136 offset:22528\n" \
"v_mfma_f32_16x16x32_bf16 a[68:71], v[40:43], v[4:7], a[68:71]\n" \
"v_mfma_f32_16x16x32_bf16 a[72:75], v[40:43], v[8:11], a[72:75]\n" \
"ds_read_b128 v[92:95], v136 offset:23552\n" \
"v_mfma_f32_16x16x32_bf16 a[76:79], v[40:43], v[12:15], a[76:79]\n" \
"v_mfma_f32_16x16x32_bf16 a[80:83], v[40:43], v[16:19], a[80:83]\n" \
"ds_read_b64_tr_b16 v[100:101], v138 offset:16384\n" \
"v_mfma_f32_16x16x32_bf16 a[84:87], v[40:43], v[20:23], a[84:87]\n" \
"v_mfma_f32_16x16x32_bf16 a[88:91], v[40:43], v[24:27], a[88:91]\n" \
"ds_read_b64_tr_b16 v[102:103], v138 offset:24576\n" \
"v_mfma_f32_16x16x32_bf16 a[92:95], v[40:43], v[28:31], a[92:95]\n" \
"v_mfma_f32_16x16x32_bf16 a[96:99], v[44:47], v[0:3], a[96:99]\n" \
"ds_read_b64_tr_b16 v[104:105], v139 offset:16384\n" \
"v_mfma_f32_16x16x32_bf16 a[100:103], v[44:47], v[4:7], a[100:103]\n" \
"v_mfma_f32_16x16x32_bf16 a[104:107], v[44:47], v[8:11], a[104:107]\n" \
"ds_read_b64_tr_b16 v[106:107], v139 offset:24576\n" \
"v_mfma_f32_16x16x32_bf16 a[108:111], v[44:47], v[12:15], a[108:111]\n" \
"v_mfma_f32_16x16x32_bf16 a[112:115], v[44:47], v[16:19], a[112:115]\n" \
"ds_read_b64_tr_b16 v[108:109], v140 offset:16384\n" \
"v_mfma_f32_16x16x32_bf16 a[116:119], v[44:47], v[20:23], a[116:119]\n" \
"v_mfma_f32_16x16x32_bf16 a[120:123], v[44:47], v[24:27], a[120:123]\n" \
"ds_read_b64_tr_b16 v[110:111], v140 offset:24576\n" \
"v_mfma_f32_16x16x32_bf16 a[124:127], v[44:47], v[28:31], a[124:127]\n" \
"v_mfma_f32_16x16x32_bf16 a[128:131], v[48:51], v[0:3], a[128:131]\n" \
"ds_read_b64_tr_b16 v[112:113], v137 offset:17408\n" \
"v_mfma_f32_16x16x32_bf16 a[132:135], v[48:51], v[4:7], a[132:135]\n" \
"v_mfma_f32_16x16x32_bf16 a[136:139], v[48:51], v[8:11], a[136:139]\n" \
"ds_read_b64_tr_b16 v[114:115], v137 offset:25600\n" \
"v_mfma_f32_16x16x32_bf16 a[140:143], v[48:51], v[12:15], a[140:143]\n" \
"v_mfma_f32_16x16x32_bf16 a[144:147], v[48:51], v[16:19], a[144:147]\n" \
"ds_read_b64_tr_b16 v[116:117], v138 offset:17408\n" \
"v_mfma_f32_16x16x32_bf16 a[148:151], v[48:51], v[20:23], a[148:151]\n" \
"v_mfma_f32_16x16x32_bf16 a[152:155], v[48:51], v[24:27], a[152:155]\n" \
"ds_read_b64_tr_b16 v[118:119], v138 offset:25600\n" \
"v_mfma_f32_16x16x32_bf16 a[156:159], v[48:51], v[28:31], a[156:159]\n" \
"v_mfma_f32_16x16x32_bf16 a[160:163], v[52:55], v[0:3], a[160:163]\n" \
"ds_read_b64_tr_b16 v[120:121], v139 offset:17408\n" \
"v_mfma_f32_16x16x32_bf16 a[164:167], v[52:55], v[4:7], a[164:167]\n" \
"v_mfma_f32_16x16x32_bf16 a[168:171], v[52:55], v[8:11], a[168:171]\n" \
"ds_read_b64_tr_b16 v[122:123], v139 offset:25600\n" \
"v_mfma_f32_16x16x32_bf16 a[172:175], v[52:55], v[12:15], a[172:175]\n" \
"v_mfma_f32_16x16x32_bf16 a[176:179], v[52:55], v[16:19], a[176:179]\n" \
"ds_read_b64_tr_b16 v[124:125], v140 offset:17408\n" \
"v_mfma_f32_16x16x32_bf16 a[180:183], v[52:55], v[20:23], a[180:183]\n" \
"v_mfma_f32_16x16x32_bf16 a[184:187], v[52:55], v[24:27], a[184:187]\n" \
"ds_read_b64_tr_b16 v[126:127], v140 offset:25600\n" \
"v_mfma_f32_16x16x32_bf16 a[188:191], v[52:55], v[28:31], a[188:191]\n" \
"v_mfma_f32_16x16x32_bf16 a[192:195], v[56:59], v[0:3], a[192:195]\n" \
"v_mfma_f32_16x16x32_bf16 a[196:199], v[56:59], v[4:7], a[196:199]\n" \
"v_mfma_f32_16x16x32_bf16 a[200:203], v[56:59], v[8:11], a[200:203]\n" \
"v_mfma_f32_16x16x32_bf16 a[204:207], v[56:59], v[12:15], a[204:207]\n" \
"v_mfma_f32_16x16x32_bf16 a[208:211], v[56:59], v[16:19], a[208:211]\n" \
"v_mfma_f32_16x16x32_bf16 a[212:215], v[56:59], v[20:23], a[212:215]\n" \
"v_mfma_f32_16x16x32_bf16 a[216:219], v[56:59], v[24:27], a[216:219]\n" \
"v_mfma_f32_16x16x32_bf16 a[220:223], v[56:59], v[28:31], a[220:223]\n" \
"v_mfma_f32_16x16x32_bf16 a[224:227], v[60:63], v[0:3], a[224:227]\n" \
"v_mfma_f32_16x16x32_bf16 a[228:231], v[60:63], v[4:7], a[228:231]\n" \
"v_mfma_f32_16x16x32_bf16 a[232:235], v[60:63], v[8:11], a[232:235]\n" \
"v_mfma_f32_16x16x32_bf16 a[236:239], v[60:63], v[12:15], a[236:239]\n" \
"v_mfma_f32_16x16x32_bf16 a[240:243], v[60:63], v[16:19], a[240:243]\n" \
"v_mfma_f32_16x16x32_bf16 a[244:247], v[60:63], v[20:23], a[244:247]\n" \
"v_mfma_f32_16x16x32_bf16 a[248:251], v[60:63], v[24:27], a[248:251]\n" \
"v_mfma_f32_16x16x32_bf16 a[252:255], v[60:63], v[28:31], a[252:255]\n" \
"s_waitcnt vmcnt(8) lgkmcnt(0)\n" \
"s_barrier\n" \
"v_mfma_f32_16x16x32_bf16 a[0:3], v[96:99], v[64:67], a[0:3]\n" \
"ds_read_b64_tr_b16 v[32:33], v137 offset:32768\n" \
"v_mfma_f32_16x16x32_bf16 a[4:7], v[96:99], v[68:71], a[4:7]\n" \
"v_mfma_f32_16x16x32_bf16 a[8:11], v[96:99], v[72:75], a[8:11]\n" \
"ds_read_b64_tr_b16 v[34:35], v137 offset:40960\n" \
"v_mfma_f32_16x16x32_bf16 a[12:15], v[96:99], v[76:79], a[12:15]\n" \
"v_mfma_f32_16x16x32_bf16 a[16:19], v[96:99], v[80:83], a[16:19]\n" \
"ds_read_b128 v[0:3], v136 offset:32768\n" \
"v_mfma_f32_16x16x32_bf16 a[20:23], v[96:99], v[84:87], a[20:23]\n" \
"v_mfma_f32_16x16x32_bf16 a[24:27], v[96:99], v[88:91], a[24:27]\n" \
"ds_read_b128 v[4:7], v136 offset:33792\n" \
"v_mfma_f32_16x16x32_bf16 a[28:31], v[96:99], v[92:95], a[28:31]\n" \
"v_mfma_f32_16x16x32_bf16 a[32:35], v[100:103], v[64:67], a[32:35]\n" \
"ds_read_b128 v[8:11], v136 offset:34816\n" \
"v_mfma_f32_16x16x32_bf16 a[36:39], v[100:103], v[68:71], a[36:39]\n" \
"v_mfma_f32_16x16x32_bf16 a[40:43], v[100:103], v[72:75], a[40:43]\n" \
"ds_read_b128 v[12:15], v136 offset:35840\n" \
"v_mfma_f32_16x16x32_bf16 a[44:47], v[100:103], v[76:79], a[44:47]\n" \
"v_mfma_f32_16x16x32_bf16 a[48:51], v[100:103], v[80:83], a[48:51]\n" \
"ds_read_b128 v[16:19], v136 offset:36864\n" \
"v_mfma_f32_16x16x32_bf16 a[52:55], v[100:103], v[84:87], a[52:55]\n" \
"v_mfma_f32_16x16x32_bf16 a[56:59], v[100:103], v[88:91], a[56:59]\n" \
"ds_read_b128 v[20:23], v136 offset:37888\n" \
"v_mfma_f32_16x16x32_bf16 a[60:63], v[100:103], v[92:95], a[60:63]\n" \
"v_mfma_f32_16x16x32_bf16 a[64:67], v[104:107], v[64:67], a[64:67]\n" \
"ds_read_b128 v[24:27], v136 offset:38912\n" \
"v_mfma_f32_16x16x32_bf16 a[68:71], v[104:107], v[68:71], a[68:71]\n" \
"v_mfma_f32_16x16x32_bf16 a[72:75], v[104:107], v[72:75], a[72:75]\n" \
"ds_read_b128 v[28:31], v136 offset:39936\n" \
"v_mfma_f32_16x16x32_bf16 a[76:79], v[104:107], v[76:79], a[76:79]\n" \
"v_mfma_f32_16x16x32_bf16 a[80:83], v[104:107], v[80:83], a[80:83]\n" \
"ds_read_b64_tr_b16 v[36:37], v138 offset:32768\n" \
"v_mfma_f32_16x16x32_bf16 a[84:87], v[104:107], v[84:87], a[84:87]\n" \
"v_mfma_f32_16x16x32_bf16 a[88:91], v[104:107], v[88:91], a[88:91]\n" \
"ds_read_b64_tr_b16 v[38:39], v138 offset:40960\n" \
"v_mfma_f32_16x16x32_bf16 a[92:95], v[104:107], v[92:95], a[92:95]\n" \
"v_mfma_f32_16x16x32_bf16 a[96:99], v[108:111], v[64:67], a[96:99]\n" \
"ds_read_b64_tr_b16 v[40:41], v139 offset:32768\n" \
"v_mfma_f32_16x16x32_bf16 a[100:103], v[108:111], v[68:71], a[100:103]\n" \
"v_mfma_f32_16x16x32_bf16 a[104:107], v[108:111], v[72:75], a[104:107]\n" \
"ds_read_b64_tr_b16 v[42:43], v139 offset:40960\n" \
"v_mfma_f32_16x16x32_bf16 a[108:111], v[108:111], v[76:79], a[108:111]\n" \
"v_mfma_f32_16x16x32_bf16 a[112:115], v[108:111], v[80:83], a[112:115]\n" \
"ds_read_b64_tr_b16 v[44:45], v140 offset:32768\n" \
"v_mfma_f32_16x16x32_bf16 a[116:119], v[108:111], v[84:87], a[116:119]\n" \
"v_mfma_f32_16x16x32_bf16 a[120:123], v[108:111], v[88:91], a[120:123]\n" \
"ds_read_b64_tr_b16 v[46:47], v140 offset:40960\n" \
"v_mfma_f32_16x16x32_bf16 a[124:127], v[108:111], v[92:95], a[124:127]\n" \
"v_mfma_f32_16x16x32_bf16 a[128:131], v[112:115], v[64:67], a[128:131]\n" \
"ds_read_b64_tr_b16 v[48:49], v137 offset:33792\n" \
"v_mfma_f32_16x16x32_bf16 a[132:135], v[112:115], v[68:71], a[132:135]\n" \
"v_mfma_f32_16x16x32_bf16 a[136:139], v[112:115], v[72:75], a[136:139]\n" \
"ds_read_b64_tr_b16 v[50:51], v137 offset:41984\n" \
"v_mfma_f32_16x16x32_bf16 a[140:143], v[112:115], v[76:79], a[140:143]\n" \
"v_mfma_f32_16x16x32_bf16 a[144:147], v[112:115], v[80:83], a[144:147]\n" \
"ds_read_b64_tr_b16 v[52:53], v138 offset:33792\n" \
"v_mfma_f32_16x16x32_bf16 a[148:151], v[112:115], v[84:87], a[148:151]\n" \
"v_mfma_f32_16x16x32_bf16 a[152:155], v[112:115], v[88:91], a[152:155]\n" \
"ds_read_b64_tr_b16 v[54:55], v138 offset:41984\n" \
"v_mfma_f32_16x16x32_bf16 a[156:159], v[112:115], v[92:95], a[156:159]\n" \
"v_mfma_f32_16x16x32_bf16 a[160:163], v[116:119], v[64:67], a[160:163]\n" \
"ds_read_b64_tr_b16 v[56:57], v139 offset:33792\n" \
"v_mfma_f32_16x16x32_bf16 a[164:167], v[116:119], v[68:71], a[164:167]\n" \
"v_mfma_f32_16x16x32_bf16 a[168:171], v[116:119], v[72:75], a[168:171]\n" \
"ds_read_b64_tr_b16 v[58:59], v139 offset:41984\n" \
"v_mfma_f32_16x16x32_bf16 a[172:175], v[116:119], v[76:79], a[172:175]\n" \
"v_mfma_f32_16x16x32_bf16 a[176:179], v[116:119], v[80:83], a[176:179]\n" \
"ds_read_b64_tr_b16 v[60:61], v140 offset:33792\n" \
"v_mfma_f32_16x16x32_bf16 a[180:183], v[116:119], v[84:87], a[180:183]\n" \
"v_mfma_f32_16x16x32_bf16 a[184:187], v[116:119], v[88:91], a[184:187]\n" \
"ds_read_b64_tr_b16 v[62:63], v140 offset:41984\n" \
"v_mfma_f32_16x16x32_bf16 a[188:191], v[116:119], v[92:95], a[188:191]\n" \
"v_mfma_f32_16x16x32_bf16 a[192:195], v[120:123], v[64:67], a[192:195]\n" \
"v_mfma_f32_16x16x32_bf16 a[196:199], v[120:123], v[68:71], a[196:199]\n" \
"v_mfma_f32_16x16x32_bf16 a[200:203], v[120:123], v[72:75], a[200:203]\n" \
"v_mfma_f32_16x16x32_bf16 a[204:207], v[120:123], v[76:79], a[204:207]\n" \
"v_mfma_f32_16x16x32_bf16 a[208:211], v[120:123], v[80:83], a[208:211]\n" \
"v_mfma_f32_16x16x32_bf16 a[212:215], v[120:123], v[84:87], a[212:215]\n" \
"v_mfma_f32_16x16x32_bf16 a[216:219], v[120:123], v[88:91], a[216:219]\n" \
"v_mfma_f32_16x16x32_bf16 a[220:223], v[120:123], v[92:95], a[220:223]\n" \
"v_mfma_f32_16x16x32_bf16 a[224:227], v[124:127], v[64:67], a[224:227]\n" \
"v_mfma_f32_16x16x32_bf16 a[228:231], v[124:127], v[68:71], a[228:231]\n" \
"v_mfma_f32_16x16x32_bf16 a[232:235], v[124:127], v[72:75], a[232:235]\n" \
"v_mfma_f32_16x16x32_bf16 a[236:239], v[124:127], v[76:79], a[236:239]\n" \
"v_mfma_f32_16x16x32_bf16 a[240:243], v[124:127], v[80:83], a[240:243]\n" \
"v_mfma_f32_16x16x32_bf16 a[244:247], v[124:127], v[84:87], a[244:247]\n" \
"v_mfma_f32_16x16x32_bf16 a[248:251], v[124:127], v[88:91], a[248:251]\n" \
"v_mfma_f32_16x16x32_bf16 a[252:255], v[124:127], v[92:95], a[252:255]\n" \
"s_waitcnt vmcnt(0) lgkmcnt(0)\n" \
"s_barrier\n" \
"v_mfma_f32_16x16x32_bf16 a[0:3], v[32:35], v[0:3], a[0:3]\n" \
"ds_read_b64_tr_b16 v[96:97], v137 offset:49152\n" \
"v_mfma_f32_16x16x32_bf16 a[4:7], v[32:35], v[4:7], a[4:7]\n" \
"v_mfma_f32_16x16x32_bf16 a[8:11], v[32:35], v[8:11], a[8:11]\n" \
"ds_read_b64_tr_b16 v[98:99], v137 offset:57344\n" \
"v_mfma_f32_16x16x32_bf16 a[12:15], v[32:35], v[12:15], a[12:15]\n" \
"v_mfma_f32_16x16x32_bf16 a[16:19], v[32:35], v[16:19], a[16:19]\n" \
"ds_read_b128 v[64:67], v136 offset:49152\n" \
"v_mfma_f32_16x16x32_bf16 a[20:23], v[32:35], v[20:23], a[20:23]\n" \
"v_mfma_f32_16x16x32_bf16 a[24:27], v[32:35], v[24:27], a[24:27]\n" \
"ds_read_b128 v[68:71], v136 offset:50176\n" \
"v_mfma_f32_16x16x32_bf16 a[28:31], v[32:35], v[28:31], a[28:31]\n" \
"v_mfma_f32_16x16x32_bf16 a[32:35], v[36:39], v[0:3], a[32:35]\n" \
"ds_read_b128 v[72:75], v136 offset:51200\n" \
"v_mfma_f32_16x16x32_bf16 a[36:39], v[36:39], v[4:7], a[36:39]\n" \
"v_mfma_f32_16x16x32_bf16 a[40:43], v[36:39], v[8:11], a[40:43]\n" \
"ds_read_b128 v[76:79], v136 offset:52224\n" \
"v_mfma_f32_16x16x32_bf16 a[44:47], v[36:39], v[12:15], a[44:47]\n" \
"v_mfma_f32_16x16x32_bf16 a[48:51], v[36:39], v[16:19], a[48:51]\n" \
"ds_read_b128 v[80:83], v136 offset:53248\n" \
"v_mfma_f32_16x16x32_bf16 a[52:55], v[36:39], v[20:23], a[52:55]\n" \
"v_mfma_f32_16x16x32_bf16 a[56:59], v[36:39], v[24:27], a[56:59]\n" \
"ds_read_b128 v[84:87], v136 offset:54272\n" \
"v_mfma_f32_16x16x32_bf16 a[60:63], v[36:39], v[28:31], a[60:63]\n" \
"v_mfma_f32_16x16x32_bf16 a[64:67], v[40:43], v[0:3], a[64:67]\n" \
"ds_read_b128 v[88:91], v136 offset:55296\n" \
"v_mfma_f32_16x16x32_bf16 a[68:71], v[40:43], v[4:7], a[68:71]\n" \
"v_mfma_f32_16x16x32_bf16 a[72:75], v[40:43], v[8:11], a[72:75]\n" \
"ds_read_b128 v[92:95], v136 offset:56320\n" \
"v_mfma_f32_16x16x32_bf16 a[76:79], v[40:43], v[12:15], a[76:79]\n" \
"v_mfma_f32_16x16x32_bf16 a[80:83], v[40:43], v[16:19], a[80:83]\n" \
"ds_read_b64_tr_b16 v[100:101], v138 offset:49152\n" \
"v_mfma_f32_16x16x32_bf16 a[84:87], v[40:43], v[20:23], a[84:87]\n" \
"v_mfma_f32_16x16x32_bf16 a[88:91], v[40:43], v[24:27], a[88:91]\n" \
"ds_read_b64_tr_b16 v[102:103], v138 offset:57344\n" \
"v_mfma_f32_16x16x32_bf16 a[92:95], v[40:43], v[28:31], a[92:95]\n" \
"v_mfma_f32_16x16x32_bf16 a[96:99], v[44:47], v[0:3], a[96:99]\n" \
"ds_read_b64_tr_b16 v[104:105], v139 offset:49152\n" \
"v_mfma_f32_16x16x32_bf16 a[100:103], v[44:47], v[4:7], a[100:103]\n" \
"v_mfma_f32_16x16x32_bf16 a[104:107], v[44:47], v[8:11], a[104:107]\n" \
"ds_read_b64_tr_b16 v[106:107], v139 offset:57344\n" \
"v_mfma_f32_16x16x32_bf16 a[108:111], v[44:47], v[12:15], a[108:111]\n" \
"v_mfma_f32_16x16x32_bf16 a[112:115], v[44:47], v[16:19], a[112:115]\n" \
"ds_read_b64_tr_b16 v[108:109], v140 offset:49152\n" \
"v_mfma_f32_16x16x32_bf16 a[116:119], v[44:47], v[20:23], a[116:119]\n" \
"v_mfma_f32_16x16x32_bf16 a[120:123], v[44:47], v[24:27], a[120:123]\n" \
"ds_read_b64_tr_b16 v[110:111], v140 offset:57344\n" \
"v_mfma_f32_16x16x32_bf16 a[124:127], v[44:47], v[28:31], a[124:127]\n" \
"v_mfma_f32_16x16x32_bf16 a[128:131], v[48:51], v[0:3], a[128:131]\n" \
"ds_read_b64_tr_b16 v[112:113], v137 offset:50176\n" \
"v_mfma_f32_16x16x32_bf16 a[132:135], v[48:51], v[4:7], a[132:135]\n" \
"v_mfma_f32_16x16x32_bf16 a[136:139], v[48:51], v[8:11], a[136:139]\n" \
"ds_read_b64_tr_b16 v[114:115], v137 offset:58368\n" \
"v_mfma_f32_16x16x32_bf16 a[140:143], v[48:51], v[12:15], a[140:143]\n" \
"v_mfma_f32_16x16x32_bf16 a[144:147], v[48:51], v[16:19], a[144:147]\n" \
"ds_read_b64_tr_b16 v[116:117], v138 offset:50176\n" \
"v_mfma_f32_16x16x32_bf16 a[148:151], v[48:51], v[20:23], a[148:151]\n" \
"v_mfma_f32_16x16x32_bf16 a[152:155], v[48:51], v[24:27], a[152:155]\n" \
"ds_read_b64_tr_b16 v[118:119], v138 offset:58368\n" \
"v_mfma_f32_16x16x32_bf16 a[156:159], v[48:51], v[28:31], a[156:159]\n" \
"v_mfma_f32_16x16x32_bf16 a[160:163], v[52:55], v[0:3], a[160:163]\n" \
"ds_read_b64_tr_b16 v[120:121], v139 offset:50176\n" \
"v_mfma_f32_16x16x32_bf16 a[164:167], v[52:55], v[4:7], a[164:167]\n" \
"v_mfma_f32_16x16x32_bf16 a[168:171], v[52:55], v[8:11], a[168:171]\n" \
"ds_read_b64_tr_b16 v[122:123], v139 offset:58368\n" \
"v_mfma_f32_16x16x32_bf16 a[172:175], v[52:55], v[12:15], a[172:175]\n" \
"v_mfma_f32_16x16x32_bf16 a[176:179], v[52:55], v[16:19], a[176:179]\n" \
"ds_read_b64_tr_b16 v[124:125], v140 offset:50176\n" \
"v_mfma_f32_16x16x32_bf16 a[180:183], v[52:55], v[20:23], a[180:183]\n" \
"v_mfma_f32_16x16x32_bf16 a[184:187], v[52:55], v[24:27], a[184:187]\n" \
"ds_read_b64_tr_b16 v[126:127], v140 offset:58368\n" \
"v_mfma_f32_16x16x32_bf16 a[188:191], v[52:55], v[28:31], a[188:191]\n" \
"v_mfma_f32_16x16x32_bf16 a[192:195], v[56:59], v[0:3], a[192:195]\n" \
"v_mfma_f32_16x16x32_bf16 a[196:199], v[56:59], v[4:7], a[196:199]\n" \
"v_mfma_f32_16x16x32_bf16 a[200:203], v[56:59], v[8:11], a[200:203]\n" \
"v_mfma_f32_16x16x32_bf16 a[204:207], v[56:59], v[12:15], a[204:207]\n" \
"v_mfma_f32_16x16x32_bf16 a[208:211], v[56:59], v[16:19], a[208:211]\n" \
"v_mfma_f32_16x16x32_bf16 a[212:215], v[56:59], v[20:23], a[212:215]\n" \
"v_mfma_f32_16x16x32_bf16 a[216:219], v[56:59], v[24:27], a[216:219]\n" \
"v_mfma_f32_16x16x32_bf16 a[220:223], v[56:59], v[28:31], a[220:223]\n" \
"v_mfma_f32_16x16x32_bf16 a[224:227], v[60:63], v[0:3], a[224:227]\n" \
"v_mfma_f32_16x16x32_bf16 a[228:231], v[60:63], v[4:7], a[228:231]\n" \
"v_mfma_f32_16x16x32_bf16 a[232:235], v[60:63], v[8:11], a[232:235]\n" \
"v_mfma_f32_16x16x32_bf16 a[236:239], v[60:63], v[12:15], a[236:239]\n" \
"v_mfma_f32_16x16x32_bf16 a[240:243], v[60:63], v[16:19], a[240:243]\n" \
"v_mfma_f32_16x16x32_bf16 a[244:247], v[60:63], v[20:23], a[244:247]\n" \
"v_mfma_f32_16x16x32_bf16 a[248:251], v[60:63], v[24:27], a[248:251]\n" \
"v_mfma_f32_16x16x32_bf16 a[252:255], v[60:63], v[28:31], a[252:255]\n" \
"s_waitcnt lgkmcnt(0)\n" \
"v_mfma_f32_16x16x32_bf16 a[0:3], v[96:99], v[64:67], a[0:3]\n" \
"v_mfma_f32_16x16x32_bf16 a[4:7], v[96:99], v[68:71], a[4:7]\n" \
"v_mfma_f32_16x16x32_bf16 a[8:11], v[96:99], v[72:75], a[8:11]\n" \
"v_mfma_f32_16x16x32_bf16 a[12:15], v[96:99], v[76:79], a[12:15]\n" \
"v_mfma_f32_16x16x32_bf16 a[16:19], v[96:99], v[80:83], a[16:19]\n" \
"v_mfma_f32_16x16x32_bf16 a[20:23], v[96:99], v[84:87], a[20:23]\n" \
"v_mfma_f32_16x16x32_bf16 a[24:27], v[96:99], v[88:91], a[24:27]\n" \
"v_mfma_f32_16x16x32_bf16 a[28:31], v[96:99], v[92:95], a[28:31]\n" \
"v_mfma_f32_16x16x32_bf16 a[32:35], v[100:103], v[64:67], a[32:35]\n" \
"v_mfma_f32_16x16x32_bf16 a[36:39], v[100:103], v[68:71], a[36:39]\n" \
"v_mfma_f32_16x16x32_bf16 a[40:43], v[100:103], v[72:75], a[40:43]\n" \
"v_mfma_f32_16x16x32_bf16 a[44:47], v[100:103], v[76:79], a[44:47]\n" \
"v_mfma_f32_16x16x32_bf16 a[48:51], v[100:103], v[80:83], a[48:51]\n" \
"v_mfma_f32_16x16x32_bf16 a[52:55], v[100:103], v[84:87], a[52:55]\n" \
"v_mfma_f32_16x16x32_bf16 a[56:59], v[100:103], v[88:91], a[56:59]\n" \
"v_mfma_f32_16x16x32_bf16 a[60:63], v[100:103], v[92:95], a[60:63]\n" \
"v_mfma_f32_16x16x32_bf16 a[64:67], v[104:107], v[64:67], a[64:67]\n" \
"v_mfma_f32_16x16x32_bf16 a[68:71], v[104:107], v[68:71], a[68:71]\n" \
"v_mfma_f32_16x16x32_bf16 a[72:75], v[104:107], v[72:75], a[72:75]\n" \
"v_mfma_f32_16x16x32_bf16 a[76:79], v[104:107], v[76:79], a[76:79]\n" \
"v_mfma_f32_16x16x32_bf16 a[80:83], v[104:107], v[80:83], a[80:83]\n" \
"v_mfma_f32_16x16x32_bf16 a[84:87], v[104:107], v[84:87], a[84:87]\n" \
"v_mfma_f32_16x16x32_bf16 a[88:91], v[104:107], v[88:91], a[88:91]\n" \
"v_mfma_f32_16x16x32_bf16 a[92:95], v[104:107], v[92:95], a[92:95]\n" \
"v_mfma_f32_16x16x32_bf16 a[96:99], v[108:111], v[64:67], a[96:99]\n" \
"v_mfma_f32_16x16x32_bf16 a[100:103], v[108:111], v[68:71], a[100:103]\n" \
"v_mfma_f32_16x16x32_bf16 a[104:107], v[108:111], v[72:75], a[104:107]\n" \
"v_mfma_f32_16x16x32_bf16 a[108:111], v[108:111], v[76:79], a[108:111]\n" \
"v_mfma_f32_16x16x32_bf16 a[112:115], v[108:111], v[80:83], a[112:115]\n" \
"v_mfma_f32_16x16x32_bf16 a[116:119], v[108:111], v[84:87], a[116:119]\n" \
"v_mfma_f32_16x16x32_bf16 a[120:123], v[108:111], v[88:91], a[120:123]\n" \
"v_mfma_f32_16x16x32_bf16 a[124:127], v[108:111], v[92:95], a[124:127]\n" \
"v_mfma_f32_16x16x32_bf16 a[128:131], v[112:115], v[64:67], a[128:131]\n" \
"v_mfma_f32_16x16x32_bf16 a[132:135], v[112:115], v[68:71], a[132:135]\n" \
"v_mfma_f32_16x16x32_bf16 a[136:139], v[112:115], v[72:75], a[136:139]\n" \
"v_mfma_f32_16x16x32_bf16 a[140:143], v[112:115], v[76:79], a[140:143]\n" \
"v_mfma_f32_16x16x32_bf16 a[144:147], v[112:115], v[80:83], a[144:147]\n" \
"v_mfma_f32_16x16x32_bf16 a[148:151], v[112:115], v[84:87], a[148:151]\n" \
"v_mfma_f32_16x16x32_bf16 a[152:155], v[112:115], v[88:91], a[152:155]\n" \
"v_mfma_f32_16x16x32_bf16 a[156:159], v[112:115], v[92:95], a[156:159]\n" \
"v_mfma_f32_16x16x32_bf16 a[160:163], v[116:119], v[64:67], a[160:163]\n" \
"v_mfma_f32_16x16x32_bf16 a[164:167], v[116:119], v[68:71], a[164:167]\n" \
"v_mfma_f32_16x16x32_bf16 a[168:171], v[116:119], v[72:75], a[168:171]\n" \
"v_mfma_f32_16x16x32_bf16 a[172:175], v[116:119], v[76:79], a[172:175]\n" \
"v_mfma_f32_16x16x32_bf16 a[176:179], v[116:119], v[80:83], a[176:179]\n" \
"v_mfma_f32_16x16x32_bf16 a[180:183], v[116:119], v[84:87], a[180:183]\n" \
"v_mfma_f32_16x16x32_bf16 a[184:187], v[116:119], v[88:91], a[184:187]\n" \
"v_mfma_f32_16x16x32_bf16 a[188:191], v[116:119], v[92:95], a[188:191]\n" \
"v_mfma_f32_16x16x32_bf16 a[192:195], v[120:123], v[64:67], a[192:195]\n" \
"v_mfma_f32_16x16x32_bf16 a[196:199], v[120:123], v[68:71], a[196:199]\n" \
"v_mfma_f32_16x16x32_bf16 a[200:203], v[120:123], v[72:75], a[200:203]\n" \
"v_mfma_f32_16x16x32_bf16 a[204:207], v[120:123], v[76:79], a[204:207]\n" \
"v_mfma_f32_16x16x32_bf16 a[208:211], v[120:123], v[80:83], a[208:211]\n" \
"v_mfma_f32_16x16x32_bf16 a[212:215], v[120:123], v[84:87], a[212:215]\n" \
"v_mfma_f32_16x16x32_bf16 a[216:219], v[120:123], v[88:91], a[216:219]\n" \
"v_mfma_f32_16x16x32_bf16 a[220:223], v[120:123], v[92:95], a[220:223]\n" \
"v_mfma_f32_16x16x32_bf16 a[224:227], v[124:127], v[64:67], a[224:227]\n" \
"v_mfma_f32_16x16x32_bf16 a[228:231], v[124:127], v[68:71], a[228:231]\n" \
"v_mfma_f32_16x16x32_bf16 a[232:235], v[124:127], v[72:75], a[232:235]\n" \
"v_mfma_f32_16x16x32_bf16 a[236:239], v[124:127], v[76:79], a[236:239]\n" \
"v_mfma_f32_16x16x32_bf16 a[240:243], v[124:127], v[80:83], a[240:243]\n" \
"v_mfma_f32_16x16x32_bf16 a[244:247], v[124:127], v[84:87], a[244:247]\n" \
"v_mfma_f32_16x16x32_bf16 a[248:251], v[124:127], v[88:91], a[248:251]\n" \
"v_mfma_f32_16x16x32_bf16 a[252:255], v[124:127], v[92:95], a[252:255]\n" \
"s_nop 7\n" \
"LG88_eF_%=:\n" \
"v_accvgpr_read_b32 v0, a0\n" \
"v_accvgpr_read_b32 v1, a1\n" \
"v_accvgpr_read_b32 v2, a2\n" \
"v_accvgpr_read_b32 v3, a3\n" \
"v_accvgpr_read_b32 v4, a32\n" \
"v_accvgpr_read_b32 v5, a33\n" \
"v_accvgpr_read_b32 v6, a34\n" \
"v_accvgpr_read_b32 v7, a35\n" \
"v_cvt_pk_bf16_f32 v0, v0, v1\n" \
"v_cvt_pk_bf16_f32 v1, v2, v3\n" \
"v_cvt_pk_bf16_f32 v2, v4, v5\n" \
"v_cvt_pk_bf16_f32 v3, v6, v7\n" \
"buffer_store_dwordx4 v[0:3], v141, s[60:63], s64 offen offset:0 sc1\n" \
"v_accvgpr_read_b32 v8, a64\n" \
"v_accvgpr_read_b32 v9, a65\n" \
"v_accvgpr_read_b32 v10, a66\n" \
"v_accvgpr_read_b32 v11, a67\n" \
"v_accvgpr_read_b32 v12, a96\n" \
"v_accvgpr_read_b32 v13, a97\n" \
"v_accvgpr_read_b32 v14, a98\n" \
"v_accvgpr_read_b32 v15, a99\n" \
"v_cvt_pk_bf16_f32 v8, v8, v9\n" \
"v_cvt_pk_bf16_f32 v9, v10, v11\n" \
"v_cvt_pk_bf16_f32 v10, v12, v13\n" \
"v_cvt_pk_bf16_f32 v11, v14, v15\n" \
"buffer_store_dwordx4 v[8:11], v141, s[60:63], s64 offen offset:1024 sc1\n" \
"v_accvgpr_read_b32 v16, a128\n" \
"v_accvgpr_read_b32 v17, a129\n" \
"v_accvgpr_read_b32 v18, a130\n" \
"v_accvgpr_read_b32 v19, a131\n" \
"v_accvgpr_read_b32 v20, a160\n" \
"v_accvgpr_read_b32 v21, a161\n" \
"v_accvgpr_read_b32 v22, a162\n" \
"v_accvgpr_read_b32 v23, a163\n" \
"v_cvt_pk_bf16_f32 v16, v16, v17\n" \
"v_cvt_pk_bf16_f32 v17, v18, v19\n" \
"v_cvt_pk_bf16_f32 v18, v20, v21\n" \
"v_cvt_pk_bf16_f32 v19, v22, v23\n" \
"buffer_store_dwordx4 v[16:19], v141, s[60:63], s64 offen offset:2048 sc1\n" \
"v_accvgpr_read_b32 v24, a192\n" \
"v_accvgpr_read_b32 v25, a193\n" \
"v_accvgpr_read_b32 v26, a194\n" \
"v_accvgpr_read_b32 v27, a195\n" \
"v_accvgpr_read_b32 v28, a224\n" \
"v_accvgpr_read_b32 v29, a225\n" \
"v_accvgpr_read_b32 v30, a226\n" \
"v_accvgpr_read_b32 v31, a227\n" \
"v_cvt_pk_bf16_f32 v24, v24, v25\n" \
"v_cvt_pk_bf16_f32 v25, v26, v27\n" \
"v_cvt_pk_bf16_f32 v26, v28, v29\n" \
"v_cvt_pk_bf16_f32 v27, v30, v31\n" \
"buffer_store_dwordx4 v[24:27], v141, s[60:63], s64 offen offset:3072 sc1\n" \
"s_add_u32 s64, s64, s65\n" \
"v_accvgpr_read_b32 v32, a4\n" \
"v_accvgpr_read_b32 v33, a5\n" \
"v_accvgpr_read_b32 v34, a6\n" \
"v_accvgpr_read_b32 v35, a7\n" \
"v_accvgpr_read_b32 v36, a36\n" \
"v_accvgpr_read_b32 v37, a37\n" \
"v_accvgpr_read_b32 v38, a38\n" \
"v_accvgpr_read_b32 v39, a39\n" \
"v_cvt_pk_bf16_f32 v32, v32, v33\n" \
"v_cvt_pk_bf16_f32 v33, v34, v35\n" \
"v_cvt_pk_bf16_f32 v34, v36, v37\n" \
"v_cvt_pk_bf16_f32 v35, v38, v39\n" \
"buffer_store_dwordx4 v[32:35], v141, s[60:63], s64 offen offset:0 sc1\n" \
"v_accvgpr_read_b32 v40, a68\n" \
"v_accvgpr_read_b32 v41, a69\n" \
"v_accvgpr_read_b32 v42, a70\n" \
"v_accvgpr_read_b32 v43, a71\n" \
"v_accvgpr_read_b32 v44, a100\n" \
"v_accvgpr_read_b32 v45, a101\n" \
"v_accvgpr_read_b32 v46, a102\n" \
"v_accvgpr_read_b32 v47, a103\n" \
"v_cvt_pk_bf16_f32 v40, v40, v41\n" \
"v_cvt_pk_bf16_f32 v41, v42, v43\n" \
"v_cvt_pk_bf16_f32 v42, v44, v45\n" \
"v_cvt_pk_bf16_f32 v43, v46, v47\n" \
"buffer_store_dwordx4 v[40:43], v141, s[60:63], s64 offen offset:1024 sc1\n" \
"v_accvgpr_read_b32 v48, a132\n" \
"v_accvgpr_read_b32 v49, a133\n" \
"v_accvgpr_read_b32 v50, a134\n" \
"v_accvgpr_read_b32 v51, a135\n" \
"v_accvgpr_read_b32 v52, a164\n" \
"v_accvgpr_read_b32 v53, a165\n" \
"v_accvgpr_read_b32 v54, a166\n" \
"v_accvgpr_read_b32 v55, a167\n" \
"v_cvt_pk_bf16_f32 v48, v48, v49\n" \
"v_cvt_pk_bf16_f32 v49, v50, v51\n" \
"v_cvt_pk_bf16_f32 v50, v52, v53\n" \
"v_cvt_pk_bf16_f32 v51, v54, v55\n" \
"buffer_store_dwordx4 v[48:51], v141, s[60:63], s64 offen offset:2048 sc1\n" \
"v_accvgpr_read_b32 v56, a196\n" \
"v_accvgpr_read_b32 v57, a197\n" \
"v_accvgpr_read_b32 v58, a198\n" \
"v_accvgpr_read_b32 v59, a199\n" \
"v_accvgpr_read_b32 v60, a228\n" \
"v_accvgpr_read_b32 v61, a229\n" \
"v_accvgpr_read_b32 v62, a230\n" \
"v_accvgpr_read_b32 v63, a231\n" \
"v_cvt_pk_bf16_f32 v56, v56, v57\n" \
"v_cvt_pk_bf16_f32 v57, v58, v59\n" \
"v_cvt_pk_bf16_f32 v58, v60, v61\n" \
"v_cvt_pk_bf16_f32 v59, v62, v63\n" \
"buffer_store_dwordx4 v[56:59], v141, s[60:63], s64 offen offset:3072 sc1\n" \
"s_add_u32 s64, s64, s65\n" \
"v_accvgpr_read_b32 v0, a8\n" \
"v_accvgpr_read_b32 v1, a9\n" \
"v_accvgpr_read_b32 v2, a10\n" \
"v_accvgpr_read_b32 v3, a11\n" \
"v_accvgpr_read_b32 v4, a40\n" \
"v_accvgpr_read_b32 v5, a41\n" \
"v_accvgpr_read_b32 v6, a42\n" \
"v_accvgpr_read_b32 v7, a43\n" \
"v_cvt_pk_bf16_f32 v0, v0, v1\n" \
"v_cvt_pk_bf16_f32 v1, v2, v3\n" \
"v_cvt_pk_bf16_f32 v2, v4, v5\n" \
"v_cvt_pk_bf16_f32 v3, v6, v7\n" \
"buffer_store_dwordx4 v[0:3], v141, s[60:63], s64 offen offset:0 sc1\n" \
"v_accvgpr_read_b32 v8, a72\n" \
"v_accvgpr_read_b32 v9, a73\n" \
"v_accvgpr_read_b32 v10, a74\n" \
"v_accvgpr_read_b32 v11, a75\n" \
"v_accvgpr_read_b32 v12, a104\n" \
"v_accvgpr_read_b32 v13, a105\n" \
"v_accvgpr_read_b32 v14, a106\n" \
"v_accvgpr_read_b32 v15, a107\n" \
"v_cvt_pk_bf16_f32 v8, v8, v9\n" \
"v_cvt_pk_bf16_f32 v9, v10, v11\n" \
"v_cvt_pk_bf16_f32 v10, v12, v13\n" \
"v_cvt_pk_bf16_f32 v11, v14, v15\n" \
"buffer_store_dwordx4 v[8:11], v141, s[60:63], s64 offen offset:1024 sc1\n" \
"v_accvgpr_read_b32 v16, a136\n" \
"v_accvgpr_read_b32 v17, a137\n" \
"v_accvgpr_read_b32 v18, a138\n" \
"v_accvgpr_read_b32 v19, a139\n" \
"v_accvgpr_read_b32 v20, a168\n" \
"v_accvgpr_read_b32 v21, a169\n" \
"v_accvgpr_read_b32 v22, a170\n" \
"v_accvgpr_read_b32 v23, a171\n" \
"v_cvt_pk_bf16_f32 v16, v16, v17\n" \
"v_cvt_pk_bf16_f32 v17, v18, v19\n" \
"v_cvt_pk_bf16_f32 v18, v20, v21\n" \
"v_cvt_pk_bf16_f32 v19, v22, v23\n" \
"buffer_store_dwordx4 v[16:19], v141, s[60:63], s64 offen offset:2048 sc1\n" \
"v_accvgpr_read_b32 v24, a200\n" \
"v_accvgpr_read_b32 v25, a201\n" \
"v_accvgpr_read_b32 v26, a202\n" \
"v_accvgpr_read_b32 v27, a203\n" \
"v_accvgpr_read_b32 v28, a232\n" \
"v_accvgpr_read_b32 v29, a233\n" \
"v_accvgpr_read_b32 v30, a234\n" \
"v_accvgpr_read_b32 v31, a235\n" \
"v_cvt_pk_bf16_f32 v24, v24, v25\n" \
"v_cvt_pk_bf16_f32 v25, v26, v27\n" \
"v_cvt_pk_bf16_f32 v26, v28, v29\n" \
"v_cvt_pk_bf16_f32 v27, v30, v31\n" \
"buffer_store_dwordx4 v[24:27], v141, s[60:63], s64 offen offset:3072 sc1\n" \
"s_add_u32 s64, s64, s65\n" \
"v_accvgpr_read_b32 v32, a12\n" \
"v_accvgpr_read_b32 v33, a13\n" \
"v_accvgpr_read_b32 v34, a14\n" \
"v_accvgpr_read_b32 v35, a15\n" \
"v_accvgpr_read_b32 v36, a44\n" \
"v_accvgpr_read_b32 v37, a45\n" \
"v_accvgpr_read_b32 v38, a46\n" \
"v_accvgpr_read_b32 v39, a47\n" \
"v_cvt_pk_bf16_f32 v32, v32, v33\n" \
"v_cvt_pk_bf16_f32 v33, v34, v35\n" \
"v_cvt_pk_bf16_f32 v34, v36, v37\n" \
"v_cvt_pk_bf16_f32 v35, v38, v39\n" \
"buffer_store_dwordx4 v[32:35], v141, s[60:63], s64 offen offset:0 sc1\n" \
"v_accvgpr_read_b32 v40, a76\n" \
"v_accvgpr_read_b32 v41, a77\n" \
"v_accvgpr_read_b32 v42, a78\n" \
"v_accvgpr_read_b32 v43, a79\n" \
"v_accvgpr_read_b32 v44, a108\n" \
"v_accvgpr_read_b32 v45, a109\n" \
"v_accvgpr_read_b32 v46, a110\n" \
"v_accvgpr_read_b32 v47, a111\n" \
"v_cvt_pk_bf16_f32 v40, v40, v41\n" \
"v_cvt_pk_bf16_f32 v41, v42, v43\n" \
"v_cvt_pk_bf16_f32 v42, v44, v45\n" \
"v_cvt_pk_bf16_f32 v43, v46, v47\n" \
"buffer_store_dwordx4 v[40:43], v141, s[60:63], s64 offen offset:1024 sc1\n" \
"v_accvgpr_read_b32 v48, a140\n" \
"v_accvgpr_read_b32 v49, a141\n" \
"v_accvgpr_read_b32 v50, a142\n" \
"v_accvgpr_read_b32 v51, a143\n" \
"v_accvgpr_read_b32 v52, a172\n" \
"v_accvgpr_read_b32 v53, a173\n" \
"v_accvgpr_read_b32 v54, a174\n" \
"v_accvgpr_read_b32 v55, a175\n" \
"v_cvt_pk_bf16_f32 v48, v48, v49\n" \
"v_cvt_pk_bf16_f32 v49, v50, v51\n" \
"v_cvt_pk_bf16_f32 v50, v52, v53\n" \
"v_cvt_pk_bf16_f32 v51, v54, v55\n" \
"buffer_store_dwordx4 v[48:51], v141, s[60:63], s64 offen offset:2048 sc1\n" \
"v_accvgpr_read_b32 v56, a204\n" \
"v_accvgpr_read_b32 v57, a205\n" \
"v_accvgpr_read_b32 v58, a206\n" \
"v_accvgpr_read_b32 v59, a207\n" \
"v_accvgpr_read_b32 v60, a236\n" \
"v_accvgpr_read_b32 v61, a237\n" \
"v_accvgpr_read_b32 v62, a238\n" \
"v_accvgpr_read_b32 v63, a239\n" \
"v_cvt_pk_bf16_f32 v56, v56, v57\n" \
"v_cvt_pk_bf16_f32 v57, v58, v59\n" \
"v_cvt_pk_bf16_f32 v58, v60, v61\n" \
"v_cvt_pk_bf16_f32 v59, v62, v63\n" \
"buffer_store_dwordx4 v[56:59], v141, s[60:63], s64 offen offset:3072 sc1\n" \
"s_add_u32 s64, s64, s65\n" \
"v_accvgpr_read_b32 v0, a16\n" \
"v_accvgpr_read_b32 v1, a17\n" \
"v_accvgpr_read_b32 v2, a18\n" \
"v_accvgpr_read_b32 v3, a19\n" \
"v_accvgpr_read_b32 v4, a48\n" \
"v_accvgpr_read_b32 v5, a49\n" \
"v_accvgpr_read_b32 v6, a50\n" \
"v_accvgpr_read_b32 v7, a51\n" \
"v_cvt_pk_bf16_f32 v0, v0, v1\n" \
"v_cvt_pk_bf16_f32 v1, v2, v3\n" \
"v_cvt_pk_bf16_f32 v2, v4, v5\n" \
"v_cvt_pk_bf16_f32 v3, v6, v7\n" \
"buffer_store_dwordx4 v[0:3], v141, s[60:63], s64 offen offset:0 sc1\n" \
"v_accvgpr_read_b32 v8, a80\n" \
"v_accvgpr_read_b32 v9, a81\n" \
"v_accvgpr_read_b32 v10, a82\n" \
"v_accvgpr_read_b32 v11, a83\n" \
"v_accvgpr_read_b32 v12, a112\n" \
"v_accvgpr_read_b32 v13, a113\n" \
"v_accvgpr_read_b32 v14, a114\n" \
"v_accvgpr_read_b32 v15, a115\n" \
"v_cvt_pk_bf16_f32 v8, v8, v9\n" \
"v_cvt_pk_bf16_f32 v9, v10, v11\n" \
"v_cvt_pk_bf16_f32 v10, v12, v13\n" \
"v_cvt_pk_bf16_f32 v11, v14, v15\n" \
"buffer_store_dwordx4 v[8:11], v141, s[60:63], s64 offen offset:1024 sc1\n" \
"v_accvgpr_read_b32 v16, a144\n" \
"v_accvgpr_read_b32 v17, a145\n" \
"v_accvgpr_read_b32 v18, a146\n" \
"v_accvgpr_read_b32 v19, a147\n" \
"v_accvgpr_read_b32 v20, a176\n" \
"v_accvgpr_read_b32 v21, a177\n" \
"v_accvgpr_read_b32 v22, a178\n" \
"v_accvgpr_read_b32 v23, a179\n" \
"v_cvt_pk_bf16_f32 v16, v16, v17\n" \
"v_cvt_pk_bf16_f32 v17, v18, v19\n" \
"v_cvt_pk_bf16_f32 v18, v20, v21\n" \
"v_cvt_pk_bf16_f32 v19, v22, v23\n" \
"buffer_store_dwordx4 v[16:19], v141, s[60:63], s64 offen offset:2048 sc1\n" \
"v_accvgpr_read_b32 v24, a208\n" \
"v_accvgpr_read_b32 v25, a209\n" \
"v_accvgpr_read_b32 v26, a210\n" \
"v_accvgpr_read_b32 v27, a211\n" \
"v_accvgpr_read_b32 v28, a240\n" \
"v_accvgpr_read_b32 v29, a241\n" \
"v_accvgpr_read_b32 v30, a242\n" \
"v_accvgpr_read_b32 v31, a243\n" \
"v_cvt_pk_bf16_f32 v24, v24, v25\n" \
"v_cvt_pk_bf16_f32 v25, v26, v27\n" \
"v_cvt_pk_bf16_f32 v26, v28, v29\n" \
"v_cvt_pk_bf16_f32 v27, v30, v31\n" \
"buffer_store_dwordx4 v[24:27], v141, s[60:63], s64 offen offset:3072 sc1\n" \
"s_add_u32 s64, s64, s65\n" \
"v_accvgpr_read_b32 v32, a20\n" \
"v_accvgpr_read_b32 v33, a21\n" \
"v_accvgpr_read_b32 v34, a22\n" \
"v_accvgpr_read_b32 v35, a23\n" \
"v_accvgpr_read_b32 v36, a52\n" \
"v_accvgpr_read_b32 v37, a53\n" \
"v_accvgpr_read_b32 v38, a54\n" \
"v_accvgpr_read_b32 v39, a55\n" \
"v_cvt_pk_bf16_f32 v32, v32, v33\n" \
"v_cvt_pk_bf16_f32 v33, v34, v35\n" \
"v_cvt_pk_bf16_f32 v34, v36, v37\n" \
"v_cvt_pk_bf16_f32 v35, v38, v39\n" \
"buffer_store_dwordx4 v[32:35], v141, s[60:63], s64 offen offset:0 sc1\n" \
"v_accvgpr_read_b32 v40, a84\n" \
"v_accvgpr_read_b32 v41, a85\n" \
"v_accvgpr_read_b32 v42, a86\n" \
"v_accvgpr_read_b32 v43, a87\n" \
"v_accvgpr_read_b32 v44, a116\n" \
"v_accvgpr_read_b32 v45, a117\n" \
"v_accvgpr_read_b32 v46, a118\n" \
"v_accvgpr_read_b32 v47, a119\n" \
"v_cvt_pk_bf16_f32 v40, v40, v41\n" \
"v_cvt_pk_bf16_f32 v41, v42, v43\n" \
"v_cvt_pk_bf16_f32 v42, v44, v45\n" \
"v_cvt_pk_bf16_f32 v43, v46, v47\n" \
"buffer_store_dwordx4 v[40:43], v141, s[60:63], s64 offen offset:1024 sc1\n" \
"v_accvgpr_read_b32 v48, a148\n" \
"v_accvgpr_read_b32 v49, a149\n" \
"v_accvgpr_read_b32 v50, a150\n" \
"v_accvgpr_read_b32 v51, a151\n" \
"v_accvgpr_read_b32 v52, a180\n" \
"v_accvgpr_read_b32 v53, a181\n" \
"v_accvgpr_read_b32 v54, a182\n" \
"v_accvgpr_read_b32 v55, a183\n" \
"v_cvt_pk_bf16_f32 v48, v48, v49\n" \
"v_cvt_pk_bf16_f32 v49, v50, v51\n" \
"v_cvt_pk_bf16_f32 v50, v52, v53\n" \
"v_cvt_pk_bf16_f32 v51, v54, v55\n" \
"buffer_store_dwordx4 v[48:51], v141, s[60:63], s64 offen offset:2048 sc1\n" \
"v_accvgpr_read_b32 v56, a212\n" \
"v_accvgpr_read_b32 v57, a213\n" \
"v_accvgpr_read_b32 v58, a214\n" \
"v_accvgpr_read_b32 v59, a215\n" \
"v_accvgpr_read_b32 v60, a244\n" \
"v_accvgpr_read_b32 v61, a245\n" \
"v_accvgpr_read_b32 v62, a246\n" \
"v_accvgpr_read_b32 v63, a247\n" \
"v_cvt_pk_bf16_f32 v56, v56, v57\n" \
"v_cvt_pk_bf16_f32 v57, v58, v59\n" \
"v_cvt_pk_bf16_f32 v58, v60, v61\n" \
"v_cvt_pk_bf16_f32 v59, v62, v63\n" \
"buffer_store_dwordx4 v[56:59], v141, s[60:63], s64 offen offset:3072 sc1\n" \
"s_add_u32 s64, s64, s65\n" \
"v_accvgpr_read_b32 v0, a24\n" \
"v_accvgpr_read_b32 v1, a25\n" \
"v_accvgpr_read_b32 v2, a26\n" \
"v_accvgpr_read_b32 v3, a27\n" \
"v_accvgpr_read_b32 v4, a56\n" \
"v_accvgpr_read_b32 v5, a57\n" \
"v_accvgpr_read_b32 v6, a58\n" \
"v_accvgpr_read_b32 v7, a59\n" \
"v_cvt_pk_bf16_f32 v0, v0, v1\n" \
"v_cvt_pk_bf16_f32 v1, v2, v3\n" \
"v_cvt_pk_bf16_f32 v2, v4, v5\n" \
"v_cvt_pk_bf16_f32 v3, v6, v7\n" \
"buffer_store_dwordx4 v[0:3], v141, s[60:63], s64 offen offset:0 sc1\n" \
"v_accvgpr_read_b32 v8, a88\n" \
"v_accvgpr_read_b32 v9, a89\n" \
"v_accvgpr_read_b32 v10, a90\n" \
"v_accvgpr_read_b32 v11, a91\n" \
"v_accvgpr_read_b32 v12, a120\n" \
"v_accvgpr_read_b32 v13, a121\n" \
"v_accvgpr_read_b32 v14, a122\n" \
"v_accvgpr_read_b32 v15, a123\n" \
"v_cvt_pk_bf16_f32 v8, v8, v9\n" \
"v_cvt_pk_bf16_f32 v9, v10, v11\n" \
"v_cvt_pk_bf16_f32 v10, v12, v13\n" \
"v_cvt_pk_bf16_f32 v11, v14, v15\n" \
"buffer_store_dwordx4 v[8:11], v141, s[60:63], s64 offen offset:1024 sc1\n" \
"v_accvgpr_read_b32 v16, a152\n" \
"v_accvgpr_read_b32 v17, a153\n" \
"v_accvgpr_read_b32 v18, a154\n" \
"v_accvgpr_read_b32 v19, a155\n" \
"v_accvgpr_read_b32 v20, a184\n" \
"v_accvgpr_read_b32 v21, a185\n" \
"v_accvgpr_read_b32 v22, a186\n" \
"v_accvgpr_read_b32 v23, a187\n" \
"v_cvt_pk_bf16_f32 v16, v16, v17\n" \
"v_cvt_pk_bf16_f32 v17, v18, v19\n" \
"v_cvt_pk_bf16_f32 v18, v20, v21\n" \
"v_cvt_pk_bf16_f32 v19, v22, v23\n" \
"buffer_store_dwordx4 v[16:19], v141, s[60:63], s64 offen offset:2048 sc1\n" \
"v_accvgpr_read_b32 v24, a216\n" \
"v_accvgpr_read_b32 v25, a217\n" \
"v_accvgpr_read_b32 v26, a218\n" \
"v_accvgpr_read_b32 v27, a219\n" \
"v_accvgpr_read_b32 v28, a248\n" \
"v_accvgpr_read_b32 v29, a249\n" \
"v_accvgpr_read_b32 v30, a250\n" \
"v_accvgpr_read_b32 v31, a251\n" \
"v_cvt_pk_bf16_f32 v24, v24, v25\n" \
"v_cvt_pk_bf16_f32 v25, v26, v27\n" \
"v_cvt_pk_bf16_f32 v26, v28, v29\n" \
"v_cvt_pk_bf16_f32 v27, v30, v31\n" \
"buffer_store_dwordx4 v[24:27], v141, s[60:63], s64 offen offset:3072 sc1\n" \
"s_add_u32 s64, s64, s65\n" \
"v_accvgpr_read_b32 v32, a28\n" \
"v_accvgpr_read_b32 v33, a29\n" \
"v_accvgpr_read_b32 v34, a30\n" \
"v_accvgpr_read_b32 v35, a31\n" \
"v_accvgpr_read_b32 v36, a60\n" \
"v_accvgpr_read_b32 v37, a61\n" \
"v_accvgpr_read_b32 v38, a62\n" \
"v_accvgpr_read_b32 v39, a63\n" \
"v_cvt_pk_bf16_f32 v32, v32, v33\n" \
"v_cvt_pk_bf16_f32 v33, v34, v35\n" \
"v_cvt_pk_bf16_f32 v34, v36, v37\n" \
"v_cvt_pk_bf16_f32 v35, v38, v39\n" \
"buffer_store_dwordx4 v[32:35], v141, s[60:63], s64 offen offset:0 sc1\n" \
"v_accvgpr_read_b32 v40, a92\n" \
"v_accvgpr_read_b32 v41, a93\n" \
"v_accvgpr_read_b32 v42, a94\n" \
"v_accvgpr_read_b32 v43, a95\n" \
"v_accvgpr_read_b32 v44, a124\n" \
"v_accvgpr_read_b32 v45, a125\n" \
"v_accvgpr_read_b32 v46, a126\n" \
"v_accvgpr_read_b32 v47, a127\n" \
"v_cvt_pk_bf16_f32 v40, v40, v41\n" \
"v_cvt_pk_bf16_f32 v41, v42, v43\n" \
"v_cvt_pk_bf16_f32 v42, v44, v45\n" \
"v_cvt_pk_bf16_f32 v43, v46, v47\n" \
"buffer_store_dwordx4 v[40:43], v141, s[60:63], s64 offen offset:1024 sc1\n" \
"v_accvgpr_read_b32 v48, a156\n" \
"v_accvgpr_read_b32 v49, a157\n" \
"v_accvgpr_read_b32 v50, a158\n" \
"v_accvgpr_read_b32 v51, a159\n" \
"v_accvgpr_read_b32 v52, a188\n" \
"v_accvgpr_read_b32 v53, a189\n" \
"v_accvgpr_read_b32 v54, a190\n" \
"v_accvgpr_read_b32 v55, a191\n" \
"v_cvt_pk_bf16_f32 v48, v48, v49\n" \
"v_cvt_pk_bf16_f32 v49, v50, v51\n" \
"v_cvt_pk_bf16_f32 v50, v52, v53\n" \
"v_cvt_pk_bf16_f32 v51, v54, v55\n" \
"buffer_store_dwordx4 v[48:51], v141, s[60:63], s64 offen offset:2048 sc1\n" \
"v_accvgpr_read_b32 v56, a220\n" \
"v_accvgpr_read_b32 v57, a221\n" \
"v_accvgpr_read_b32 v58, a222\n" \
"v_accvgpr_read_b32 v59, a223\n" \
"v_accvgpr_read_b32 v60, a252\n" \
"v_accvgpr_read_b32 v61, a253\n" \
"v_accvgpr_read_b32 v62, a254\n" \
"v_accvgpr_read_b32 v63, a255\n" \
"v_cvt_pk_bf16_f32 v56, v56, v57\n" \
"v_cvt_pk_bf16_f32 v57, v58, v59\n" \
"v_cvt_pk_bf16_f32 v58, v60, v61\n" \
"v_cvt_pk_bf16_f32 v59, v62, v63\n" \
"buffer_store_dwordx4 v[56:59], v141, s[60:63], s64 offen offset:3072 sc1\n" \
"s_add_u32 s64, s64, s65\n" \
"LG88_end_%=:\n" \
"s_nop 1\n"
#define ASM_G88S \
"s_add_u32 m0, s56, 0\n" \
"s_nop 0\n" \
"buffer_load_dwordx4 v128, s[40:43], s52 offen offset:0 sc1 lds\n" \
"buffer_load_dwordx4 v129, s[40:43], s52 offen offset:1024 sc1 lds\n" \
"buffer_load_dwordx4 v130, s[40:43], s52 offen offset:2048 sc1 lds\n" \
"buffer_load_dwordx4 v131, s[40:43], s52 offen offset:3072 sc1 lds\n" \
"s_add_u32 m0, s57, 0\n" \
"s_nop 0\n" \
"buffer_load_dwordx4 v132, s[44:47], s53 offen offset:0 lds\n" \
"buffer_load_dwordx4 v133, s[48:51], s53 offen offset:1024 lds\n" \
"buffer_load_dwordx4 v134, s[44:47], s53 offen offset:2048 lds\n" \
"buffer_load_dwordx4 v135, s[48:51], s53 offen offset:3072 lds\n" \
"s_add_u32 s52, s52, 1024\n" \
"s_add_u32 s53, s53, s54\n" \
"s_add_u32 m0, s56, 16384\n" \
"s_nop 0\n" \
"buffer_load_dwordx4 v128, s[40:43], s52 offen offset:0 sc1 lds\n" \
"buffer_load_dwordx4 v129, s[40:43], s52 offen offset:1024 sc1 lds\n" \
"buffer_load_dwordx4 v130, s[40:43], s52 offen offset:2048 sc1 lds\n" \
"buffer_load_dwordx4 v131, s[40:43], s52 offen offset:3072 sc1 lds\n" \
"s_add_u32 m0, s57, 16384\n" \
"s_nop 0\n" \
"buffer_load_dwordx4 v132, s[44:47], s53 offen offset:0 lds\n" \
"buffer_load_dwordx4 v133, s[48:51], s53 offen offset:1024 lds\n" \
"buffer_load_dwordx4 v134, s[44:47], s53 offen offset:2048 lds\n" \
"buffer_load_dwordx4 v135, s[48:51], s53 offen offset:3072 lds\n" \
"s_add_u32 s52, s52, 1024\n" \
"s_add_u32 s53, s53, s54\n" \
"s_add_u32 m0, s56, 32768\n" \
"s_nop 0\n" \
"buffer_load_dwordx4 v128, s[40:43], s52 offen offset:0 sc1 lds\n" \
"buffer_load_dwordx4 v129, s[40:43], s52 offen offset:1024 sc1 lds\n" \
"buffer_load_dwordx4 v130, s[40:43], s52 offen offset:2048 sc1 lds\n" \
"buffer_load_dwordx4 v131, s[40:43], s52 offen offset:3072 sc1 lds\n" \
"s_add_u32 m0, s57, 32768\n" \
"s_nop 0\n" \
"buffer_load_dwordx4 v132, s[44:47], s53 offen offset:0 lds\n" \
"buffer_load_dwordx4 v133, s[48:51], s53 offen offset:1024 lds\n" \
"buffer_load_dwordx4 v134, s[44:47], s53 offen offset:2048 lds\n" \
"buffer_load_dwordx4 v135, s[48:51], s53 offen offset:3072 lds\n" \
"s_add_u32 s52, s52, 1024\n" \
"s_add_u32 s53, s53, s54\n" \
"s_add_u32 m0, s56, 49152\n" \
"s_nop 0\n" \
"buffer_load_dwordx4 v128, s[40:43], s52 offen offset:0 sc1 lds\n" \
"buffer_load_dwordx4 v129, s[40:43], s52 offen offset:1024 sc1 lds\n" \
"buffer_load_dwordx4 v130, s[40:43], s52 offen offset:2048 sc1 lds\n" \
"buffer_load_dwordx4 v131, s[40:43], s52 offen offset:3072 sc1 lds\n" \
"s_add_u32 m0, s57, 49152\n" \
"s_nop 0\n" \
"buffer_load_dwordx4 v132, s[44:47], s53 offen offset:0 lds\n" \
"buffer_load_dwordx4 v133, s[48:51], s53 offen offset:1024 lds\n" \
"buffer_load_dwordx4 v134, s[44:47], s53 offen offset:2048 lds\n" \
"buffer_load_dwordx4 v135, s[48:51], s53 offen offset:3072 lds\n" \
"s_add_u32 s52, s52, 1024\n" \
"s_add_u32 s53, s53, s54\n" \
"v_accvgpr_write_b32 a0, 0\n" \
"v_accvgpr_write_b32 a1, 0\n" \
"v_accvgpr_write_b32 a2, 0\n" \
"v_accvgpr_write_b32 a3, 0\n" \
"v_accvgpr_write_b32 a4, 0\n" \
"v_accvgpr_write_b32 a5, 0\n" \
"v_accvgpr_write_b32 a6, 0\n" \
"v_accvgpr_write_b32 a7, 0\n" \
"v_accvgpr_write_b32 a8, 0\n" \
"v_accvgpr_write_b32 a9, 0\n" \
"v_accvgpr_write_b32 a10, 0\n" \
"v_accvgpr_write_b32 a11, 0\n" \
"v_accvgpr_write_b32 a12, 0\n" \
"v_accvgpr_write_b32 a13, 0\n" \
"v_accvgpr_write_b32 a14, 0\n" \
"v_accvgpr_write_b32 a15, 0\n" \
"v_accvgpr_write_b32 a16, 0\n" \
"v_accvgpr_write_b32 a17, 0\n" \
"v_accvgpr_write_b32 a18, 0\n" \
"v_accvgpr_write_b32 a19, 0\n" \
"v_accvgpr_write_b32 a20, 0\n" \
"v_accvgpr_write_b32 a21, 0\n" \
"v_accvgpr_write_b32 a22, 0\n" \
"v_accvgpr_write_b32 a23, 0\n" \
"v_accvgpr_write_b32 a24, 0\n" \
"v_accvgpr_write_b32 a25, 0\n" \
"v_accvgpr_write_b32 a26, 0\n" \
"v_accvgpr_write_b32 a27, 0\n" \
"v_accvgpr_write_b32 a28, 0\n" \
"v_accvgpr_write_b32 a29, 0\n" \
"v_accvgpr_write_b32 a30, 0\n" \
"v_accvgpr_write_b32 a31, 0\n" \
"v_accvgpr_write_b32 a32, 0\n" \
"v_accvgpr_write_b32 a33, 0\n" \
"v_accvgpr_write_b32 a34, 0\n" \
"v_accvgpr_write_b32 a35, 0\n" \
"v_accvgpr_write_b32 a36, 0\n" \
"v_accvgpr_write_b32 a37, 0\n" \
"v_accvgpr_write_b32 a38, 0\n" \
"v_accvgpr_write_b32 a39, 0\n" \
"v_accvgpr_write_b32 a40, 0\n" \
"v_accvgpr_write_b32 a41, 0\n" \
"v_accvgpr_write_b32 a42, 0\n" \
"v_accvgpr_write_b32 a43, 0\n" \
"v_accvgpr_write_b32 a44, 0\n" \
"v_accvgpr_write_b32 a45, 0\n" \
"v_accvgpr_write_b32 a46, 0\n" \
"v_accvgpr_write_b32 a47, 0\n" \
"v_accvgpr_write_b32 a48, 0\n" \
"v_accvgpr_write_b32 a49, 0\n" \
"v_accvgpr_write_b32 a50, 0\n" \
"v_accvgpr_write_b32 a51, 0\n" \
"v_accvgpr_write_b32 a52, 0\n" \
"v_accvgpr_write_b32 a53, 0\n" \
"v_accvgpr_write_b32 a54, 0\n" \
"v_accvgpr_write_b32 a55, 0\n" \
"v_accvgpr_write_b32 a56, 0\n" \
"v_accvgpr_write_b32 a57, 0\n" \
"v_accvgpr_write_b32 a58, 0\n" \
"v_accvgpr_write_b32 a59, 0\n" \
"v_accvgpr_write_b32 a60, 0\n" \
"v_accvgpr_write_b32 a61, 0\n" \
"v_accvgpr_write_b32 a62, 0\n" \
"v_accvgpr_write_b32 a63, 0\n" \
"v_accvgpr_write_b32 a64, 0\n" \
"v_accvgpr_write_b32 a65, 0\n" \
"v_accvgpr_write_b32 a66, 0\n" \
"v_accvgpr_write_b32 a67, 0\n" \
"v_accvgpr_write_b32 a68, 0\n" \
"v_accvgpr_write_b32 a69, 0\n" \
"v_accvgpr_write_b32 a70, 0\n" \
"v_accvgpr_write_b32 a71, 0\n" \
"v_accvgpr_write_b32 a72, 0\n" \
"v_accvgpr_write_b32 a73, 0\n" \
"v_accvgpr_write_b32 a74, 0\n" \
"v_accvgpr_write_b32 a75, 0\n" \
"v_accvgpr_write_b32 a76, 0\n" \
"v_accvgpr_write_b32 a77, 0\n" \
"v_accvgpr_write_b32 a78, 0\n" \
"v_accvgpr_write_b32 a79, 0\n" \
"v_accvgpr_write_b32 a80, 0\n" \
"v_accvgpr_write_b32 a81, 0\n" \
"v_accvgpr_write_b32 a82, 0\n" \
"v_accvgpr_write_b32 a83, 0\n" \
"v_accvgpr_write_b32 a84, 0\n" \
"v_accvgpr_write_b32 a85, 0\n" \
"v_accvgpr_write_b32 a86, 0\n" \
"v_accvgpr_write_b32 a87, 0\n" \
"v_accvgpr_write_b32 a88, 0\n" \
"v_accvgpr_write_b32 a89, 0\n" \
"v_accvgpr_write_b32 a90, 0\n" \
"v_accvgpr_write_b32 a91, 0\n" \
"v_accvgpr_write_b32 a92, 0\n" \
"v_accvgpr_write_b32 a93, 0\n" \
"v_accvgpr_write_b32 a94, 0\n" \
"v_accvgpr_write_b32 a95, 0\n" \
"v_accvgpr_write_b32 a96, 0\n" \
"v_accvgpr_write_b32 a97, 0\n" \
"v_accvgpr_write_b32 a98, 0\n" \
"v_accvgpr_write_b32 a99, 0\n" \
"v_accvgpr_write_b32 a100, 0\n" \
"v_accvgpr_write_b32 a101, 0\n" \
"v_accvgpr_write_b32 a102, 0\n" \
"v_accvgpr_write_b32 a103, 0\n" \
"v_accvgpr_write_b32 a104, 0\n" \
"v_accvgpr_write_b32 a105, 0\n" \
"v_accvgpr_write_b32 a106, 0\n" \
"v_accvgpr_write_b32 a107, 0\n" \
"v_accvgpr_write_b32 a108, 0\n" \
"v_accvgpr_write_b32 a109, 0\n" \
"v_accvgpr_write_b32 a110, 0\n" \
"v_accvgpr_write_b32 a111, 0\n" \
"v_accvgpr_write_b32 a112, 0\n" \
"v_accvgpr_write_b32 a113, 0\n" \
"v_accvgpr_write_b32 a114, 0\n" \
"v_accvgpr_write_b32 a115, 0\n" \
"v_accvgpr_write_b32 a116, 0\n" \
"v_accvgpr_write_b32 a117, 0\n" \
"v_accvgpr_write_b32 a118, 0\n" \
"v_accvgpr_write_b32 a119, 0\n" \
"v_accvgpr_write_b32 a120, 0\n" \
"v_accvgpr_write_b32 a121, 0\n" \
"v_accvgpr_write_b32 a122, 0\n" \
"v_accvgpr_write_b32 a123, 0\n" \
"v_accvgpr_write_b32 a124, 0\n" \
"v_accvgpr_write_b32 a125, 0\n" \
"v_accvgpr_write_b32 a126, 0\n" \
"v_accvgpr_write_b32 a127, 0\n" \
"v_accvgpr_write_b32 a128, 0\n" \
"v_accvgpr_write_b32 a129, 0\n" \
"v_accvgpr_write_b32 a130, 0\n" \
"v_accvgpr_write_b32 a131, 0\n" \
"v_accvgpr_write_b32 a132, 0\n" \
"v_accvgpr_write_b32 a133, 0\n" \
"v_accvgpr_write_b32 a134, 0\n" \
"v_accvgpr_write_b32 a135, 0\n" \
"v_accvgpr_write_b32 a136, 0\n" \
"v_accvgpr_write_b32 a137, 0\n" \
"v_accvgpr_write_b32 a138, 0\n" \
"v_accvgpr_write_b32 a139, 0\n" \
"v_accvgpr_write_b32 a140, 0\n" \
"v_accvgpr_write_b32 a141, 0\n" \
"v_accvgpr_write_b32 a142, 0\n" \
"v_accvgpr_write_b32 a143, 0\n" \
"v_accvgpr_write_b32 a144, 0\n" \
"v_accvgpr_write_b32 a145, 0\n" \
"v_accvgpr_write_b32 a146, 0\n" \
"v_accvgpr_write_b32 a147, 0\n" \
"v_accvgpr_write_b32 a148, 0\n" \
"v_accvgpr_write_b32 a149, 0\n" \
"v_accvgpr_write_b32 a150, 0\n" \
"v_accvgpr_write_b32 a151, 0\n" \
"v_accvgpr_write_b32 a152, 0\n" \
"v_accvgpr_write_b32 a153, 0\n" \
"v_accvgpr_write_b32 a154, 0\n" \
"v_accvgpr_write_b32 a155, 0\n" \
"v_accvgpr_write_b32 a156, 0\n" \
"v_accvgpr_write_b32 a157, 0\n" \
"v_accvgpr_write_b32 a158, 0\n" \
"v_accvgpr_write_b32 a159, 0\n" \
"v_accvgpr_write_b32 a160, 0\n" \
"v_accvgpr_write_b32 a161, 0\n" \
"v_accvgpr_write_b32 a162, 0\n" \
"v_accvgpr_write_b32 a163, 0\n" \
"v_accvgpr_write_b32 a164, 0\n" \
"v_accvgpr_write_b32 a165, 0\n" \
"v_accvgpr_write_b32 a166, 0\n" \
"v_accvgpr_write_b32 a167, 0\n" \
"v_accvgpr_write_b32 a168, 0\n" \
"v_accvgpr_write_b32 a169, 0\n" \
"v_accvgpr_write_b32 a170, 0\n" \
"v_accvgpr_write_b32 a171, 0\n" \
"v_accvgpr_write_b32 a172, 0\n" \
"v_accvgpr_write_b32 a173, 0\n" \
"v_accvgpr_write_b32 a174, 0\n" \
"v_accvgpr_write_b32 a175, 0\n" \
"v_accvgpr_write_b32 a176, 0\n" \
"v_accvgpr_write_b32 a177, 0\n" \
"v_accvgpr_write_b32 a178, 0\n" \
"v_accvgpr_write_b32 a179, 0\n" \
"v_accvgpr_write_b32 a180, 0\n" \
"v_accvgpr_write_b32 a181, 0\n" \
"v_accvgpr_write_b32 a182, 0\n" \
"v_accvgpr_write_b32 a183, 0\n" \
"v_accvgpr_write_b32 a184, 0\n" \
"v_accvgpr_write_b32 a185, 0\n" \
"v_accvgpr_write_b32 a186, 0\n" \
"v_accvgpr_write_b32 a187, 0\n" \
"v_accvgpr_write_b32 a188, 0\n" \
"v_accvgpr_write_b32 a189, 0\n" \
"v_accvgpr_write_b32 a190, 0\n" \
"v_accvgpr_write_b32 a191, 0\n" \
"v_accvgpr_write_b32 a192, 0\n" \
"v_accvgpr_write_b32 a193, 0\n" \
"v_accvgpr_write_b32 a194, 0\n" \
"v_accvgpr_write_b32 a195, 0\n" \
"v_accvgpr_write_b32 a196, 0\n" \
"v_accvgpr_write_b32 a197, 0\n" \
"v_accvgpr_write_b32 a198, 0\n" \
"v_accvgpr_write_b32 a199, 0\n" \
"v_accvgpr_write_b32 a200, 0\n" \
"v_accvgpr_write_b32 a201, 0\n" \
"v_accvgpr_write_b32 a202, 0\n" \
"v_accvgpr_write_b32 a203, 0\n" \
"v_accvgpr_write_b32 a204, 0\n" \
"v_accvgpr_write_b32 a205, 0\n" \
"v_accvgpr_write_b32 a206, 0\n" \
"v_accvgpr_write_b32 a207, 0\n" \
"v_accvgpr_write_b32 a208, 0\n" \
"v_accvgpr_write_b32 a209, 0\n" \
"v_accvgpr_write_b32 a210, 0\n" \
"v_accvgpr_write_b32 a211, 0\n" \
"v_accvgpr_write_b32 a212, 0\n" \
"v_accvgpr_write_b32 a213, 0\n" \
"v_accvgpr_write_b32 a214, 0\n" \
"v_accvgpr_write_b32 a215, 0\n" \
"v_accvgpr_write_b32 a216, 0\n" \
"v_accvgpr_write_b32 a217, 0\n" \
"v_accvgpr_write_b32 a218, 0\n" \
"v_accvgpr_write_b32 a219, 0\n" \
"v_accvgpr_write_b32 a220, 0\n" \
"v_accvgpr_write_b32 a221, 0\n" \
"v_accvgpr_write_b32 a222, 0\n" \
"v_accvgpr_write_b32 a223, 0\n" \
"v_accvgpr_write_b32 a224, 0\n" \
"v_accvgpr_write_b32 a225, 0\n" \
"v_accvgpr_write_b32 a226, 0\n" \
"v_accvgpr_write_b32 a227, 0\n" \
"v_accvgpr_write_b32 a228, 0\n" \
"v_accvgpr_write_b32 a229, 0\n" \
"v_accvgpr_write_b32 a230, 0\n" \
"v_accvgpr_write_b32 a231, 0\n" \
"v_accvgpr_write_b32 a232, 0\n" \
"v_accvgpr_write_b32 a233, 0\n" \
"v_accvgpr_write_b32 a234, 0\n" \
"v_accvgpr_write_b32 a235, 0\n" \
"v_accvgpr_write_b32 a236, 0\n" \
"v_accvgpr_write_b32 a237, 0\n" \
"v_accvgpr_write_b32 a238, 0\n" \
"v_accvgpr_write_b32 a239, 0\n" \
"v_accvgpr_write_b32 a240, 0\n" \
"v_accvgpr_write_b32 a241, 0\n" \
"v_accvgpr_write_b32 a242, 0\n" \
"v_accvgpr_write_b32 a243, 0\n" \
"v_accvgpr_write_b32 a244, 0\n" \
"v_accvgpr_write_b32 a245, 0\n" \
"v_accvgpr_write_b32 a246, 0\n" \
"v_accvgpr_write_b32 a247, 0\n" \
"v_accvgpr_write_b32 a248, 0\n" \
"v_accvgpr_write_b32 a249, 0\n" \
"v_accvgpr_write_b32 a250, 0\n" \
"v_accvgpr_write_b32 a251, 0\n" \
"v_accvgpr_write_b32 a252, 0\n" \
"v_accvgpr_write_b32 a253, 0\n" \
"v_accvgpr_write_b32 a254, 0\n" \
"v_accvgpr_write_b32 a255, 0\n" \
"s_waitcnt vmcnt(24)\n" \
"s_barrier\n" \
"ds_read_b64_tr_b16 v[32:33], v137 offset:0\n" \
"ds_read_b64_tr_b16 v[34:35], v137 offset:8192\n" \
"ds_read_b128 v[0:3], v136 offset:0\n" \
"ds_read_b128 v[4:7], v136 offset:1024\n" \
"ds_read_b128 v[8:11], v136 offset:2048\n" \
"ds_read_b128 v[12:15], v136 offset:3072\n" \
"ds_read_b128 v[16:19], v136 offset:4096\n" \
"ds_read_b128 v[20:23], v136 offset:5120\n" \
"ds_read_b128 v[24:27], v136 offset:6144\n" \
"ds_read_b128 v[28:31], v136 offset:7168\n" \
"ds_read_b64_tr_b16 v[36:37], v138 offset:0\n" \
"ds_read_b64_tr_b16 v[38:39], v138 offset:8192\n" \
"ds_read_b64_tr_b16 v[40:41], v139 offset:0\n" \
"ds_read_b64_tr_b16 v[42:43], v139 offset:8192\n" \
"ds_read_b64_tr_b16 v[44:45], v140 offset:0\n" \
"ds_read_b64_tr_b16 v[46:47], v140 offset:8192\n" \
"ds_read_b64_tr_b16 v[48:49], v137 offset:1024\n" \
"ds_read_b64_tr_b16 v[50:51], v137 offset:9216\n" \
"ds_read_b64_tr_b16 v[52:53], v138 offset:1024\n" \
"ds_read_b64_tr_b16 v[54:55], v138 offset:9216\n" \
"ds_read_b64_tr_b16 v[56:57], v139 offset:1024\n" \
"ds_read_b64_tr_b16 v[58:59], v139 offset:9216\n" \
"ds_read_b64_tr_b16 v[60:61], v140 offset:1024\n" \
"ds_read_b64_tr_b16 v[62:63], v140 offset:9216\n" \
"s_cmp_eq_u32 s55, 0\n" \
"s_cbranch_scc1 LG88S_tail_%=\n" \
"LG88S_loop_%=:\n" \
"s_waitcnt vmcnt(16) lgkmcnt(0)\n" \
"s_barrier\n" \
"v_mfma_f32_16x16x32_bf16 a[0:3], v[0:3], v[32:35], a[0:3]\n" \
"ds_read_b64_tr_b16 v[96:97], v137 offset:16384\n" \
"s_add_u32 m0, s56, 0\n" \
"v_mfma_f32_16x16x32_bf16 a[4:7], v[4:7], v[32:35], a[4:7]\n" \
"buffer_load_dwordx4 v128, s[40:43], s52 offen offset:0 sc1 lds\n" \
"v_mfma_f32_16x16x32_bf16 a[8:11], v[8:11], v[32:35], a[8:11]\n" \
"ds_read_b64_tr_b16 v[98:99], v137 offset:24576\n" \
"v_mfma_f32_16x16x32_bf16 a[12:15], v[12:15], v[32:35], a[12:15]\n" \
"v_mfma_f32_16x16x32_bf16 a[16:19], v[16:19], v[32:35], a[16:19]\n" \
"ds_read_b128 v[64:67], v136 offset:16384\n" \
"v_mfma_f32_16x16x32_bf16 a[20:23], v[20:23], v[32:35], a[20:23]\n" \
"v_mfma_f32_16x16x32_bf16 a[24:27], v[24:27], v[32:35], a[24:27]\n" \
"ds_read_b128 v[68:71], v136 offset:17408\n" \
"v_mfma_f32_16x16x32_bf16 a[28:31], v[28:31], v[32:35], a[28:31]\n" \
"v_mfma_f32_16x16x32_bf16 a[32:35], v[0:3], v[36:39], a[32:35]\n" \
"ds_read_b128 v[72:75], v136 offset:18432\n" \
"v_mfma_f32_16x16x32_bf16 a[36:39], v[4:7], v[36:39], a[36:39]\n" \
"buffer_load_dwordx4 v129, s[40:43], s52 offen offset:1024 sc1 lds\n" \
"v_mfma_f32_16x16x32_bf16 a[40:43], v[8:11], v[36:39], a[40:43]\n" \
"ds_read_b128 v[76:79], v136 offset:19456\n" \
"v_mfma_f32_16x16x32_bf16 a[44:47], v[12:15], v[36:39], a[44:47]\n" \
"v_mfma_f32_16x16x32_bf16 a[48:51], v[16:19], v[36:39], a[48:51]\n" \
"ds_read_b128 v[80:83], v136 offset:20480\n" \
"v_mfma_f32_16x16x32_bf16 a[52:55], v[20:23], v[36:39], a[52:55]\n" \
"v_mfma_f32_16x16x32_bf16 a[56:59], v[24:27], v[36:39], a[56:59]\n" \
"ds_read_b128 v[84:87], v136 offset:21504\n" \
"v_mfma_f32_16x16x32_bf16 a[60:63], v[28:31], v[36:39], a[60:63]\n" \
"v_mfma_f32_16x16x32_bf16 a[64:67], v[0:3], v[40:43], a[64:67]\n" \
"ds_read_b128 v[88:91], v136 offset:22528\n" \
"v_mfma_f32_16x16x32_bf16 a[68:71], v[4:7], v[40:43], a[68:71]\n" \
"buffer_load_dwordx4 v130, s[40:43], s52 offen offset:2048 sc1 lds\n" \
"v_mfma_f32_16x16x32_bf16 a[72:75], v[8:11], v[40:43], a[72:75]\n" \
"ds_read_b128 v[92:95], v136 offset:23552\n" \
"v_mfma_f32_16x16x32_bf16 a[76:79], v[12:15], v[40:43], a[76:79]\n" \
"v_mfma_f32_16x16x32_bf16 a[80:83], v[16:19], v[40:43], a[80:83]\n" \
"ds_read_b64_tr_b16 v[100:101], v138 offset:16384\n" \
"v_mfma_f32_16x16x32_bf16 a[84:87], v[20:23], v[40:43], a[84:87]\n" \
"v_mfma_f32_16x16x32_bf16 a[88:91], v[24:27], v[40:43], a[88:91]\n" \
"ds_read_b64_tr_b16 v[102:103], v138 offset:24576\n" \
"v_mfma_f32_16x16x32_bf16 a[92:95], v[28:31], v[40:43], a[92:95]\n" \
"v_mfma_f32_16x16x32_bf16 a[96:99], v[0:3], v[44:47], a[96:99]\n" \
"ds_read_b64_tr_b16 v[104:105], v139 offset:16384\n" \
"v_mfma_f32_16x16x32_bf16 a[100:103], v[4:7], v[44:47], a[100:103]\n" \
"buffer_load_dwordx4 v131, s[40:43], s52 offen offset:3072 sc1 lds\n" \
"v_mfma_f32_16x16x32_bf16 a[104:107], v[8:11], v[44:47], a[104:107]\n" \
"ds_read_b64_tr_b16 v[106:107], v139 offset:24576\n" \
"v_mfma_f32_16x16x32_bf16 a[108:111], v[12:15], v[44:47], a[108:111]\n" \
"v_mfma_f32_16x16x32_bf16 a[112:115], v[16:19], v[44:47], a[112:115]\n" \
"ds_read_b64_tr_b16 v[108:109], v140 offset:16384\n" \
"v_mfma_f32_16x16x32_bf16 a[116:119], v[20:23], v[44:47], a[116:119]\n" \
"v_mfma_f32_16x16x32_bf16 a[120:123], v[24:27], v[44:47], a[120:123]\n" \
"ds_read_b64_tr_b16 v[110:111], v140 offset:24576\n" \
"v_mfma_f32_16x16x32_bf16 a[124:127], v[28:31], v[44:47], a[124:127]\n" \
"v_mfma_f32_16x16x32_bf16 a[128:131], v[0:3], v[48:51], a[128:131]\n" \
"ds_read_b64_tr_b16 v[112:113], v137 offset:17408\n" \
"s_add_u32 m0, s57, 0\n" \
"v_mfma_f32_16x16x32_bf16 a[132:135], v[4:7], v[48:51], a[132:135]\n" \
"buffer_load_dwordx4 v132, s[44:47], s53 offen offset:0 lds\n" \
"v_mfma_f32_16x16x32_bf16 a[136:139], v[8:11], v[48:51], a[136:139]\n" \
"ds_read_b64_tr_b16 v[114:115], v137 offset:25600\n" \
"v_mfma_f32_16x16x32_bf16 a[140:143], v[12:15], v[48:51], a[140:143]\n" \
"v_mfma_f32_16x16x32_bf16 a[144:147], v[16:19], v[48:51], a[144:147]\n" \
"ds_read_b64_tr_b16 v[116:117], v138 offset:17408\n" \
"v_mfma_f32_16x16x32_bf16 a[148:151], v[20:23], v[48:51], a[148:151]\n" \
"v_mfma_f32_16x16x32_bf16 a[152:155], v[24:27], v[48:51], a[152:155]\n" \
"ds_read_b64_tr_b16 v[118:119], v138 offset:25600\n" \
"v_mfma_f32_16x16x32_bf16 a[156:159], v[28:31], v[48:51], a[156:159]\n" \
"v_mfma_f32_16x16x32_bf16 a[160:163], v[0:3], v[52:55], a[160:163]\n" \
"ds_read_b64_tr_b16 v[120:121], v139 offset:17408\n" \
"v_mfma_f32_16x16x32_bf16 a[164:167], v[4:7], v[52:55], a[164:167]\n" \
"buffer_load_dwordx4 v133, s[48:51], s53 offen offset:1024 lds\n" \
"v_mfma_f32_16x16x32_bf16 a[168:171], v[8:11], v[52:55], a[168:171]\n" \
"ds_read_b64_tr_b16 v[122:123], v139 offset:25600\n" \
"v_mfma_f32_16x16x32_bf16 a[172:175], v[12:15], v[52:55], a[172:175]\n" \
"v_mfma_f32_16x16x32_bf16 a[176:179], v[16:19], v[52:55], a[176:179]\n" \
"ds_read_b64_tr_b16 v[124:125], v140 offset:17408\n" \
"v_mfma_f32_16x16x32_bf16 a[180:183], v[20:23], v[52:55], a[180:183]\n" \
"v_mfma_f32_16x16x32_bf16 a[184:187], v[24:27], v[52:55], a[184:187]\n" \
"ds_read_b64_tr_b16 v[126:127], v140 offset:25600\n" \
"v_mfma_f32_16x16x32_bf16 a[188:191], v[28:31], v[52:55], a[188:191]\n" \
"v_mfma_f32_16x16x32_bf16 a[192:195], v[0:3], v[56:59], a[192:195]\n" \
"v_mfma_f32_16x16x32_bf16 a[196:199], v[4:7], v[56:59], a[196:199]\n" \
"buffer_load_dwordx4 v134, s[44:47], s53 offen offset:2048 lds\n" \
"v_mfma_f32_16x16x32_bf16 a[200:203], v[8:11], v[56:59], a[200:203]\n" \
"v_mfma_f32_16x16x32_bf16 a[204:207], v[12:15], v[56:59], a[204:207]\n" \
"v_mfma_f32_16x16x32_bf16 a[208:211], v[16:19], v[56:59], a[208:211]\n" \
"v_mfma_f32_16x16x32_bf16 a[212:215], v[20:23], v[56:59], a[212:215]\n" \
"v_mfma_f32_16x16x32_bf16 a[216:219], v[24:27], v[56:59], a[216:219]\n" \
"v_mfma_f32_16x16x32_bf16 a[220:223], v[28:31], v[56:59], a[220:223]\n" \
"v_mfma_f32_16x16x32_bf16 a[224:227], v[0:3], v[60:63], a[224:227]\n" \
"v_mfma_f32_16x16x32_bf16 a[228:231], v[4:7], v[60:63], a[228:231]\n" \
"buffer_load_dwordx4 v135, s[48:51], s53 offen offset:3072 lds\n" \
"v_mfma_f32_16x16x32_bf16 a[232:235], v[8:11], v[60:63], a[232:235]\n" \
"v_mfma_f32_16x16x32_bf16 a[236:239], v[12:15], v[60:63], a[236:239]\n" \
"v_mfma_f32_16x16x32_bf16 a[240:243], v[16:19], v[60:63], a[240:243]\n" \
"v_mfma_f32_16x16x32_bf16 a[244:247], v[20:23], v[60:63], a[244:247]\n" \
"v_mfma_f32_16x16x32_bf16 a[248:251], v[24:27], v[60:63], a[248:251]\n" \
"v_mfma_f32_16x16x32_bf16 a[252:255], v[28:31], v[60:63], a[252:255]\n" \
"s_add_u32 s52, s52, 1024\n" \
"s_add_u32 s53, s53, s54\n" \
"s_waitcnt vmcnt(16) lgkmcnt(0)\n" \
"s_barrier\n" \
"v_mfma_f32_16x16x32_bf16 a[0:3], v[64:67], v[96:99], a[0:3]\n" \
"ds_read_b64_tr_b16 v[32:33], v137 offset:32768\n" \
"s_add_u32 m0, s56, 16384\n" \
"v_mfma_f32_16x16x32_bf16 a[4:7], v[68:71], v[96:99], a[4:7]\n" \
"buffer_load_dwordx4 v128, s[40:43], s52 offen offset:0 sc1 lds\n" \
"v_mfma_f32_16x16x32_bf16 a[8:11], v[72:75], v[96:99], a[8:11]\n" \
"ds_read_b64_tr_b16 v[34:35], v137 offset:40960\n" \
"v_mfma_f32_16x16x32_bf16 a[12:15], v[76:79], v[96:99], a[12:15]\n" \
"v_mfma_f32_16x16x32_bf16 a[16:19], v[80:83], v[96:99], a[16:19]\n" \
"ds_read_b128 v[0:3], v136 offset:32768\n" \
"v_mfma_f32_16x16x32_bf16 a[20:23], v[84:87], v[96:99], a[20:23]\n" \
"v_mfma_f32_16x16x32_bf16 a[24:27], v[88:91], v[96:99], a[24:27]\n" \
"ds_read_b128 v[4:7], v136 offset:33792\n" \
"v_mfma_f32_16x16x32_bf16 a[28:31], v[92:95], v[96:99], a[28:31]\n" \
"v_mfma_f32_16x16x32_bf16 a[32:35], v[64:67], v[100:103], a[32:35]\n" \
"ds_read_b128 v[8:11], v136 offset:34816\n" \
"v_mfma_f32_16x16x32_bf16 a[36:39], v[68:71], v[100:103], a[36:39]\n" \
"buffer_load_dwordx4 v129, s[40:43], s52 offen offset:1024 sc1 lds\n" \
"v_mfma_f32_16x16x32_bf16 a[40:43], v[72:75], v[100:103], a[40:43]\n" \
"ds_read_b128 v[12:15], v136 offset:35840\n" \
"v_mfma_f32_16x16x32_bf16 a[44:47], v[76:79], v[100:103], a[44:47]\n" \
"v_mfma_f32_16x16x32_bf16 a[48:51], v[80:83], v[100:103], a[48:51]\n" \
"ds_read_b128 v[16:19], v136 offset:36864\n" \
"v_mfma_f32_16x16x32_bf16 a[52:55], v[84:87], v[100:103], a[52:55]\n" \
"v_mfma_f32_16x16x32_bf16 a[56:59], v[88:91], v[100:103], a[56:59]\n" \
"ds_read_b128 v[20:23], v136 offset:37888\n" \
"v_mfma_f32_16x16x32_bf16 a[60:63], v[92:95], v[100:103], a[60:63]\n" \
"v_mfma_f32_16x16x32_bf16 a[64:67], v[64:67], v[104:107], a[64:67]\n" \
"ds_read_b128 v[24:27], v136 offset:38912\n" \
"v_mfma_f32_16x16x32_bf16 a[68:71], v[68:71], v[104:107], a[68:71]\n" \
"buffer_load_dwordx4 v130, s[40:43], s52 offen offset:2048 sc1 lds\n" \
"v_mfma_f32_16x16x32_bf16 a[72:75], v[72:75], v[104:107], a[72:75]\n" \
"ds_read_b128 v[28:31], v136 offset:39936\n" \
"v_mfma_f32_16x16x32_bf16 a[76:79], v[76:79], v[104:107], a[76:79]\n" \
"v_mfma_f32_16x16x32_bf16 a[80:83], v[80:83], v[104:107], a[80:83]\n" \
"ds_read_b64_tr_b16 v[36:37], v138 offset:32768\n" \
"v_mfma_f32_16x16x32_bf16 a[84:87], v[84:87], v[104:107], a[84:87]\n" \
"v_mfma_f32_16x16x32_bf16 a[88:91], v[88:91], v[104:107], a[88:91]\n" \
"ds_read_b64_tr_b16 v[38:39], v138 offset:40960\n" \
"v_mfma_f32_16x16x32_bf16 a[92:95], v[92:95], v[104:107], a[92:95]\n" \
"v_mfma_f32_16x16x32_bf16 a[96:99], v[64:67], v[108:111], a[96:99]\n" \
"ds_read_b64_tr_b16 v[40:41], v139 offset:32768\n" \
"v_mfma_f32_16x16x32_bf16 a[100:103], v[68:71], v[108:111], a[100:103]\n" \
"buffer_load_dwordx4 v131, s[40:43], s52 offen offset:3072 sc1 lds\n" \
"v_mfma_f32_16x16x32_bf16 a[104:107], v[72:75], v[108:111], a[104:107]\n" \
"ds_read_b64_tr_b16 v[42:43], v139 offset:40960\n" \
"v_mfma_f32_16x16x32_bf16 a[108:111], v[76:79], v[108:111], a[108:111]\n" \
"v_mfma_f32_16x16x32_bf16 a[112:115], v[80:83], v[108:111], a[112:115]\n" \
"ds_read_b64_tr_b16 v[44:45], v140 offset:32768\n" \
"v_mfma_f32_16x16x32_bf16 a[116:119], v[84:87], v[108:111], a[116:119]\n" \
"v_mfma_f32_16x16x32_bf16 a[120:123], v[88:91], v[108:111], a[120:123]\n" \
"ds_read_b64_tr_b16 v[46:47], v140 offset:40960\n" \
"v_mfma_f32_16x16x32_bf16 a[124:127], v[92:95], v[108:111], a[124:127]\n" \
"v_mfma_f32_16x16x32_bf16 a[128:131], v[64:67], v[112:115], a[128:131]\n" \
"ds_read_b64_tr_b16 v[48:49], v137 offset:33792\n" \
"s_add_u32 m0, s57, 16384\n" \
"v_mfma_f32_16x16x32_bf16 a[132:135], v[68:71], v[112:115], a[132:135]\n" \
"buffer_load_dwordx4 v132, s[44:47], s53 offen offset:0 lds\n" \
"v_mfma_f32_16x16x32_bf16 a[136:139], v[72:75], v[112:115], a[136:139]\n" \
"ds_read_b64_tr_b16 v[50:51], v137 offset:41984\n" \
"v_mfma_f32_16x16x32_bf16 a[140:143], v[76:79], v[112:115], a[140:143]\n" \
"v_mfma_f32_16x16x32_bf16 a[144:147], v[80:83], v[112:115], a[144:147]\n" \
"ds_read_b64_tr_b16 v[52:53], v138 offset:33792\n" \
"v_mfma_f32_16x16x32_bf16 a[148:151], v[84:87], v[112:115], a[148:151]\n" \
"v_mfma_f32_16x16x32_bf16 a[152:155], v[88:91], v[112:115], a[152:155]\n" \
"ds_read_b64_tr_b16 v[54:55], v138 offset:41984\n" \
"v_mfma_f32_16x16x32_bf16 a[156:159], v[92:95], v[112:115], a[156:159]\n" \
"v_mfma_f32_16x16x32_bf16 a[160:163], v[64:67], v[116:119], a[160:163]\n" \
"ds_read_b64_tr_b16 v[56:57], v139 offset:33792\n" \
"v_mfma_f32_16x16x32_bf16 a[164:167], v[68:71], v[116:119], a[164:167]\n" \
"buffer_load_dwordx4 v133, s[48:51], s53 offen offset:1024 lds\n" \
"v_mfma_f32_16x16x32_bf16 a[168:171], v[72:75], v[116:119], a[168:171]\n" \
"ds_read_b64_tr_b16 v[58:59], v139 offset:41984\n" \
"v_mfma_f32_16x16x32_bf16 a[172:175], v[76:79], v[116:119], a[172:175]\n" \
"v_mfma_f32_16x16x32_bf16 a[176:179], v[80:83], v[116:119], a[176:179]\n" \
"ds_read_b64_tr_b16 v[60:61], v140 offset:33792\n" \
"v_mfma_f32_16x16x32_bf16 a[180:183], v[84:87], v[116:119], a[180:183]\n" \
"v_mfma_f32_16x16x32_bf16 a[184:187], v[88:91], v[116:119], a[184:187]\n" \
"ds_read_b64_tr_b16 v[62:63], v140 offset:41984\n" \
"v_mfma_f32_16x16x32_bf16 a[188:191], v[92:95], v[116:119], a[188:191]\n" \
"v_mfma_f32_16x16x32_bf16 a[192:195], v[64:67], v[120:123], a[192:195]\n" \
"v_mfma_f32_16x16x32_bf16 a[196:199], v[68:71], v[120:123], a[196:199]\n" \
"buffer_load_dwordx4 v134, s[44:47], s53 offen offset:2048 lds\n" \
"v_mfma_f32_16x16x32_bf16 a[200:203], v[72:75], v[120:123], a[200:203]\n" \
"v_mfma_f32_16x16x32_bf16 a[204:207], v[76:79], v[120:123], a[204:207]\n" \
"v_mfma_f32_16x16x32_bf16 a[208:211], v[80:83], v[120:123], a[208:211]\n" \
"v_mfma_f32_16x16x32_bf16 a[212:215], v[84:87], v[120:123], a[212:215]\n" \
"v_mfma_f32_16x16x32_bf16 a[216:219], v[88:91], v[120:123], a[216:219]\n" \
"v_mfma_f32_16x16x32_bf16 a[220:223], v[92:95], v[120:123], a[220:223]\n" \
"v_mfma_f32_16x16x32_bf16 a[224:227], v[64:67], v[124:127], a[224:227]\n" \
"v_mfma_f32_16x16x32_bf16 a[228:231], v[68:71], v[124:127], a[228:231]\n" \
"buffer_load_dwordx4 v135, s[48:51], s53 offen offset:3072 lds\n" \
"v_mfma_f32_16x16x32_bf16 a[232:235], v[72:75], v[124:127], a[232:235]\n" \
"v_mfma_f32_16x16x32_bf16 a[236:239], v[76:79], v[124:127], a[236:239]\n" \
"v_mfma_f32_16x16x32_bf16 a[240:243], v[80:83], v[124:127], a[240:243]\n" \
"v_mfma_f32_16x16x32_bf16 a[244:247], v[84:87], v[124:127], a[244:247]\n" \
"v_mfma_f32_16x16x32_bf16 a[248:251], v[88:91], v[124:127], a[248:251]\n" \
"v_mfma_f32_16x16x32_bf16 a[252:255], v[92:95], v[124:127], a[252:255]\n" \
"s_add_u32 s52, s52, 1024\n" \
"s_add_u32 s53, s53, s54\n" \
"s_waitcnt vmcnt(16) lgkmcnt(0)\n" \
"s_barrier\n" \
"v_mfma_f32_16x16x32_bf16 a[0:3], v[0:3], v[32:35], a[0:3]\n" \
"ds_read_b64_tr_b16 v[96:97], v137 offset:49152\n" \
"s_add_u32 m0, s56, 32768\n" \
"v_mfma_f32_16x16x32_bf16 a[4:7], v[4:7], v[32:35], a[4:7]\n" \
"buffer_load_dwordx4 v128, s[40:43], s52 offen offset:0 sc1 lds\n" \
"v_mfma_f32_16x16x32_bf16 a[8:11], v[8:11], v[32:35], a[8:11]\n" \
"ds_read_b64_tr_b16 v[98:99], v137 offset:57344\n" \
"v_mfma_f32_16x16x32_bf16 a[12:15], v[12:15], v[32:35], a[12:15]\n" \
"v_mfma_f32_16x16x32_bf16 a[16:19], v[16:19], v[32:35], a[16:19]\n" \
"ds_read_b128 v[64:67], v136 offset:49152\n" \
"v_mfma_f32_16x16x32_bf16 a[20:23], v[20:23], v[32:35], a[20:23]\n" \
"v_mfma_f32_16x16x32_bf16 a[24:27], v[24:27], v[32:35], a[24:27]\n" \
"ds_read_b128 v[68:71], v136 offset:50176\n" \
"v_mfma_f32_16x16x32_bf16 a[28:31], v[28:31], v[32:35], a[28:31]\n" \
"v_mfma_f32_16x16x32_bf16 a[32:35], v[0:3], v[36:39], a[32:35]\n" \
"ds_read_b128 v[72:75], v136 offset:51200\n" \
"v_mfma_f32_16x16x32_bf16 a[36:39], v[4:7], v[36:39], a[36:39]\n" \
"buffer_load_dwordx4 v129, s[40:43], s52 offen offset:1024 sc1 lds\n" \
"v_mfma_f32_16x16x32_bf16 a[40:43], v[8:11], v[36:39], a[40:43]\n" \
"ds_read_b128 v[76:79], v136 offset:52224\n" \
"v_mfma_f32_16x16x32_bf16 a[44:47], v[12:15], v[36:39], a[44:47]\n" \
"v_mfma_f32_16x16x32_bf16 a[48:51], v[16:19], v[36:39], a[48:51]\n" \
"ds_read_b128 v[80:83], v136 offset:53248\n" \
"v_mfma_f32_16x16x32_bf16 a[52:55], v[20:23], v[36:39], a[52:55]\n" \
"v_mfma_f32_16x16x32_bf16 a[56:59], v[24:27], v[36:39], a[56:59]\n" \
"ds_read_b128 v[84:87], v136 offset:54272\n" \
"v_mfma_f32_16x16x32_bf16 a[60:63], v[28:31], v[36:39], a[60:63]\n" \
"v_mfma_f32_16x16x32_bf16 a[64:67], v[0:3], v[40:43], a[64:67]\n" \
"ds_read_b128 v[88:91], v136 offset:55296\n" \
"v_mfma_f32_16x16x32_bf16 a[68:71], v[4:7], v[40:43], a[68:71]\n" \
"buffer_load_dwordx4 v130, s[40:43], s52 offen offset:2048 sc1 lds\n" \
"v_mfma_f32_16x16x32_bf16 a[72:75], v[8:11], v[40:43], a[72:75]\n" \
"ds_read_b128 v[92:95], v136 offset:56320\n" \
"v_mfma_f32_16x16x32_bf16 a[76:79], v[12:15], v[40:43], a[76:79]\n" \
"v_mfma_f32_16x16x32_bf16 a[80:83], v[16:19], v[40:43], a[80:83]\n" \
"ds_read_b64_tr_b16 v[100:101], v138 offset:49152\n" \
"v_mfma_f32_16x16x32_bf16 a[84:87], v[20:23], v[40:43], a[84:87]\n" \
"v_mfma_f32_16x16x32_bf16 a[88:91], v[24:27], v[40:43], a[88:91]\n" \
"ds_read_b64_tr_b16 v[102:103], v138 offset:57344\n" \
"v_mfma_f32_16x16x32_bf16 a[92:95], v[28:31], v[40:43], a[92:95]\n" \
"v_mfma_f32_16x16x32_bf16 a[96:99], v[0:3], v[44:47], a[96:99]\n" \
"ds_read_b64_tr_b16 v[104:105], v139 offset:49152\n" \
"v_mfma_f32_16x16x32_bf16 a[100:103], v[4:7], v[44:47], a[100:103]\n" \
"buffer_load_dwordx4 v131, s[40:43], s52 offen offset:3072 sc1 lds\n" \
"v_mfma_f32_16x16x32_bf16 a[104:107], v[8:11], v[44:47], a[104:107]\n" \
"ds_read_b64_tr_b16 v[106:107], v139 offset:57344\n" \
"v_mfma_f32_16x16x32_bf16 a[108:111], v[12:15], v[44:47], a[108:111]\n" \
"v_mfma_f32_16x16x32_bf16 a[112:115], v[16:19], v[44:47], a[112:115]\n" \
"ds_read_b64_tr_b16 v[108:109], v140 offset:49152\n" \
"v_mfma_f32_16x16x32_bf16 a[116:119], v[20:23], v[44:47], a[116:119]\n" \
"v_mfma_f32_16x16x32_bf16 a[120:123], v[24:27], v[44:47], a[120:123]\n" \
"ds_read_b64_tr_b16 v[110:111], v140 offset:57344\n" \
"v_mfma_f32_16x16x32_bf16 a[124:127], v[28:31], v[44:47], a[124:127]\n" \
"v_mfma_f32_16x16x32_bf16 a[128:131], v[0:3], v[48:51], a[128:131]\n" \
"ds_read_b64_tr_b16 v[112:113], v137 offset:50176\n" \
"s_add_u32 m0, s57, 32768\n" \
"v_mfma_f32_16x16x32_bf16 a[132:135], v[4:7], v[48:51], a[132:135]\n" \
"buffer_load_dwordx4 v132, s[44:47], s53 offen offset:0 lds\n" \
"v_mfma_f32_16x16x32_bf16 a[136:139], v[8:11], v[48:51], a[136:139]\n" \
"ds_read_b64_tr_b16 v[114:115], v137 offset:58368\n" \
"v_mfma_f32_16x16x32_bf16 a[140:143], v[12:15], v[48:51], a[140:143]\n" \
"v_mfma_f32_16x16x32_bf16 a[144:147], v[16:19], v[48:51], a[144:147]\n" \
"ds_read_b64_tr_b16 v[116:117], v138 offset:50176\n" \
"v_mfma_f32_16x16x32_bf16 a[148:151], v[20:23], v[48:51], a[148:151]\n" \
"v_mfma_f32_16x16x32_bf16 a[152:155], v[24:27], v[48:51], a[152:155]\n" \
"ds_read_b64_tr_b16 v[118:119], v138 offset:58368\n" \
"v_mfma_f32_16x16x32_bf16 a[156:159], v[28:31], v[48:51], a[156:159]\n" \
"v_mfma_f32_16x16x32_bf16 a[160:163], v[0:3], v[52:55], a[160:163]\n" \
"ds_read_b64_tr_b16 v[120:121], v139 offset:50176\n" \
"v_mfma_f32_16x16x32_bf16 a[164:167], v[4:7], v[52:55], a[164:167]\n" \
"buffer_load_dwordx4 v133, s[48:51], s53 offen offset:1024 lds\n" \
"v_mfma_f32_16x16x32_bf16 a[168:171], v[8:11], v[52:55], a[168:171]\n" \
"ds_read_b64_tr_b16 v[122:123], v139 offset:58368\n" \
"v_mfma_f32_16x16x32_bf16 a[172:175], v[12:15], v[52:55], a[172:175]\n" \
"v_mfma_f32_16x16x32_bf16 a[176:179], v[16:19], v[52:55], a[176:179]\n" \
"ds_read_b64_tr_b16 v[124:125], v140 offset:50176\n" \
"v_mfma_f32_16x16x32_bf16 a[180:183], v[20:23], v[52:55], a[180:183]\n" \
"v_mfma_f32_16x16x32_bf16 a[184:187], v[24:27], v[52:55], a[184:187]\n" \
"ds_read_b64_tr_b16 v[126:127], v140 offset:58368\n" \
"v_mfma_f32_16x16x32_bf16 a[188:191], v[28:31], v[52:55], a[188:191]\n" \
"v_mfma_f32_16x16x32_bf16 a[192:195], v[0:3], v[56:59], a[192:195]\n" \
"v_mfma_f32_16x16x32_bf16 a[196:199], v[4:7], v[56:59], a[196:199]\n" \
"buffer_load_dwordx4 v134, s[44:47], s53 offen offset:2048 lds\n" \
"v_mfma_f32_16x16x32_bf16 a[200:203], v[8:11], v[56:59], a[200:203]\n" \
"v_mfma_f32_16x16x32_bf16 a[204:207], v[12:15], v[56:59], a[204:207]\n" \
"v_mfma_f32_16x16x32_bf16 a[208:211], v[16:19], v[56:59], a[208:211]\n" \
"v_mfma_f32_16x16x32_bf16 a[212:215], v[20:23], v[56:59], a[212:215]\n" \
"v_mfma_f32_16x16x32_bf16 a[216:219], v[24:27], v[56:59], a[216:219]\n" \
"v_mfma_f32_16x16x32_bf16 a[220:223], v[28:31], v[56:59], a[220:223]\n" \
"v_mfma_f32_16x16x32_bf16 a[224:227], v[0:3], v[60:63], a[224:227]\n" \
"v_mfma_f32_16x16x32_bf16 a[228:231], v[4:7], v[60:63], a[228:231]\n" \
"buffer_load_dwordx4 v135, s[48:51], s53 offen offset:3072 lds\n" \
"v_mfma_f32_16x16x32_bf16 a[232:235], v[8:11], v[60:63], a[232:235]\n" \
"v_mfma_f32_16x16x32_bf16 a[236:239], v[12:15], v[60:63], a[236:239]\n" \
"v_mfma_f32_16x16x32_bf16 a[240:243], v[16:19], v[60:63], a[240:243]\n" \
"v_mfma_f32_16x16x32_bf16 a[244:247], v[20:23], v[60:63], a[244:247]\n" \
"v_mfma_f32_16x16x32_bf16 a[248:251], v[24:27], v[60:63], a[248:251]\n" \
"v_mfma_f32_16x16x32_bf16 a[252:255], v[28:31], v[60:63], a[252:255]\n" \
"s_add_u32 s52, s52, 1024\n" \
"s_add_u32 s53, s53, s54\n" \
"s_waitcnt vmcnt(16) lgkmcnt(0)\n" \
"s_barrier\n" \
"v_mfma_f32_16x16x32_bf16 a[0:3], v[64:67], v[96:99], a[0:3]\n" \
"ds_read_b64_tr_b16 v[32:33], v137 offset:0\n" \
"s_add_u32 m0, s56, 49152\n" \
"v_mfma_f32_16x16x32_bf16 a[4:7], v[68:71], v[96:99], a[4:7]\n" \
"buffer_load_dwordx4 v128, s[40:43], s52 offen offset:0 sc1 lds\n" \
"v_mfma_f32_16x16x32_bf16 a[8:11], v[72:75], v[96:99], a[8:11]\n" \
"ds_read_b64_tr_b16 v[34:35], v137 offset:8192\n" \
"v_mfma_f32_16x16x32_bf16 a[12:15], v[76:79], v[96:99], a[12:15]\n" \
"v_mfma_f32_16x16x32_bf16 a[16:19], v[80:83], v[96:99], a[16:19]\n" \
"ds_read_b128 v[0:3], v136 offset:0\n" \
"v_mfma_f32_16x16x32_bf16 a[20:23], v[84:87], v[96:99], a[20:23]\n" \
"v_mfma_f32_16x16x32_bf16 a[24:27], v[88:91], v[96:99], a[24:27]\n" \
"ds_read_b128 v[4:7], v136 offset:1024\n" \
"v_mfma_f32_16x16x32_bf16 a[28:31], v[92:95], v[96:99], a[28:31]\n" \
"v_mfma_f32_16x16x32_bf16 a[32:35], v[64:67], v[100:103], a[32:35]\n" \
"ds_read_b128 v[8:11], v136 offset:2048\n" \
"v_mfma_f32_16x16x32_bf16 a[36:39], v[68:71], v[100:103], a[36:39]\n" \
"buffer_load_dwordx4 v129, s[40:43], s52 offen offset:1024 sc1 lds\n" \
"v_mfma_f32_16x16x32_bf16 a[40:43], v[72:75], v[100:103], a[40:43]\n" \
"ds_read_b128 v[12:15], v136 offset:3072\n" \
"v_mfma_f32_16x16x32_bf16 a[44:47], v[76:79], v[100:103], a[44:47]\n" \
"v_mfma_f32_16x16x32_bf16 a[48:51], v[80:83], v[100:103], a[48:51]\n" \
"ds_read_b128 v[16:19], v136 offset:4096\n" \
"v_mfma_f32_16x16x32_bf16 a[52:55], v[84:87], v[100:103], a[52:55]\n" \
"v_mfma_f32_16x16x32_bf16 a[56:59], v[88:91], v[100:103], a[56:59]\n" \
"ds_read_b128 v[20:23], v136 offset:5120\n" \
"v_mfma_f32_16x16x32_bf16 a[60:63], v[92:95], v[100:103], a[60:63]\n" \
"v_mfma_f32_16x16x32_bf16 a[64:67], v[64:67], v[104:107], a[64:67]\n" \
"ds_read_b128 v[24:27], v136 offset:6144\n" \
"v_mfma_f32_16x16x32_bf16 a[68:71], v[68:71], v[104:107], a[68:71]\n" \
"buffer_load_dwordx4 v130, s[40:43], s52 offen offset:2048 sc1 lds\n" \
"v_mfma_f32_16x16x32_bf16 a[72:75], v[72:75], v[104:107], a[72:75]\n" \
"ds_read_b128 v[28:31], v136 offset:7168\n" \
"v_mfma_f32_16x16x32_bf16 a[76:79], v[76:79], v[104:107], a[76:79]\n" \
"v_mfma_f32_16x16x32_bf16 a[80:83], v[80:83], v[104:107], a[80:83]\n" \
"ds_read_b64_tr_b16 v[36:37], v138 offset:0\n" \
"v_mfma_f32_16x16x32_bf16 a[84:87], v[84:87], v[104:107], a[84:87]\n" \
"v_mfma_f32_16x16x32_bf16 a[88:91], v[88:91], v[104:107], a[88:91]\n" \
"ds_read_b64_tr_b16 v[38:39], v138 offset:8192\n" \
"v_mfma_f32_16x16x32_bf16 a[92:95], v[92:95], v[104:107], a[92:95]\n" \
"v_mfma_f32_16x16x32_bf16 a[96:99], v[64:67], v[108:111], a[96:99]\n" \
"ds_read_b64_tr_b16 v[40:41], v139 offset:0\n" \
"v_mfma_f32_16x16x32_bf16 a[100:103], v[68:71], v[108:111], a[100:103]\n" \
"buffer_load_dwordx4 v131, s[40:43], s52 offen offset:3072 sc1 lds\n" \
"v_mfma_f32_16x16x32_bf16 a[104:107], v[72:75], v[108:111], a[104:107]\n" \
"ds_read_b64_tr_b16 v[42:43], v139 offset:8192\n" \
"v_mfma_f32_16x16x32_bf16 a[108:111], v[76:79], v[108:111], a[108:111]\n" \
"v_mfma_f32_16x16x32_bf16 a[112:115], v[80:83], v[108:111], a[112:115]\n" \
"ds_read_b64_tr_b16 v[44:45], v140 offset:0\n" \
"v_mfma_f32_16x16x32_bf16 a[116:119], v[84:87], v[108:111], a[116:119]\n" \
"v_mfma_f32_16x16x32_bf16 a[120:123], v[88:91], v[108:111], a[120:123]\n" \
"ds_read_b64_tr_b16 v[46:47], v140 offset:8192\n" \
"v_mfma_f32_16x16x32_bf16 a[124:127], v[92:95], v[108:111], a[124:127]\n" \
"v_mfma_f32_16x16x32_bf16 a[128:131], v[64:67], v[112:115], a[128:131]\n" \
"ds_read_b64_tr_b16 v[48:49], v137 offset:1024\n" \
"s_add_u32 m0, s57, 49152\n" \
"v_mfma_f32_16x16x32_bf16 a[132:135], v[68:71], v[112:115], a[132:135]\n" \
"buffer_load_dwordx4 v132, s[44:47], s53 offen offset:0 lds\n" \
"v_mfma_f32_16x16x32_bf16 a[136:139], v[72:75], v[112:115], a[136:139]\n" \
"ds_read_b64_tr_b16 v[50:51], v137 offset:9216\n" \
"v_mfma_f32_16x16x32_bf16 a[140:143], v[76:79], v[112:115], a[140:143]\n" \
"v_mfma_f32_16x16x32_bf16 a[144:147], v[80:83], v[112:115], a[144:147]\n" \
"ds_read_b64_tr_b16 v[52:53], v138 offset:1024\n" \
"v_mfma_f32_16x16x32_bf16 a[148:151], v[84:87], v[112:115], a[148:151]\n" \
"v_mfma_f32_16x16x32_bf16 a[152:155], v[88:91], v[112:115], a[152:155]\n" \
"ds_read_b64_tr_b16 v[54:55], v138 offset:9216\n" \
"v_mfma_f32_16x16x32_bf16 a[156:159], v[92:95], v[112:115], a[156:159]\n" \
"v_mfma_f32_16x16x32_bf16 a[160:163], v[64:67], v[116:119], a[160:163]\n" \
"ds_read_b64_tr_b16 v[56:57], v139 offset:1024\n" \
"v_mfma_f32_16x16x32_bf16 a[164:167], v[68:71], v[116:119], a[164:167]\n" \
"buffer_load_dwordx4 v133, s[48:51], s53 offen offset:1024 lds\n" \
"v_mfma_f32_16x16x32_bf16 a[168:171], v[72:75], v[116:119], a[168:171]\n" \
"ds_read_b64_tr_b16 v[58:59], v139 offset:9216\n" \
"v_mfma_f32_16x16x32_bf16 a[172:175], v[76:79], v[116:119], a[172:175]\n" \
"v_mfma_f32_16x16x32_bf16 a[176:179], v[80:83], v[116:119], a[176:179]\n" \
"ds_read_b64_tr_b16 v[60:61], v140 offset:1024\n" \
"v_mfma_f32_16x16x32_bf16 a[180:183], v[84:87], v[116:119], a[180:183]\n" \
"v_mfma_f32_16x16x32_bf16 a[184:187], v[88:91], v[116:119], a[184:187]\n" \
"ds_read_b64_tr_b16 v[62:63], v140 offset:9216\n" \
"v_mfma_f32_16x16x32_bf16 a[188:191], v[92:95], v[116:119], a[188:191]\n" \
"v_mfma_f32_16x16x32_bf16 a[192:195], v[64:67], v[120:123], a[192:195]\n" \
"v_mfma_f32_16x16x32_bf16 a[196:199], v[68:71], v[120:123], a[196:199]\n" \
"buffer_load_dwordx4 v134, s[44:47], s53 offen offset:2048 lds\n" \
"v_mfma_f32_16x16x32_bf16 a[200:203], v[72:75], v[120:123], a[200:203]\n" \
"v_mfma_f32_16x16x32_bf16 a[204:207], v[76:79], v[120:123], a[204:207]\n" \
"v_mfma_f32_16x16x32_bf16 a[208:211], v[80:83], v[120:123], a[208:211]\n" \
"v_mfma_f32_16x16x32_bf16 a[212:215], v[84:87], v[120:123], a[212:215]\n" \
"v_mfma_f32_16x16x32_bf16 a[216:219], v[88:91], v[120:123], a[216:219]\n" \
"v_mfma_f32_16x16x32_bf16 a[220:223], v[92:95], v[120:123], a[220:223]\n" \
"v_mfma_f32_16x16x32_bf16 a[224:227], v[64:67], v[124:127], a[224:227]\n" \
"v_mfma_f32_16x16x32_bf16 a[228:231], v[68:71], v[124:127], a[228:231]\n" \
"buffer_load_dwordx4 v135, s[48:51], s53 offen offset:3072 lds\n" \
"v_mfma_f32_16x16x32_bf16 a[232:235], v[72:75], v[124:127], a[232:235]\n" \
"v_mfma_f32_16x16x32_bf16 a[236:239], v[76:79], v[124:127], a[236:239]\n" \
"v_mfma_f32_16x16x32_bf16 a[240:243], v[80:83], v[124:127], a[240:243]\n" \
"v_mfma_f32_16x16x32_bf16 a[244:247], v[84:87], v[124:127], a[244:247]\n" \
"v_mfma_f32_16x16x32_bf16 a[248:251], v[88:91], v[124:127], a[248:251]\n" \
"v_mfma_f32_16x16x32_bf16 a[252:255], v[92:95], v[124:127], a[252:255]\n" \
"s_add_u32 s52, s52, 1024\n" \
"s_add_u32 s53, s53, s54\n" \
"s_sub_u32 s55, s55, 1\n" \
"s_cmp_lg_u32 s55, 0\n" \
"s_cbranch_scc1 LG88S_loop_%=\n" \
"LG88S_tail_%=:\n" \
"s_waitcnt vmcnt(16) lgkmcnt(0)\n" \
"s_barrier\n" \
"v_mfma_f32_16x16x32_bf16 a[0:3], v[0:3], v[32:35], a[0:3]\n" \
"ds_read_b64_tr_b16 v[96:97], v137 offset:16384\n" \
"v_mfma_f32_16x16x32_bf16 a[4:7], v[4:7], v[32:35], a[4:7]\n" \
"v_mfma_f32_16x16x32_bf16 a[8:11], v[8:11], v[32:35], a[8:11]\n" \
"ds_read_b64_tr_b16 v[98:99], v137 offset:24576\n" \
"v_mfma_f32_16x16x32_bf16 a[12:15], v[12:15], v[32:35], a[12:15]\n" \
"v_mfma_f32_16x16x32_bf16 a[16:19], v[16:19], v[32:35], a[16:19]\n" \
"ds_read_b128 v[64:67], v136 offset:16384\n" \
"v_mfma_f32_16x16x32_bf16 a[20:23], v[20:23], v[32:35], a[20:23]\n" \
"v_mfma_f32_16x16x32_bf16 a[24:27], v[24:27], v[32:35], a[24:27]\n" \
"ds_read_b128 v[68:71], v136 offset:17408\n" \
"v_mfma_f32_16x16x32_bf16 a[28:31], v[28:31], v[32:35], a[28:31]\n" \
"v_mfma_f32_16x16x32_bf16 a[32:35], v[0:3], v[36:39], a[32:35]\n" \
"ds_read_b128 v[72:75], v136 offset:18432\n" \
"v_mfma_f32_16x16x32_bf16 a[36:39], v[4:7], v[36:39], a[36:39]\n" \
"v_mfma_f32_16x16x32_bf16 a[40:43], v[8:11], v[36:39], a[40:43]\n" \
"ds_read_b128 v[76:79], v136 offset:19456\n" \
"v_mfma_f32_16x16x32_bf16 a[44:47], v[12:15], v[36:39], a[44:47]\n" \
"v_mfma_f32_16x16x32_bf16 a[48:51], v[16:19], v[36:39], a[48:51]\n" \
"ds_read_b128 v[80:83], v136 offset:20480\n" \
"v_mfma_f32_16x16x32_bf16 a[52:55], v[20:23], v[36:39], a[52:55]\n" \
"v_mfma_f32_16x16x32_bf16 a[56:59], v[24:27], v[36:39], a[56:59]\n" \
"ds_read_b128 v[84:87], v136 offset:21504\n" \
"v_mfma_f32_16x16x32_bf16 a[60:63], v[28:31], v[36:39], a[60:63]\n" \
"v_mfma_f32_16x16x32_bf16 a[64:67], v[0:3], v[40:43], a[64:67]\n" \
"ds_read_b128 v[88:91], v136 offset:22528\n" \
"v_mfma_f32_16x16x32_bf16 a[68:71], v[4:7], v[40:43], a[68:71]\n" \
"v_mfma_f32_16x16x32_bf16 a[72:75], v[8:11], v[40:43], a[72:75]\n" \
"ds_read_b128 v[92:95], v136 offset:23552\n" \
"v_mfma_f32_16x16x32_bf16 a[76:79], v[12:15], v[40:43], a[76:79]\n" \
"v_mfma_f32_16x16x32_bf16 a[80:83], v[16:19], v[40:43], a[80:83]\n" \
"ds_read_b64_tr_b16 v[100:101], v138 offset:16384\n" \
"v_mfma_f32_16x16x32_bf16 a[84:87], v[20:23], v[40:43], a[84:87]\n" \
"v_mfma_f32_16x16x32_bf16 a[88:91], v[24:27], v[40:43], a[88:91]\n" \
"ds_read_b64_tr_b16 v[102:103], v138 offset:24576\n" \
"v_mfma_f32_16x16x32_bf16 a[92:95], v[28:31], v[40:43], a[92:95]\n" \
"v_mfma_f32_16x16x32_bf16 a[96:99], v[0:3], v[44:47], a[96:99]\n" \
"ds_read_b64_tr_b16 v[104:105], v139 offset:16384\n" \
"v_mfma_f32_16x16x32_bf16 a[100:103], v[4:7], v[44:47], a[100:103]\n" \
"v_mfma_f32_16x16x32_bf16 a[104:107], v[8:11], v[44:47], a[104:107]\n" \
"ds_read_b64_tr_b16 v[106:107], v139 offset:24576\n" \
"v_mfma_f32_16x16x32_bf16 a[108:111], v[12:15], v[44:47], a[108:111]\n" \
"v_mfma_f32_16x16x32_bf16 a[112:115], v[16:19], v[44:47], a[112:115]\n" \
"ds_read_b64_tr_b16 v[108:109], v140 offset:16384\n" \
"v_mfma_f32_16x16x32_bf16 a[116:119], v[20:23], v[44:47], a[116:119]\n" \
"v_mfma_f32_16x16x32_bf16 a[120:123], v[24:27], v[44:47], a[120:123]\n" \
"ds_read_b64_tr_b16 v[110:111], v140 offset:24576\n" \
"v_mfma_f32_16x16x32_bf16 a[124:127], v[28:31], v[44:47], a[124:127]\n" \
"v_mfma_f32_16x16x32_bf16 a[128:131], v[0:3], v[48:51], a[128:131]\n" \
"ds_read_b64_tr_b16 v[112:113], v137 offset:17408\n" \
"v_mfma_f32_16x16x32_bf16 a[132:135], v[4:7], v[48:51], a[132:135]\n" \
"v_mfma_f32_16x16x32_bf16 a[136:139], v[8:11], v[48:51], a[136:139]\n" \
"ds_read_b64_tr_b16 v[114:115], v137 offset:25600\n" \
"v_mfma_f32_16x16x32_bf16 a[140:143], v[12:15], v[48:51], a[140:143]\n" \
"v_mfma_f32_16x16x32_bf16 a[144:147], v[16:19], v[48:51], a[144:147]\n" \
"ds_read_b64_tr_b16 v[116:117], v138 offset:17408\n" \
"v_mfma_f32_16x16x32_bf16 a[148:151], v[20:23], v[48:51], a[148:151]\n" \
"v_mfma_f32_16x16x32_bf16 a[152:155], v[24:27], v[48:51], a[152:155]\n" \
"ds_read_b64_tr_b16 v[118:119], v138 offset:25600\n" \
"v_mfma_f32_16x16x32_bf16 a[156:159], v[28:31], v[48:51], a[156:159]\n" \
"v_mfma_f32_16x16x32_bf16 a[160:163], v[0:3], v[52:55], a[160:163]\n" \
"ds_read_b64_tr_b16 v[120:121], v139 offset:17408\n" \
"v_mfma_f32_16x16x32_bf16 a[164:167], v[4:7], v[52:55], a[164:167]\n" \
"v_mfma_f32_16x16x32_bf16 a[168:171], v[8:11], v[52:55], a[168:171]\n" \
"ds_read_b64_tr_b16 v[122:123], v139 offset:25600\n" \
"v_mfma_f32_16x16x32_bf16 a[172:175], v[12:15], v[52:55], a[172:175]\n" \
"v_mfma_f32_16x16x32_bf16 a[176:179], v[16:19], v[52:55], a[176:179]\n" \
"ds_read_b64_tr_b16 v[124:125], v140 offset:17408\n" \
"v_mfma_f32_16x16x32_bf16 a[180:183], v[20:23], v[52:55], a[180:183]\n" \
"v_mfma_f32_16x16x32_bf16 a[184:187], v[24:27], v[52:55], a[184:187]\n" \
"ds_read_b64_tr_b16 v[126:127], v140 offset:25600\n" \
"v_mfma_f32_16x16x32_bf16 a[188:191], v[28:31], v[52:55], a[188:191]\n" \
"v_mfma_f32_16x16x32_bf16 a[192:195], v[0:3], v[56:59], a[192:195]\n" \
"v_mfma_f32_16x16x32_bf16 a[196:199], v[4:7], v[56:59], a[196:199]\n" \
"v_mfma_f32_16x16x32_bf16 a[200:203], v[8:11], v[56:59], a[200:203]\n" \
"v_mfma_f32_16x16x32_bf16 a[204:207], v[12:15], v[56:59], a[204:207]\n" \
"v_mfma_f32_16x16x32_bf16 a[208:211], v[16:19], v[56:59], a[208:211]\n" \
"v_mfma_f32_16x16x32_bf16 a[212:215], v[20:23], v[56:59], a[212:215]\n" \
"v_mfma_f32_16x16x32_bf16 a[216:219], v[24:27], v[56:59], a[216:219]\n" \
"v_mfma_f32_16x16x32_bf16 a[220:223], v[28:31], v[56:59], a[220:223]\n" \
"v_mfma_f32_16x16x32_bf16 a[224:227], v[0:3], v[60:63], a[224:227]\n" \
"v_mfma_f32_16x16x32_bf16 a[228:231], v[4:7], v[60:63], a[228:231]\n" \
"v_mfma_f32_16x16x32_bf16 a[232:235], v[8:11], v[60:63], a[232:235]\n" \
"v_mfma_f32_16x16x32_bf16 a[236:239], v[12:15], v[60:63], a[236:239]\n" \
"v_mfma_f32_16x16x32_bf16 a[240:243], v[16:19], v[60:63], a[240:243]\n" \
"v_mfma_f32_16x16x32_bf16 a[244:247], v[20:23], v[60:63], a[244:247]\n" \
"v_mfma_f32_16x16x32_bf16 a[248:251], v[24:27], v[60:63], a[248:251]\n" \
"v_mfma_f32_16x16x32_bf16 a[252:255], v[28:31], v[60:63], a[252:255]\n" \
"s_waitcnt vmcnt(8) lgkmcnt(0)\n" \
"s_barrier\n" \
"v_mfma_f32_16x16x32_bf16 a[0:3], v[64:67], v[96:99], a[0:3]\n" \
"ds_read_b64_tr_b16 v[32:33], v137 offset:32768\n" \
"v_mfma_f32_16x16x32_bf16 a[4:7], v[68:71], v[96:99], a[4:7]\n" \
"v_mfma_f32_16x16x32_bf16 a[8:11], v[72:75], v[96:99], a[8:11]\n" \
"ds_read_b64_tr_b16 v[34:35], v137 offset:40960\n" \
"v_mfma_f32_16x16x32_bf16 a[12:15], v[76:79], v[96:99], a[12:15]\n" \
"v_mfma_f32_16x16x32_bf16 a[16:19], v[80:83], v[96:99], a[16:19]\n" \
"ds_read_b128 v[0:3], v136 offset:32768\n" \
"v_mfma_f32_16x16x32_bf16 a[20:23], v[84:87], v[96:99], a[20:23]\n" \
"v_mfma_f32_16x16x32_bf16 a[24:27], v[88:91], v[96:99], a[24:27]\n" \
"ds_read_b128 v[4:7], v136 offset:33792\n" \
"v_mfma_f32_16x16x32_bf16 a[28:31], v[92:95], v[96:99], a[28:31]\n" \
"v_mfma_f32_16x16x32_bf16 a[32:35], v[64:67], v[100:103], a[32:35]\n" \
"ds_read_b128 v[8:11], v136 offset:34816\n" \
"v_mfma_f32_16x16x32_bf16 a[36:39], v[68:71], v[100:103], a[36:39]\n" \
"v_mfma_f32_16x16x32_bf16 a[40:43], v[72:75], v[100:103], a[40:43]\n" \
"ds_read_b128 v[12:15], v136 offset:35840\n" \
"v_mfma_f32_16x16x32_bf16 a[44:47], v[76:79], v[100:103], a[44:47]\n" \
"v_mfma_f32_16x16x32_bf16 a[48:51], v[80:83], v[100:103], a[48:51]\n" \
"ds_read_b128 v[16:19], v136 offset:36864\n" \
"v_mfma_f32_16x16x32_bf16 a[52:55], v[84:87], v[100:103], a[52:55]\n" \
"v_mfma_f32_16x16x32_bf16 a[56:59], v[88:91], v[100:103], a[56:59]\n" \
"ds_read_b128 v[20:23], v136 offset:37888\n" \
"v_mfma_f32_16x16x32_bf16 a[60:63], v[92:95], v[100:103], a[60:63]\n" \
"v_mfma_f32_16x16x32_bf16 a[64:67], v[64:67], v[104:107], a[64:67]\n" \
"ds_read_b128 v[24:27], v136 offset:38912\n" \
"v_mfma_f32_16x16x32_bf16 a[68:71], v[68:71], v[104:107], a[68:71]\n" \
"v_mfma_f32_16x16x32_bf16 a[72:75], v[72:75], v[104:107], a[72:75]\n" \
"ds_read_b128 v[28:31], v136 offset:39936\n" \
"v_mfma_f32_16x16x32_bf16 a[76:79], v[76:79], v[104:107], a[76:79]\n" \
"v_mfma_f32_16x16x32_bf16 a[80:83], v[80:83], v[104:107], a[80:83]\n" \
"ds_read_b64_tr_b16 v[36:37], v138 offset:32768\n" \
"v_mfma_f32_16x16x32_bf16 a[84:87], v[84:87], v[104:107], a[84:87]\n" \
"v_mfma_f32_16x16x32_bf16 a[88:91], v[88:91], v[104:107], a[88:91]\n" \
"ds_read_b64_tr_b16 v[38:39], v138 offset:40960\n" \
"v_mfma_f32_16x16x32_bf16 a[92:95], v[92:95], v[104:107], a[92:95]\n" \
"v_mfma_f32_16x16x32_bf16 a[96:99], v[64:67], v[108:111], a[96:99]\n" \
"ds_read_b64_tr_b16 v[40:41], v139 offset:32768\n" \
"v_mfma_f32_16x16x32_bf16 a[100:103], v[68:71], v[108:111], a[100:103]\n" \
"v_mfma_f32_16x16x32_bf16 a[104:107], v[72:75], v[108:111], a[104:107]\n" \
"ds_read_b64_tr_b16 v[42:43], v139 offset:40960\n" \
"v_mfma_f32_16x16x32_bf16 a[108:111], v[76:79], v[108:111], a[108:111]\n" \
"v_mfma_f32_16x16x32_bf16 a[112:115], v[80:83], v[108:111], a[112:115]\n" \
"ds_read_b64_tr_b16 v[44:45], v140 offset:32768\n" \
"v_mfma_f32_16x16x32_bf16 a[116:119], v[84:87], v[108:111], a[116:119]\n" \
"v_mfma_f32_16x16x32_bf16 a[120:123], v[88:91], v[108:111], a[120:123]\n" \
"ds_read_b64_tr_b16 v[46:47], v140 offset:40960\n" \
"v_mfma_f32_16x16x32_bf16 a[124:127], v[92:95], v[108:111], a[124:127]\n" \
"v_mfma_f32_16x16x32_bf16 a[128:131], v[64:67], v[112:115], a[128:131]\n" \
"ds_read_b64_tr_b16 v[48:49], v137 offset:33792\n" \
"v_mfma_f32_16x16x32_bf16 a[132:135], v[68:71], v[112:115], a[132:135]\n" \
"v_mfma_f32_16x16x32_bf16 a[136:139], v[72:75], v[112:115], a[136:139]\n" \
"ds_read_b64_tr_b16 v[50:51], v137 offset:41984\n" \
"v_mfma_f32_16x16x32_bf16 a[140:143], v[76:79], v[112:115], a[140:143]\n" \
"v_mfma_f32_16x16x32_bf16 a[144:147], v[80:83], v[112:115], a[144:147]\n" \
"ds_read_b64_tr_b16 v[52:53], v138 offset:33792\n" \
"v_mfma_f32_16x16x32_bf16 a[148:151], v[84:87], v[112:115], a[148:151]\n" \
"v_mfma_f32_16x16x32_bf16 a[152:155], v[88:91], v[112:115], a[152:155]\n" \
"ds_read_b64_tr_b16 v[54:55], v138 offset:41984\n" \
"v_mfma_f32_16x16x32_bf16 a[156:159], v[92:95], v[112:115], a[156:159]\n" \
"v_mfma_f32_16x16x32_bf16 a[160:163], v[64:67], v[116:119], a[160:163]\n" \
"ds_read_b64_tr_b16 v[56:57], v139 offset:33792\n" \
"v_mfma_f32_16x16x32_bf16 a[164:167], v[68:71], v[116:119], a[164:167]\n" \
"v_mfma_f32_16x16x32_bf16 a[168:171], v[72:75], v[116:119], a[168:171]\n" \
"ds_read_b64_tr_b16 v[58:59], v139 offset:41984\n" \
"v_mfma_f32_16x16x32_bf16 a[172:175], v[76:79], v[116:119], a[172:175]\n" \
"v_mfma_f32_16x16x32_bf16 a[176:179], v[80:83], v[116:119], a[176:179]\n" \
"ds_read_b64_tr_b16 v[60:61], v140 offset:33792\n" \
"v_mfma_f32_16x16x32_bf16 a[180:183], v[84:87], v[116:119], a[180:183]\n" \
"v_mfma_f32_16x16x32_bf16 a[184:187], v[88:91], v[116:119], a[184:187]\n" \
"ds_read_b64_tr_b16 v[62:63], v140 offset:41984\n" \
"v_mfma_f32_16x16x32_bf16 a[188:191], v[92:95], v[116:119], a[188:191]\n" \
"v_mfma_f32_16x16x32_bf16 a[192:195], v[64:67], v[120:123], a[192:195]\n" \
"v_mfma_f32_16x16x32_bf16 a[196:199], v[68:71], v[120:123], a[196:199]\n" \
"v_mfma_f32_16x16x32_bf16 a[200:203], v[72:75], v[120:123], a[200:203]\n" \
"v_mfma_f32_16x16x32_bf16 a[204:207], v[76:79], v[120:123], a[204:207]\n" \
"v_mfma_f32_16x16x32_bf16 a[208:211], v[80:83], v[120:123], a[208:211]\n" \
"v_mfma_f32_16x16x32_bf16 a[212:215], v[84:87], v[120:123], a[212:215]\n" \
"v_mfma_f32_16x16x32_bf16 a[216:219], v[88:91], v[120:123], a[216:219]\n" \
"v_mfma_f32_16x16x32_bf16 a[220:223], v[92:95], v[120:123], a[220:223]\n" \
"v_mfma_f32_16x16x32_bf16 a[224:227], v[64:67], v[124:127], a[224:227]\n" \
"v_mfma_f32_16x16x32_bf16 a[228:231], v[68:71], v[124:127], a[228:231]\n" \
"v_mfma_f32_16x16x32_bf16 a[232:235], v[72:75], v[124:127], a[232:235]\n" \
"v_mfma_f32_16x16x32_bf16 a[236:239], v[76:79], v[124:127], a[236:239]\n" \
"v_mfma_f32_16x16x32_bf16 a[240:243], v[80:83], v[124:127], a[240:243]\n" \
"v_mfma_f32_16x16x32_bf16 a[244:247], v[84:87], v[124:127], a[244:247]\n" \
"v_mfma_f32_16x16x32_bf16 a[248:251], v[88:91], v[124:127], a[248:251]\n" \
"v_mfma_f32_16x16x32_bf16 a[252:255], v[92:95], v[124:127], a[252:255]\n" \
"s_waitcnt vmcnt(0) lgkmcnt(0)\n" \
"s_barrier\n" \
"v_mfma_f32_16x16x32_bf16 a[0:3], v[0:3], v[32:35], a[0:3]\n" \
"ds_read_b64_tr_b16 v[96:97], v137 offset:49152\n" \
"v_mfma_f32_16x16x32_bf16 a[4:7], v[4:7], v[32:35], a[4:7]\n" \
"v_mfma_f32_16x16x32_bf16 a[8:11], v[8:11], v[32:35], a[8:11]\n" \
"ds_read_b64_tr_b16 v[98:99], v137 offset:57344\n" \
"v_mfma_f32_16x16x32_bf16 a[12:15], v[12:15], v[32:35], a[12:15]\n" \
"v_mfma_f32_16x16x32_bf16 a[16:19], v[16:19], v[32:35], a[16:19]\n" \
"ds_read_b128 v[64:67], v136 offset:49152\n" \
"v_mfma_f32_16x16x32_bf16 a[20:23], v[20:23], v[32:35], a[20:23]\n" \
"v_mfma_f32_16x16x32_bf16 a[24:27], v[24:27], v[32:35], a[24:27]\n" \
"ds_read_b128 v[68:71], v136 offset:50176\n" \
"v_mfma_f32_16x16x32_bf16 a[28:31], v[28:31], v[32:35], a[28:31]\n" \
"v_mfma_f32_16x16x32_bf16 a[32:35], v[0:3], v[36:39], a[32:35]\n" \
"ds_read_b128 v[72:75], v136 offset:51200\n" \
"v_mfma_f32_16x16x32_bf16 a[36:39], v[4:7], v[36:39], a[36:39]\n" \
"v_mfma_f32_16x16x32_bf16 a[40:43], v[8:11], v[36:39], a[40:43]\n" \
"ds_read_b128 v[76:79], v136 offset:52224\n" \
"v_mfma_f32_16x16x32_bf16 a[44:47], v[12:15], v[36:39], a[44:47]\n" \
"v_mfma_f32_16x16x32_bf16 a[48:51], v[16:19], v[36:39], a[48:51]\n" \
"ds_read_b128 v[80:83], v136 offset:53248\n" \
"v_mfma_f32_16x16x32_bf16 a[52:55], v[20:23], v[36:39], a[52:55]\n" \
"v_mfma_f32_16x16x32_bf16 a[56:59], v[24:27], v[36:39], a[56:59]\n" \
"ds_read_b128 v[84:87], v136 offset:54272\n" \
"v_mfma_f32_16x16x32_bf16 a[60:63], v[28:31], v[36:39], a[60:63]\n" \
"v_mfma_f32_16x16x32_bf16 a[64:67], v[0:3], v[40:43], a[64:67]\n" \
"ds_read_b128 v[88:91], v136 offset:55296\n" \
"v_mfma_f32_16x16x32_bf16 a[68:71], v[4:7], v[40:43], a[68:71]\n" \
"v_mfma_f32_16x16x32_bf16 a[72:75], v[8:11], v[40:43], a[72:75]\n" \
"ds_read_b128 v[92:95], v136 offset:56320\n" \
"v_mfma_f32_16x16x32_bf16 a[76:79], v[12:15], v[40:43], a[76:79]\n" \
"v_mfma_f32_16x16x32_bf16 a[80:83], v[16:19], v[40:43], a[80:83]\n" \
"ds_read_b64_tr_b16 v[100:101], v138 offset:49152\n" \
"v_mfma_f32_16x16x32_bf16 a[84:87], v[20:23], v[40:43], a[84:87]\n" \
"v_mfma_f32_16x16x32_bf16 a[88:91], v[24:27], v[40:43], a[88:91]\n" \
"ds_read_b64_tr_b16 v[102:103], v138 offset:57344\n" \
"v_mfma_f32_16x16x32_bf16 a[92:95], v[28:31], v[40:43], a[92:95]\n" \
"v_mfma_f32_16x16x32_bf16 a[96:99], v[0:3], v[44:47], a[96:99]\n" \
"ds_read_b64_tr_b16 v[104:105], v139 offset:49152\n" \
"v_mfma_f32_16x16x32_bf16 a[100:103], v[4:7], v[44:47], a[100:103]\n" \
"v_mfma_f32_16x16x32_bf16 a[104:107], v[8:11], v[44:47], a[104:107]\n" \
"ds_read_b64_tr_b16 v[106:107], v139 offset:57344\n" \
"v_mfma_f32_16x16x32_bf16 a[108:111], v[12:15], v[44:47], a[108:111]\n" \
"v_mfma_f32_16x16x32_bf16 a[112:115], v[16:19], v[44:47], a[112:115]\n" \
"ds_read_b64_tr_b16 v[108:109], v140 offset:49152\n" \
"v_mfma_f32_16x16x32_bf16 a[116:119], v[20:23], v[44:47], a[116:119]\n" \
"v_mfma_f32_16x16x32_bf16 a[120:123], v[24:27], v[44:47], a[120:123]\n" \
"ds_read_b64_tr_b16 v[110:111], v140 offset:57344\n" \
"v_mfma_f32_16x16x32_bf16 a[124:127], v[28:31], v[44:47], a[124:127]\n" \
"v_mfma_f32_16x16x32_bf16 a[128:131], v[0:3], v[48:51], a[128:131]\n" \
"ds_read_b64_tr_b16 v[112:113], v137 offset:50176\n" \
"v_mfma_f32_16x16x32_bf16 a[132:135], v[4:7], v[48:51], a[132:135]\n" \
"v_mfma_f32_16x16x32_bf16 a[136:139], v[8:11], v[48:51], a[136:139]\n" \
"ds_read_b64_tr_b16 v[114:115], v137 offset:58368\n" \
"v_mfma_f32_16x16x32_bf16 a[140:143], v[12:15], v[48:51], a[140:143]\n" \
"v_mfma_f32_16x16x32_bf16 a[144:147], v[16:19], v[48:51], a[144:147]\n" \
"ds_read_b64_tr_b16 v[116:117], v138 offset:50176\n" \
"v_mfma_f32_16x16x32_bf16 a[148:151], v[20:23], v[48:51], a[148:151]\n" \
"v_mfma_f32_16x16x32_bf16 a[152:155], v[24:27], v[48:51], a[152:155]\n" \
"ds_read_b64_tr_b16 v[118:119], v138 offset:58368\n" \
"v_mfma_f32_16x16x32_bf16 a[156:159], v[28:31], v[48:51], a[156:159]\n" \
"v_mfma_f32_16x16x32_bf16 a[160:163], v[0:3], v[52:55], a[160:163]\n" \
"ds_read_b64_tr_b16 v[120:121], v139 offset:50176\n" \
"v_mfma_f32_16x16x32_bf16 a[164:167], v[4:7], v[52:55], a[164:167]\n" \
"v_mfma_f32_16x16x32_bf16 a[168:171], v[8:11], v[52:55], a[168:171]\n" \
"ds_read_b64_tr_b16 v[122:123], v139 offset:58368\n" \
"v_mfma_f32_16x16x32_bf16 a[172:175], v[12:15], v[52:55], a[172:175]\n" \
"v_mfma_f32_16x16x32_bf16 a[176:179], v[16:19], v[52:55], a[176:179]\n" \
"ds_read_b64_tr_b16 v[124:125], v140 offset:50176\n" \
"v_mfma_f32_16x16x32_bf16 a[180:183], v[20:23], v[52:55], a[180:183]\n" \
"v_mfma_f32_16x16x32_bf16 a[184:187], v[24:27], v[52:55], a[184:187]\n" \
"ds_read_b64_tr_b16 v[126:127], v140 offset:58368\n" \
"v_mfma_f32_16x16x32_bf16 a[188:191], v[28:31], v[52:55], a[188:191]\n" \
"v_mfma_f32_16x16x32_bf16 a[192:195], v[0:3], v[56:59], a[192:195]\n" \
"v_mfma_f32_16x16x32_bf16 a[196:199], v[4:7], v[56:59], a[196:199]\n" \
"v_mfma_f32_16x16x32_bf16 a[200:203], v[8:11], v[56:59], a[200:203]\n" \
"v_mfma_f32_16x16x32_bf16 a[204:207], v[12:15], v[56:59], a[204:207]\n" \
"v_mfma_f32_16x16x32_bf16 a[208:211], v[16:19], v[56:59], a[208:211]\n" \
"v_mfma_f32_16x16x32_bf16 a[212:215], v[20:23], v[56:59], a[212:215]\n" \
"v_mfma_f32_16x16x32_bf16 a[216:219], v[24:27], v[56:59], a[216:219]\n" \
"v_mfma_f32_16x16x32_bf16 a[220:223], v[28:31], v[56:59], a[220:223]\n" \
"v_mfma_f32_16x16x32_bf16 a[224:227], v[0:3], v[60:63], a[224:227]\n" \
"v_mfma_f32_16x16x32_bf16 a[228:231], v[4:7], v[60:63], a[228:231]\n" \
"v_mfma_f32_16x16x32_bf16 a[232:235], v[8:11], v[60:63], a[232:235]\n" \
"v_mfma_f32_16x16x32_bf16 a[236:239], v[12:15], v[60:63], a[236:239]\n" \
"v_mfma_f32_16x16x32_bf16 a[240:243], v[16:19], v[60:63], a[240:243]\n" \
"v_mfma_f32_16x16x32_bf16 a[244:247], v[20:23], v[60:63], a[244:247]\n" \
"v_mfma_f32_16x16x32_bf16 a[248:251], v[24:27], v[60:63], a[248:251]\n" \
"v_mfma_f32_16x16x32_bf16 a[252:255], v[28:31], v[60:63], a[252:255]\n" \
"s_waitcnt lgkmcnt(0)\n" \
"v_mfma_f32_16x16x32_bf16 a[0:3], v[64:67], v[96:99], a[0:3]\n" \
"v_mfma_f32_16x16x32_bf16 a[4:7], v[68:71], v[96:99], a[4:7]\n" \
"v_mfma_f32_16x16x32_bf16 a[8:11], v[72:75], v[96:99], a[8:11]\n" \
"v_mfma_f32_16x16x32_bf16 a[12:15], v[76:79], v[96:99], a[12:15]\n" \
"v_mfma_f32_16x16x32_bf16 a[16:19], v[80:83], v[96:99], a[16:19]\n" \
"v_mfma_f32_16x16x32_bf16 a[20:23], v[84:87], v[96:99], a[20:23]\n" \
"v_mfma_f32_16x16x32_bf16 a[24:27], v[88:91], v[96:99], a[24:27]\n" \
"v_mfma_f32_16x16x32_bf16 a[28:31], v[92:95], v[96:99], a[28:31]\n" \
"v_mfma_f32_16x16x32_bf16 a[32:35], v[64:67], v[100:103], a[32:35]\n" \
"v_mfma_f32_16x16x32_bf16 a[36:39], v[68:71], v[100:103], a[36:39]\n" \
"v_mfma_f32_16x16x32_bf16 a[40:43], v[72:75], v[100:103], a[40:43]\n" \
"v_mfma_f32_16x16x32_bf16 a[44:47], v[76:79], v[100:103], a[44:47]\n" \
"v_mfma_f32_16x16x32_bf16 a[48:51], v[80:83], v[100:103], a[48:51]\n" \
"v_mfma_f32_16x16x32_bf16 a[52:55], v[84:87], v[100:103], a[52:55]\n" \
"v_mfma_f32_16x16x32_bf16 a[56:59], v[88:91], v[100:103], a[56:59]\n" \
"v_mfma_f32_16x16x32_bf16 a[60:63], v[92:95], v[100:103], a[60:63]\n" \
"v_mfma_f32_16x16x32_bf16 a[64:67], v[64:67], v[104:107], a[64:67]\n" \
"v_mfma_f32_16x16x32_bf16 a[68:71], v[68:71], v[104:107], a[68:71]\n" \
"v_mfma_f32_16x16x32_bf16 a[72:75], v[72:75], v[104:107], a[72:75]\n" \
"v_mfma_f32_16x16x32_bf16 a[76:79], v[76:79], v[104:107], a[76:79]\n" \
"v_mfma_f32_16x16x32_bf16 a[80:83], v[80:83], v[104:107], a[80:83]\n" \
"v_mfma_f32_16x16x32_bf16 a[84:87], v[84:87], v[104:107], a[84:87]\n" \
"v_mfma_f32_16x16x32_bf16 a[88:91], v[88:91], v[104:107], a[88:91]\n" \
"v_mfma_f32_16x16x32_bf16 a[92:95], v[92:95], v[104:107], a[92:95]\n" \
"v_mfma_f32_16x16x32_bf16 a[96:99], v[64:67], v[108:111], a[96:99]\n" \
"v_mfma_f32_16x16x32_bf16 a[100:103], v[68:71], v[108:111], a[100:103]\n" \
"v_mfma_f32_16x16x32_bf16 a[104:107], v[72:75], v[108:111], a[104:107]\n" \
"v_mfma_f32_16x16x32_bf16 a[108:111], v[76:79], v[108:111], a[108:111]\n" \
"v_mfma_f32_16x16x32_bf16 a[112:115], v[80:83], v[108:111], a[112:115]\n" \
"v_mfma_f32_16x16x32_bf16 a[116:119], v[84:87], v[108:111], a[116:119]\n" \
"v_mfma_f32_16x16x32_bf16 a[120:123], v[88:91], v[108:111], a[120:123]\n" \
"v_mfma_f32_16x16x32_bf16 a[124:127], v[92:95], v[108:111], a[124:127]\n" \
"v_mfma_f32_16x16x32_bf16 a[128:131], v[64:67], v[112:115], a[128:131]\n" \
"v_mfma_f32_16x16x32_bf16 a[132:135], v[68:71], v[112:115], a[132:135]\n" \
"v_mfma_f32_16x16x32_bf16 a[136:139], v[72:75], v[112:115], a[136:139]\n" \
"v_mfma_f32_16x16x32_bf16 a[140:143], v[76:79], v[112:115], a[140:143]\n" \
"v_mfma_f32_16x16x32_bf16 a[144:147], v[80:83], v[112:115], a[144:147]\n" \
"v_mfma_f32_16x16x32_bf16 a[148:151], v[84:87], v[112:115], a[148:151]\n" \
"v_mfma_f32_16x16x32_bf16 a[152:155], v[88:91], v[112:115], a[152:155]\n" \
"v_mfma_f32_16x16x32_bf16 a[156:159], v[92:95], v[112:115], a[156:159]\n" \
"v_mfma_f32_16x16x32_bf16 a[160:163], v[64:67], v[116:119], a[160:163]\n" \
"v_mfma_f32_16x16x32_bf16 a[164:167], v[68:71], v[116:119], a[164:167]\n" \
"v_mfma_f32_16x16x32_bf16 a[168:171], v[72:75], v[116:119], a[168:171]\n" \
"v_mfma_f32_16x16x32_bf16 a[172:175], v[76:79], v[116:119], a[172:175]\n" \
"v_mfma_f32_16x16x32_bf16 a[176:179], v[80:83], v[116:119], a[176:179]\n" \
"v_mfma_f32_16x16x32_bf16 a[180:183], v[84:87], v[116:119], a[180:183]\n" \
"v_mfma_f32_16x16x32_bf16 a[184:187], v[88:91], v[116:119], a[184:187]\n" \
"v_mfma_f32_16x16x32_bf16 a[188:191], v[92:95], v[116:119], a[188:191]\n" \
"v_mfma_f32_16x16x32_bf16 a[192:195], v[64:67], v[120:123], a[192:195]\n" \
"v_mfma_f32_16x16x32_bf16 a[196:199], v[68:71], v[120:123], a[196:199]\n" \
"v_mfma_f32_16x16x32_bf16 a[200:203], v[72:75], v[120:123], a[200:203]\n" \
"v_mfma_f32_16x16x32_bf16 a[204:207], v[76:79], v[120:123], a[204:207]\n" \
"v_mfma_f32_16x16x32_bf16 a[208:211], v[80:83], v[120:123], a[208:211]\n" \
"v_mfma_f32_16x16x32_bf16 a[212:215], v[84:87], v[120:123], a[212:215]\n" \
"v_mfma_f32_16x16x32_bf16 a[216:219], v[88:91], v[120:123], a[216:219]\n" \
"v_mfma_f32_16x16x32_bf16 a[220:223], v[92:95], v[120:123], a[220:223]\n" \
"v_mfma_f32_16x16x32_bf16 a[224:227], v[64:67], v[124:127], a[224:227]\n" \
"v_mfma_f32_16x16x32_bf16 a[228:231], v[68:71], v[124:127], a[228:231]\n" \
"v_mfma_f32_16x16x32_bf16 a[232:235], v[72:75], v[124:127], a[232:235]\n" \
"v_mfma_f32_16x16x32_bf16 a[236:239], v[76:79], v[124:127], a[236:239]\n" \
"v_mfma_f32_16x16x32_bf16 a[240:243], v[80:83], v[124:127], a[240:243]\n" \
"v_mfma_f32_16x16x32_bf16 a[244:247], v[84:87], v[124:127], a[244:247]\n" \
"v_mfma_f32_16x16x32_bf16 a[248:251], v[88:91], v[124:127], a[248:251]\n" \
"v_mfma_f32_16x16x32_bf16 a[252:255], v[92:95], v[124:127], a[252:255]\n" \
"s_nop 7\n" \
"LG88S_eV_%=:\n" \
"s_add_u32 s66, s64, 4096\n" \
"v_accvgpr_read_b32 v0, a0\n" \
"v_accvgpr_read_b32 v1, a1\n" \
"v_accvgpr_read_b32 v2, a2\n" \
"v_accvgpr_read_b32 v3, a3\n" \
"v_accvgpr_read_b32 v4, a4\n" \
"v_accvgpr_read_b32 v5, a5\n" \
"v_accvgpr_read_b32 v6, a6\n" \
"v_accvgpr_read_b32 v7, a7\n" \
"v_cvt_pk_bf16_f32 v0, v0, v1\n" \
"v_cvt_pk_bf16_f32 v1, v2, v3\n" \
"v_cvt_pk_bf16_f32 v2, v4, v5\n" \
"v_cvt_pk_bf16_f32 v3, v6, v7\n" \
"buffer_store_dwordx4 v[0:3], v141, s[60:63], s64 offen offset:0 sc1\n" \
"v_accvgpr_read_b32 v8, a32\n" \
"v_accvgpr_read_b32 v9, a33\n" \
"v_accvgpr_read_b32 v10, a34\n" \
"v_accvgpr_read_b32 v11, a35\n" \
"v_accvgpr_read_b32 v12, a36\n" \
"v_accvgpr_read_b32 v13, a37\n" \
"v_accvgpr_read_b32 v14, a38\n" \
"v_accvgpr_read_b32 v15, a39\n" \
"v_cvt_pk_bf16_f32 v8, v8, v9\n" \
"v_cvt_pk_bf16_f32 v9, v10, v11\n" \
"v_cvt_pk_bf16_f32 v10, v12, v13\n" \
"v_cvt_pk_bf16_f32 v11, v14, v15\n" \
"buffer_store_dwordx4 v[8:11], v141, s[60:63], s64 offen offset:1024 sc1\n" \
"v_accvgpr_read_b32 v16, a64\n" \
"v_accvgpr_read_b32 v17, a65\n" \
"v_accvgpr_read_b32 v18, a66\n" \
"v_accvgpr_read_b32 v19, a67\n" \
"v_accvgpr_read_b32 v20, a68\n" \
"v_accvgpr_read_b32 v21, a69\n" \
"v_accvgpr_read_b32 v22, a70\n" \
"v_accvgpr_read_b32 v23, a71\n" \
"v_cvt_pk_bf16_f32 v16, v16, v17\n" \
"v_cvt_pk_bf16_f32 v17, v18, v19\n" \
"v_cvt_pk_bf16_f32 v18, v20, v21\n" \
"v_cvt_pk_bf16_f32 v19, v22, v23\n" \
"buffer_store_dwordx4 v[16:19], v141, s[60:63], s64 offen offset:2048 sc1\n" \
"v_accvgpr_read_b32 v24, a96\n" \
"v_accvgpr_read_b32 v25, a97\n" \
"v_accvgpr_read_b32 v26, a98\n" \
"v_accvgpr_read_b32 v27, a99\n" \
"v_accvgpr_read_b32 v28, a100\n" \
"v_accvgpr_read_b32 v29, a101\n" \
"v_accvgpr_read_b32 v30, a102\n" \
"v_accvgpr_read_b32 v31, a103\n" \
"v_cvt_pk_bf16_f32 v24, v24, v25\n" \
"v_cvt_pk_bf16_f32 v25, v26, v27\n" \
"v_cvt_pk_bf16_f32 v26, v28, v29\n" \
"v_cvt_pk_bf16_f32 v27, v30, v31\n" \
"buffer_store_dwordx4 v[24:27], v141, s[60:63], s64 offen offset:3072 sc1\n" \
"v_accvgpr_read_b32 v32, a128\n" \
"v_accvgpr_read_b32 v33, a129\n" \
"v_accvgpr_read_b32 v34, a130\n" \
"v_accvgpr_read_b32 v35, a131\n" \
"v_accvgpr_read_b32 v36, a132\n" \
"v_accvgpr_read_b32 v37, a133\n" \
"v_accvgpr_read_b32 v38, a134\n" \
"v_accvgpr_read_b32 v39, a135\n" \
"v_cvt_pk_bf16_f32 v32, v32, v33\n" \
"v_cvt_pk_bf16_f32 v33, v34, v35\n" \
"v_cvt_pk_bf16_f32 v34, v36, v37\n" \
"v_cvt_pk_bf16_f32 v35, v38, v39\n" \
"buffer_store_dwordx4 v[32:35], v141, s[60:63], s66 offen offset:0 sc1\n" \
"v_accvgpr_read_b32 v40, a160\n" \
"v_accvgpr_read_b32 v41, a161\n" \
"v_accvgpr_read_b32 v42, a162\n" \
"v_accvgpr_read_b32 v43, a163\n" \
"v_accvgpr_read_b32 v44, a164\n" \
"v_accvgpr_read_b32 v45, a165\n" \
"v_accvgpr_read_b32 v46, a166\n" \
"v_accvgpr_read_b32 v47, a167\n" \
"v_cvt_pk_bf16_f32 v40, v40, v41\n" \
"v_cvt_pk_bf16_f32 v41, v42, v43\n" \
"v_cvt_pk_bf16_f32 v42, v44, v45\n" \
"v_cvt_pk_bf16_f32 v43, v46, v47\n" \
"buffer_store_dwordx4 v[40:43], v141, s[60:63], s66 offen offset:1024 sc1\n" \
"v_accvgpr_read_b32 v48, a192\n" \
"v_accvgpr_read_b32 v49, a193\n" \
"v_accvgpr_read_b32 v50, a194\n" \
"v_accvgpr_read_b32 v51, a195\n" \
"v_accvgpr_read_b32 v52, a196\n" \
"v_accvgpr_read_b32 v53, a197\n" \
"v_accvgpr_read_b32 v54, a198\n" \
"v_accvgpr_read_b32 v55, a199\n" \
"v_cvt_pk_bf16_f32 v48, v48, v49\n" \
"v_cvt_pk_bf16_f32 v49, v50, v51\n" \
"v_cvt_pk_bf16_f32 v50, v52, v53\n" \
"v_cvt_pk_bf16_f32 v51, v54, v55\n" \
"buffer_store_dwordx4 v[48:51], v141, s[60:63], s66 offen offset:2048 sc1\n" \
"v_accvgpr_read_b32 v56, a224\n" \
"v_accvgpr_read_b32 v57, a225\n" \
"v_accvgpr_read_b32 v58, a226\n" \
"v_accvgpr_read_b32 v59, a227\n" \
"v_accvgpr_read_b32 v60, a228\n" \
"v_accvgpr_read_b32 v61, a229\n" \
"v_accvgpr_read_b32 v62, a230\n" \
"v_accvgpr_read_b32 v63, a231\n" \
"v_cvt_pk_bf16_f32 v56, v56, v57\n" \
"v_cvt_pk_bf16_f32 v57, v58, v59\n" \
"v_cvt_pk_bf16_f32 v58, v60, v61\n" \
"v_cvt_pk_bf16_f32 v59, v62, v63\n" \
"buffer_store_dwordx4 v[56:59], v141, s[60:63], s66 offen offset:3072 sc1\n" \
"s_add_u32 s64, s64, s65\n" \
"s_add_u32 s66, s64, 4096\n" \
"v_accvgpr_read_b32 v0, a8\n" \
"v_accvgpr_read_b32 v1, a9\n" \
"v_accvgpr_read_b32 v2, a10\n" \
"v_accvgpr_read_b32 v3, a11\n" \
"v_accvgpr_read_b32 v4, a12\n" \
"v_accvgpr_read_b32 v5, a13\n" \
"v_accvgpr_read_b32 v6, a14\n" \
"v_accvgpr_read_b32 v7, a15\n" \
"v_cvt_pk_bf16_f32 v0, v0, v1\n" \
"v_cvt_pk_bf16_f32 v1, v2, v3\n" \
"v_cvt_pk_bf16_f32 v2, v4, v5\n" \
"v_cvt_pk_bf16_f32 v3, v6, v7\n" \
"buffer_store_dwordx4 v[0:3], v141, s[60:63], s64 offen offset:0 sc1\n" \
"v_accvgpr_read_b32 v8, a40\n" \
"v_accvgpr_read_b32 v9, a41\n" \
"v_accvgpr_read_b32 v10, a42\n" \
"v_accvgpr_read_b32 v11, a43\n" \
"v_accvgpr_read_b32 v12, a44\n" \
"v_accvgpr_read_b32 v13, a45\n" \
"v_accvgpr_read_b32 v14, a46\n" \
"v_accvgpr_read_b32 v15, a47\n" \
"v_cvt_pk_bf16_f32 v8, v8, v9\n" \
"v_cvt_pk_bf16_f32 v9, v10, v11\n" \
"v_cvt_pk_bf16_f32 v10, v12, v13\n" \
"v_cvt_pk_bf16_f32 v11, v14, v15\n" \
"buffer_store_dwordx4 v[8:11], v141, s[60:63], s64 offen offset:1024 sc1\n" \
"v_accvgpr_read_b32 v16, a72\n" \
"v_accvgpr_read_b32 v17, a73\n" \
"v_accvgpr_read_b32 v18, a74\n" \
"v_accvgpr_read_b32 v19, a75\n" \
"v_accvgpr_read_b32 v20, a76\n" \
"v_accvgpr_read_b32 v21, a77\n" \
"v_accvgpr_read_b32 v22, a78\n" \
"v_accvgpr_read_b32 v23, a79\n" \
"v_cvt_pk_bf16_f32 v16, v16, v17\n" \
"v_cvt_pk_bf16_f32 v17, v18, v19\n" \
"v_cvt_pk_bf16_f32 v18, v20, v21\n" \
"v_cvt_pk_bf16_f32 v19, v22, v23\n" \
"buffer_store_dwordx4 v[16:19], v141, s[60:63], s64 offen offset:2048 sc1\n" \
"v_accvgpr_read_b32 v24, a104\n" \
"v_accvgpr_read_b32 v25, a105\n" \
"v_accvgpr_read_b32 v26, a106\n" \
"v_accvgpr_read_b32 v27, a107\n" \
"v_accvgpr_read_b32 v28, a108\n" \
"v_accvgpr_read_b32 v29, a109\n" \
"v_accvgpr_read_b32 v30, a110\n" \
"v_accvgpr_read_b32 v31, a111\n" \
"v_cvt_pk_bf16_f32 v24, v24, v25\n" \
"v_cvt_pk_bf16_f32 v25, v26, v27\n" \
"v_cvt_pk_bf16_f32 v26, v28, v29\n" \
"v_cvt_pk_bf16_f32 v27, v30, v31\n" \
"buffer_store_dwordx4 v[24:27], v141, s[60:63], s64 offen offset:3072 sc1\n" \
"v_accvgpr_read_b32 v32, a136\n" \
"v_accvgpr_read_b32 v33, a137\n" \
"v_accvgpr_read_b32 v34, a138\n" \
"v_accvgpr_read_b32 v35, a139\n" \
"v_accvgpr_read_b32 v36, a140\n" \
"v_accvgpr_read_b32 v37, a141\n" \
"v_accvgpr_read_b32 v38, a142\n" \
"v_accvgpr_read_b32 v39, a143\n" \
"v_cvt_pk_bf16_f32 v32, v32, v33\n" \
"v_cvt_pk_bf16_f32 v33, v34, v35\n" \
"v_cvt_pk_bf16_f32 v34, v36, v37\n" \
"v_cvt_pk_bf16_f32 v35, v38, v39\n" \
"buffer_store_dwordx4 v[32:35], v141, s[60:63], s66 offen offset:0 sc1\n" \
"v_accvgpr_read_b32 v40, a168\n" \
"v_accvgpr_read_b32 v41, a169\n" \
"v_accvgpr_read_b32 v42, a170\n" \
"v_accvgpr_read_b32 v43, a171\n" \
"v_accvgpr_read_b32 v44, a172\n" \
"v_accvgpr_read_b32 v45, a173\n" \
"v_accvgpr_read_b32 v46, a174\n" \
"v_accvgpr_read_b32 v47, a175\n" \
"v_cvt_pk_bf16_f32 v40, v40, v41\n" \
"v_cvt_pk_bf16_f32 v41, v42, v43\n" \
"v_cvt_pk_bf16_f32 v42, v44, v45\n" \
"v_cvt_pk_bf16_f32 v43, v46, v47\n" \
"buffer_store_dwordx4 v[40:43], v141, s[60:63], s66 offen offset:1024 sc1\n" \
"v_accvgpr_read_b32 v48, a200\n" \
"v_accvgpr_read_b32 v49, a201\n" \
"v_accvgpr_read_b32 v50, a202\n" \
"v_accvgpr_read_b32 v51, a203\n" \
"v_accvgpr_read_b32 v52, a204\n" \
"v_accvgpr_read_b32 v53, a205\n" \
"v_accvgpr_read_b32 v54, a206\n" \
"v_accvgpr_read_b32 v55, a207\n" \
"v_cvt_pk_bf16_f32 v48, v48, v49\n" \
"v_cvt_pk_bf16_f32 v49, v50, v51\n" \
"v_cvt_pk_bf16_f32 v50, v52, v53\n" \
"v_cvt_pk_bf16_f32 v51, v54, v55\n" \
"buffer_store_dwordx4 v[48:51], v141, s[60:63], s66 offen offset:2048 sc1\n" \
"v_accvgpr_read_b32 v56, a232\n" \
"v_accvgpr_read_b32 v57, a233\n" \
"v_accvgpr_read_b32 v58, a234\n" \
"v_accvgpr_read_b32 v59, a235\n" \
"v_accvgpr_read_b32 v60, a236\n" \
"v_accvgpr_read_b32 v61, a237\n" \
"v_accvgpr_read_b32 v62, a238\n" \
"v_accvgpr_read_b32 v63, a239\n" \
"v_cvt_pk_bf16_f32 v56, v56, v57\n" \
"v_cvt_pk_bf16_f32 v57, v58, v59\n" \
"v_cvt_pk_bf16_f32 v58, v60, v61\n" \
"v_cvt_pk_bf16_f32 v59, v62, v63\n" \
"buffer_store_dwordx4 v[56:59], v141, s[60:63], s66 offen offset:3072 sc1\n" \
"s_add_u32 s64, s64, s65\n" \
"s_add_u32 s66, s64, 4096\n" \
"v_accvgpr_read_b32 v0, a16\n" \
"v_accvgpr_read_b32 v1, a17\n" \
"v_accvgpr_read_b32 v2, a18\n" \
"v_accvgpr_read_b32 v3, a19\n" \
"v_accvgpr_read_b32 v4, a20\n" \
"v_accvgpr_read_b32 v5, a21\n" \
"v_accvgpr_read_b32 v6, a22\n" \
"v_accvgpr_read_b32 v7, a23\n" \
"v_cvt_pk_bf16_f32 v0, v0, v1\n" \
"v_cvt_pk_bf16_f32 v1, v2, v3\n" \
"v_cvt_pk_bf16_f32 v2, v4, v5\n" \
"v_cvt_pk_bf16_f32 v3, v6, v7\n" \
"buffer_store_dwordx4 v[0:3], v141, s[60:63], s64 offen offset:0 sc1\n" \
"v_accvgpr_read_b32 v8, a48\n" \
"v_accvgpr_read_b32 v9, a49\n" \
"v_accvgpr_read_b32 v10, a50\n" \
"v_accvgpr_read_b32 v11, a51\n" \
"v_accvgpr_read_b32 v12, a52\n" \
"v_accvgpr_read_b32 v13, a53\n" \
"v_accvgpr_read_b32 v14, a54\n" \
"v_accvgpr_read_b32 v15, a55\n" \
"v_cvt_pk_bf16_f32 v8, v8, v9\n" \
"v_cvt_pk_bf16_f32 v9, v10, v11\n" \
"v_cvt_pk_bf16_f32 v10, v12, v13\n" \
"v_cvt_pk_bf16_f32 v11, v14, v15\n" \
"buffer_store_dwordx4 v[8:11], v141, s[60:63], s64 offen offset:1024 sc1\n" \
"v_accvgpr_read_b32 v16, a80\n" \
"v_accvgpr_read_b32 v17, a81\n" \
"v_accvgpr_read_b32 v18, a82\n" \
"v_accvgpr_read_b32 v19, a83\n" \
"v_accvgpr_read_b32 v20, a84\n" \
"v_accvgpr_read_b32 v21, a85\n" \
"v_accvgpr_read_b32 v22, a86\n" \
"v_accvgpr_read_b32 v23, a87\n" \
"v_cvt_pk_bf16_f32 v16, v16, v17\n" \
"v_cvt_pk_bf16_f32 v17, v18, v19\n" \
"v_cvt_pk_bf16_f32 v18, v20, v21\n" \
"v_cvt_pk_bf16_f32 v19, v22, v23\n" \
"buffer_store_dwordx4 v[16:19], v141, s[60:63], s64 offen offset:2048 sc1\n" \
"v_accvgpr_read_b32 v24, a112\n" \
"v_accvgpr_read_b32 v25, a113\n" \
"v_accvgpr_read_b32 v26, a114\n" \
"v_accvgpr_read_b32 v27, a115\n" \
"v_accvgpr_read_b32 v28, a116\n" \
"v_accvgpr_read_b32 v29, a117\n" \
"v_accvgpr_read_b32 v30, a118\n" \
"v_accvgpr_read_b32 v31, a119\n" \
"v_cvt_pk_bf16_f32 v24, v24, v25\n" \
"v_cvt_pk_bf16_f32 v25, v26, v27\n" \
"v_cvt_pk_bf16_f32 v26, v28, v29\n" \
"v_cvt_pk_bf16_f32 v27, v30, v31\n" \
"buffer_store_dwordx4 v[24:27], v141, s[60:63], s64 offen offset:3072 sc1\n" \
"v_accvgpr_read_b32 v32, a144\n" \
"v_accvgpr_read_b32 v33, a145\n" \
"v_accvgpr_read_b32 v34, a146\n" \
"v_accvgpr_read_b32 v35, a147\n" \
"v_accvgpr_read_b32 v36, a148\n" \
"v_accvgpr_read_b32 v37, a149\n" \
"v_accvgpr_read_b32 v38, a150\n" \
"v_accvgpr_read_b32 v39, a151\n" \
"v_cvt_pk_bf16_f32 v32, v32, v33\n" \
"v_cvt_pk_bf16_f32 v33, v34, v35\n" \
"v_cvt_pk_bf16_f32 v34, v36, v37\n" \
"v_cvt_pk_bf16_f32 v35, v38, v39\n" \
"buffer_store_dwordx4 v[32:35], v141, s[60:63], s66 offen offset:0 sc1\n" \
"v_accvgpr_read_b32 v40, a176\n" \
"v_accvgpr_read_b32 v41, a177\n" \
"v_accvgpr_read_b32 v42, a178\n" \
"v_accvgpr_read_b32 v43, a179\n" \
"v_accvgpr_read_b32 v44, a180\n" \
"v_accvgpr_read_b32 v45, a181\n" \
"v_accvgpr_read_b32 v46, a182\n" \
"v_accvgpr_read_b32 v47, a183\n" \
"v_cvt_pk_bf16_f32 v40, v40, v41\n" \
"v_cvt_pk_bf16_f32 v41, v42, v43\n" \
"v_cvt_pk_bf16_f32 v42, v44, v45\n" \
"v_cvt_pk_bf16_f32 v43, v46, v47\n" \
"buffer_store_dwordx4 v[40:43], v141, s[60:63], s66 offen offset:1024 sc1\n" \
"v_accvgpr_read_b32 v48, a208\n" \
"v_accvgpr_read_b32 v49, a209\n" \
"v_accvgpr_read_b32 v50, a210\n" \
"v_accvgpr_read_b32 v51, a211\n" \
"v_accvgpr_read_b32 v52, a212\n" \
"v_accvgpr_read_b32 v53, a213\n" \
"v_accvgpr_read_b32 v54, a214\n" \
"v_accvgpr_read_b32 v55, a215\n" \
"v_cvt_pk_bf16_f32 v48, v48, v49\n" \
"v_cvt_pk_bf16_f32 v49, v50, v51\n" \
"v_cvt_pk_bf16_f32 v50, v52, v53\n" \
"v_cvt_pk_bf16_f32 v51, v54, v55\n" \
"buffer_store_dwordx4 v[48:51], v141, s[60:63], s66 offen offset:2048 sc1\n" \
"v_accvgpr_read_b32 v56, a240\n" \
"v_accvgpr_read_b32 v57, a241\n" \
"v_accvgpr_read_b32 v58, a242\n" \
"v_accvgpr_read_b32 v59, a243\n" \
"v_accvgpr_read_b32 v60, a244\n" \
"v_accvgpr_read_b32 v61, a245\n" \
"v_accvgpr_read_b32 v62, a246\n" \
"v_accvgpr_read_b32 v63, a247\n" \
"v_cvt_pk_bf16_f32 v56, v56, v57\n" \
"v_cvt_pk_bf16_f32 v57, v58, v59\n" \
"v_cvt_pk_bf16_f32 v58, v60, v61\n" \
"v_cvt_pk_bf16_f32 v59, v62, v63\n" \
"buffer_store_dwordx4 v[56:59], v141, s[60:63], s66 offen offset:3072 sc1\n" \
"s_add_u32 s64, s64, s65\n" \
"s_add_u32 s66, s64, 4096\n" \
"v_accvgpr_read_b32 v0, a24\n" \
"v_accvgpr_read_b32 v1, a25\n" \
"v_accvgpr_read_b32 v2, a26\n" \
"v_accvgpr_read_b32 v3, a27\n" \
"v_accvgpr_read_b32 v4, a28\n" \
"v_accvgpr_read_b32 v5, a29\n" \
"v_accvgpr_read_b32 v6, a30\n" \
"v_accvgpr_read_b32 v7, a31\n" \
"v_cvt_pk_bf16_f32 v0, v0, v1\n" \
"v_cvt_pk_bf16_f32 v1, v2, v3\n" \
"v_cvt_pk_bf16_f32 v2, v4, v5\n" \
"v_cvt_pk_bf16_f32 v3, v6, v7\n" \
"buffer_store_dwordx4 v[0:3], v141, s[60:63], s64 offen offset:0 sc1\n" \
"v_accvgpr_read_b32 v8, a56\n" \
"v_accvgpr_read_b32 v9, a57\n" \
"v_accvgpr_read_b32 v10, a58\n" \
"v_accvgpr_read_b32 v11, a59\n" \
"v_accvgpr_read_b32 v12, a60\n" \
"v_accvgpr_read_b32 v13, a61\n" \
"v_accvgpr_read_b32 v14, a62\n" \
"v_accvgpr_read_b32 v15, a63\n" \
"v_cvt_pk_bf16_f32 v8, v8, v9\n" \
"v_cvt_pk_bf16_f32 v9, v10, v11\n" \
"v_cvt_pk_bf16_f32 v10, v12, v13\n" \
"v_cvt_pk_bf16_f32 v11, v14, v15\n" \
"buffer_store_dwordx4 v[8:11], v141, s[60:63], s64 offen offset:1024 sc1\n" \
"v_accvgpr_read_b32 v16, a88\n" \
"v_accvgpr_read_b32 v17, a89\n" \
"v_accvgpr_read_b32 v18, a90\n" \
"v_accvgpr_read_b32 v19, a91\n" \
"v_accvgpr_read_b32 v20, a92\n" \
"v_accvgpr_read_b32 v21, a93\n" \
"v_accvgpr_read_b32 v22, a94\n" \
"v_accvgpr_read_b32 v23, a95\n" \
"v_cvt_pk_bf16_f32 v16, v16, v17\n" \
"v_cvt_pk_bf16_f32 v17, v18, v19\n" \
"v_cvt_pk_bf16_f32 v18, v20, v21\n" \
"v_cvt_pk_bf16_f32 v19, v22, v23\n" \
"buffer_store_dwordx4 v[16:19], v141, s[60:63], s64 offen offset:2048 sc1\n" \
"v_accvgpr_read_b32 v24, a120\n" \
"v_accvgpr_read_b32 v25, a121\n" \
"v_accvgpr_read_b32 v26, a122\n" \
"v_accvgpr_read_b32 v27, a123\n" \
"v_accvgpr_read_b32 v28, a124\n" \
"v_accvgpr_read_b32 v29, a125\n" \
"v_accvgpr_read_b32 v30, a126\n" \
"v_accvgpr_read_b32 v31, a127\n" \
"v_cvt_pk_bf16_f32 v24, v24, v25\n" \
"v_cvt_pk_bf16_f32 v25, v26, v27\n" \
"v_cvt_pk_bf16_f32 v26, v28, v29\n" \
"v_cvt_pk_bf16_f32 v27, v30, v31\n" \
"buffer_store_dwordx4 v[24:27], v141, s[60:63], s64 offen offset:3072 sc1\n" \
"v_accvgpr_read_b32 v32, a152\n" \
"v_accvgpr_read_b32 v33, a153\n" \
"v_accvgpr_read_b32 v34, a154\n" \
"v_accvgpr_read_b32 v35, a155\n" \
"v_accvgpr_read_b32 v36, a156\n" \
"v_accvgpr_read_b32 v37, a157\n" \
"v_accvgpr_read_b32 v38, a158\n" \
"v_accvgpr_read_b32 v39, a159\n" \
"v_cvt_pk_bf16_f32 v32, v32, v33\n" \
"v_cvt_pk_bf16_f32 v33, v34, v35\n" \
"v_cvt_pk_bf16_f32 v34, v36, v37\n" \
"v_cvt_pk_bf16_f32 v35, v38, v39\n" \
"buffer_store_dwordx4 v[32:35], v141, s[60:63], s66 offen offset:0 sc1\n" \
"v_accvgpr_read_b32 v40, a184\n" \
"v_accvgpr_read_b32 v41, a185\n" \
"v_accvgpr_read_b32 v42, a186\n" \
"v_accvgpr_read_b32 v43, a187\n" \
"v_accvgpr_read_b32 v44, a188\n" \
"v_accvgpr_read_b32 v45, a189\n" \
"v_accvgpr_read_b32 v46, a190\n" \
"v_accvgpr_read_b32 v47, a191\n" \
"v_cvt_pk_bf16_f32 v40, v40, v41\n" \
"v_cvt_pk_bf16_f32 v41, v42, v43\n" \
"v_cvt_pk_bf16_f32 v42, v44, v45\n" \
"v_cvt_pk_bf16_f32 v43, v46, v47\n" \
"buffer_store_dwordx4 v[40:43], v141, s[60:63], s66 offen offset:1024 sc1\n" \
"v_accvgpr_read_b32 v48, a216\n" \
"v_accvgpr_read_b32 v49, a217\n" \
"v_accvgpr_read_b32 v50, a218\n" \
"v_accvgpr_read_b32 v51, a219\n" \
"v_accvgpr_read_b32 v52, a220\n" \
"v_accvgpr_read_b32 v53, a221\n" \
"v_accvgpr_read_b32 v54, a222\n" \
"v_accvgpr_read_b32 v55, a223\n" \
"v_cvt_pk_bf16_f32 v48, v48, v49\n" \
"v_cvt_pk_bf16_f32 v49, v50, v51\n" \
"v_cvt_pk_bf16_f32 v50, v52, v53\n" \
"v_cvt_pk_bf16_f32 v51, v54, v55\n" \
"buffer_store_dwordx4 v[48:51], v141, s[60:63], s66 offen offset:2048 sc1\n" \
"v_accvgpr_read_b32 v56, a248\n" \
"v_accvgpr_read_b32 v57, a249\n" \
"v_accvgpr_read_b32 v58, a250\n" \
"v_accvgpr_read_b32 v59, a251\n" \
"v_accvgpr_read_b32 v60, a252\n" \
"v_accvgpr_read_b32 v61, a253\n" \
"v_accvgpr_read_b32 v62, a254\n" \
"v_accvgpr_read_b32 v63, a255\n" \
"v_cvt_pk_bf16_f32 v56, v56, v57\n" \
"v_cvt_pk_bf16_f32 v57, v58, v59\n" \
"v_cvt_pk_bf16_f32 v58, v60, v61\n" \
"v_cvt_pk_bf16_f32 v59, v62, v63\n" \
"buffer_store_dwordx4 v[56:59], v141, s[60:63], s66 offen offset:3072 sc1\n" \
"s_add_u32 s64, s64, s65\n" \
"LG88S_end_%=:\n" \
"s_nop 1\n"
#define ASM_G48 \
"s_add_u32 m0, s56, 0\n" \
"s_nop 0\n" \
"buffer_load_dwordx4 v128, s[40:43], s52 offen offset:0 sc1 lds\n" \
"buffer_load_dwordx4 v129, s[40:43], s52 offen offset:1024 sc1 lds\n" \
"s_add_u32 m0, s57, 0\n" \
"s_nop 0\n" \
"buffer_load_dwordx4 v132, s[44:47], s53 offen offset:0 lds\n" \
"buffer_load_dwordx4 v133, s[48:51], s53 offen offset:1024 lds\n" \
"buffer_load_dwordx4 v134, s[44:47], s53 offen offset:2048 lds\n" \
"buffer_load_dwordx4 v135, s[48:51], s53 offen offset:3072 lds\n" \
"s_add_u32 s52, s52, 1024\n" \
"s_add_u32 s53, s53, s54\n" \
"s_add_u32 m0, s56, 16384\n" \
"s_nop 0\n" \
"buffer_load_dwordx4 v128, s[40:43], s52 offen offset:0 sc1 lds\n" \
"buffer_load_dwordx4 v129, s[40:43], s52 offen offset:1024 sc1 lds\n" \
"s_add_u32 m0, s57, 16384\n" \
"s_nop 0\n" \
"buffer_load_dwordx4 v132, s[44:47], s53 offen offset:0 lds\n" \
"buffer_load_dwordx4 v133, s[48:51], s53 offen offset:1024 lds\n" \
"buffer_load_dwordx4 v134, s[44:47], s53 offen offset:2048 lds\n" \
"buffer_load_dwordx4 v135, s[48:51], s53 offen offset:3072 lds\n" \
"s_add_u32 s52, s52, 1024\n" \
"s_add_u32 s53, s53, s54\n" \
"s_add_u32 m0, s56, 32768\n" \
"s_nop 0\n" \
"buffer_load_dwordx4 v128, s[40:43], s52 offen offset:0 sc1 lds\n" \
"buffer_load_dwordx4 v129, s[40:43], s52 offen offset:1024 sc1 lds\n" \
"s_add_u32 m0, s57, 32768\n" \
"s_nop 0\n" \
"buffer_load_dwordx4 v132, s[44:47], s53 offen offset:0 lds\n" \
"buffer_load_dwordx4 v133, s[48:51], s53 offen offset:1024 lds\n" \
"buffer_load_dwordx4 v134, s[44:47], s53 offen offset:2048 lds\n" \
"buffer_load_dwordx4 v135, s[48:51], s53 offen offset:3072 lds\n" \
"s_add_u32 s52, s52, 1024\n" \
"s_add_u32 s53, s53, s54\n" \
"s_add_u32 m0, s56, 49152\n" \
"s_nop 0\n" \
"buffer_load_dwordx4 v128, s[40:43], s52 offen offset:0 sc1 lds\n" \
"buffer_load_dwordx4 v129, s[40:43], s52 offen offset:1024 sc1 lds\n" \
"s_add_u32 m0, s57, 49152\n" \
"s_nop 0\n" \
"buffer_load_dwordx4 v132, s[44:47], s53 offen offset:0 lds\n" \
"buffer_load_dwordx4 v133, s[48:51], s53 offen offset:1024 lds\n" \
"buffer_load_dwordx4 v134, s[44:47], s53 offen offset:2048 lds\n" \
"buffer_load_dwordx4 v135, s[48:51], s53 offen offset:3072 lds\n" \
"s_add_u32 s52, s52, 1024\n" \
"s_add_u32 s53, s53, s54\n" \
"v_accvgpr_write_b32 a0, 0\n" \
"v_accvgpr_write_b32 a1, 0\n" \
"v_accvgpr_write_b32 a2, 0\n" \
"v_accvgpr_write_b32 a3, 0\n" \
"v_accvgpr_write_b32 a4, 0\n" \
"v_accvgpr_write_b32 a5, 0\n" \
"v_accvgpr_write_b32 a6, 0\n" \
"v_accvgpr_write_b32 a7, 0\n" \
"v_accvgpr_write_b32 a8, 0\n" \
"v_accvgpr_write_b32 a9, 0\n" \
"v_accvgpr_write_b32 a10, 0\n" \
"v_accvgpr_write_b32 a11, 0\n" \
"v_accvgpr_write_b32 a12, 0\n" \
"v_accvgpr_write_b32 a13, 0\n" \
"v_accvgpr_write_b32 a14, 0\n" \
"v_accvgpr_write_b32 a15, 0\n" \
"v_accvgpr_write_b32 a16, 0\n" \
"v_accvgpr_write_b32 a17, 0\n" \
"v_accvgpr_write_b32 a18, 0\n" \
"v_accvgpr_write_b32 a19, 0\n" \
"v_accvgpr_write_b32 a20, 0\n" \
"v_accvgpr_write_b32 a21, 0\n" \
"v_accvgpr_write_b32 a22, 0\n" \
"v_accvgpr_write_b32 a23, 0\n" \
"v_accvgpr_write_b32 a24, 0\n" \
"v_accvgpr_write_b32 a25, 0\n" \
"v_accvgpr_write_b32 a26, 0\n" \
"v_accvgpr_write_b32 a27, 0\n" \
"v_accvgpr_write_b32 a28, 0\n" \
"v_accvgpr_write_b32 a29, 0\n" \
"v_accvgpr_write_b32 a30, 0\n" \
"v_accvgpr_write_b32 a31, 0\n" \
"v_accvgpr_write_b32 a32, 0\n" \
"v_accvgpr_write_b32 a33, 0\n" \
"v_accvgpr_write_b32 a34, 0\n" \
"v_accvgpr_write_b32 a35, 0\n" \
"v_accvgpr_write_b32 a36, 0\n" \
"v_accvgpr_write_b32 a37, 0\n" \
"v_accvgpr_write_b32 a38, 0\n" \
"v_accvgpr_write_b32 a39, 0\n" \
"v_accvgpr_write_b32 a40, 0\n" \
"v_accvgpr_write_b32 a41, 0\n" \
"v_accvgpr_write_b32 a42, 0\n" \
"v_accvgpr_write_b32 a43, 0\n" \
"v_accvgpr_write_b32 a44, 0\n" \
"v_accvgpr_write_b32 a45, 0\n" \
"v_accvgpr_write_b32 a46, 0\n" \
"v_accvgpr_write_b32 a47, 0\n" \
"v_accvgpr_write_b32 a48, 0\n" \
"v_accvgpr_write_b32 a49, 0\n" \
"v_accvgpr_write_b32 a50, 0\n" \
"v_accvgpr_write_b32 a51, 0\n" \
"v_accvgpr_write_b32 a52, 0\n" \
"v_accvgpr_write_b32 a53, 0\n" \
"v_accvgpr_write_b32 a54, 0\n" \
"v_accvgpr_write_b32 a55, 0\n" \
"v_accvgpr_write_b32 a56, 0\n" \
"v_accvgpr_write_b32 a57, 0\n" \
"v_accvgpr_write_b32 a58, 0\n" \
"v_accvgpr_write_b32 a59, 0\n" \
"v_accvgpr_write_b32 a60, 0\n" \
"v_accvgpr_write_b32 a61, 0\n" \
"v_accvgpr_write_b32 a62, 0\n" \
"v_accvgpr_write_b32 a63, 0\n" \
"v_accvgpr_write_b32 a64, 0\n" \
"v_accvgpr_write_b32 a65, 0\n" \
"v_accvgpr_write_b32 a66, 0\n" \
"v_accvgpr_write_b32 a67, 0\n" \
"v_accvgpr_write_b32 a68, 0\n" \
"v_accvgpr_write_b32 a69, 0\n" \
"v_accvgpr_write_b32 a70, 0\n" \
"v_accvgpr_write_b32 a71, 0\n" \
"v_accvgpr_write_b32 a72, 0\n" \
"v_accvgpr_write_b32 a73, 0\n" \
"v_accvgpr_write_b32 a74, 0\n" \
"v_accvgpr_write_b32 a75, 0\n" \
"v_accvgpr_write_b32 a76, 0\n" \
"v_accvgpr_write_b32 a77, 0\n" \
"v_accvgpr_write_b32 a78, 0\n" \
"v_accvgpr_write_b32 a79, 0\n" \
"v_accvgpr_write_b32 a80, 0\n" \
"v_accvgpr_write_b32 a81, 0\n" \
"v_accvgpr_write_b32 a82, 0\n" \
"v_accvgpr_write_b32 a83, 0\n" \
"v_accvgpr_write_b32 a84, 0\n" \
"v_accvgpr_write_b32 a85, 0\n" \
"v_accvgpr_write_b32 a86, 0\n" \
"v_accvgpr_write_b32 a87, 0\n" \
"v_accvgpr_write_b32 a88, 0\n" \
"v_accvgpr_write_b32 a89, 0\n" \
"v_accvgpr_write_b32 a90, 0\n" \
"v_accvgpr_write_b32 a91, 0\n" \
"v_accvgpr_write_b32 a92, 0\n" \
"v_accvgpr_write_b32 a93, 0\n" \
"v_accvgpr_write_b32 a94, 0\n" \
"v_accvgpr_write_b32 a95, 0\n" \
"v_accvgpr_write_b32 a96, 0\n" \
"v_accvgpr_write_b32 a97, 0\n" \
"v_accvgpr_write_b32 a98, 0\n" \
"v_accvgpr_write_b32 a99, 0\n" \
"v_accvgpr_write_b32 a100, 0\n" \
"v_accvgpr_write_b32 a101, 0\n" \
"v_accvgpr_write_b32 a102, 0\n" \
"v_accvgpr_write_b32 a103, 0\n" \
"v_accvgpr_write_b32 a104, 0\n" \
"v_accvgpr_write_b32 a105, 0\n" \
"v_accvgpr_write_b32 a106, 0\n" \
"v_accvgpr_write_b32 a107, 0\n" \
"v_accvgpr_write_b32 a108, 0\n" \
"v_accvgpr_write_b32 a109, 0\n" \
"v_accvgpr_write_b32 a110, 0\n" \
"v_accvgpr_write_b32 a111, 0\n" \
"v_accvgpr_write_b32 a112, 0\n" \
"v_accvgpr_write_b32 a113, 0\n" \
"v_accvgpr_write_b32 a114, 0\n" \
"v_accvgpr_write_b32 a115, 0\n" \
"v_accvgpr_write_b32 a116, 0\n" \
"v_accvgpr_write_b32 a117, 0\n" \
"v_accvgpr_write_b32 a118, 0\n" \
"v_accvgpr_write_b32 a119, 0\n" \
"v_accvgpr_write_b32 a120, 0\n" \
"v_accvgpr_write_b32 a121, 0\n" \
"v_accvgpr_write_b32 a122, 0\n" \
"v_accvgpr_write_b32 a123, 0\n" \
"v_accvgpr_write_b32 a124, 0\n" \
"v_accvgpr_write_b32 a125, 0\n" \
"v_accvgpr_write_b32 a126, 0\n" \
"v_accvgpr_write_b32 a127, 0\n" \
"s_waitcnt vmcnt(18)\n" \
"s_barrier\n" \
"ds_read_b64_tr_b16 v[32:33], v137 offset:0\n" \
"ds_read_b64_tr_b16 v[34:35], v137 offset:8192\n" \
"ds_read_b128 v[0:3], v136 offset:0\n" \
"ds_read_b128 v[4:7], v136 offset:1024\n" \
"ds_read_b128 v[8:11], v136 offset:2048\n" \
"ds_read_b128 v[12:15], v136 offset:3072\n" \
"ds_read_b64_tr_b16 v[36:37], v138 offset:0\n" \
"ds_read_b64_tr_b16 v[38:39], v138 offset:8192\n" \
"ds_read_b64_tr_b16 v[40:41], v139 offset:0\n" \
"ds_read_b64_tr_b16 v[42:43], v139 offset:8192\n" \
"ds_read_b64_tr_b16 v[44:45], v140 offset:0\n" \
"ds_read_b64_tr_b16 v[46:47], v140 offset:8192\n" \
"ds_read_b64_tr_b16 v[48:49], v137 offset:1024\n" \
"ds_read_b64_tr_b16 v[50:51], v137 offset:9216\n" \
"ds_read_b64_tr_b16 v[52:53], v138 offset:1024\n" \
"ds_read_b64_tr_b16 v[54:55], v138 offset:9216\n" \
"ds_read_b64_tr_b16 v[56:57], v139 offset:1024\n" \
"ds_read_b64_tr_b16 v[58:59], v139 offset:9216\n" \
"ds_read_b64_tr_b16 v[60:61], v140 offset:1024\n" \
"ds_read_b64_tr_b16 v[62:63], v140 offset:9216\n" \
"s_cmp_eq_u32 s55, 0\n" \
"s_cbranch_scc1 LG48_tail_%=\n" \
"LG48_loop_%=:\n" \
"s_waitcnt vmcnt(12) lgkmcnt(0)\n" \
"s_barrier\n" \
"v_mfma_f32_16x16x32_bf16 a[0:3], v[32:35], v[0:3], a[0:3]\n" \
"ds_read_b64_tr_b16 v[96:97], v137 offset:16384\n" \
"s_add_u32 m0, s56, 0\n" \
"v_mfma_f32_16x16x32_bf16 a[4:7], v[32:35], v[4:7], a[4:7]\n" \
"ds_read_b64_tr_b16 v[98:99], v137 offset:24576\n" \
"buffer_load_dwordx4 v128, s[40:43], s52 offen offset:0 sc1 lds\n" \
"v_mfma_f32_16x16x32_bf16 a[8:11], v[32:35], v[8:11], a[8:11]\n" \
"ds_read_b128 v[64:67], v136 offset:16384\n" \
"v_mfma_f32_16x16x32_bf16 a[12:15], v[32:35], v[12:15], a[12:15]\n" \
"ds_read_b128 v[68:71], v136 offset:17408\n" \
"v_mfma_f32_16x16x32_bf16 a[16:19], v[36:39], v[0:3], a[16:19]\n" \
"ds_read_b128 v[72:75], v136 offset:18432\n" \
"v_mfma_f32_16x16x32_bf16 a[20:23], v[36:39], v[4:7], a[20:23]\n" \
"v_mfma_f32_16x16x32_bf16 a[24:27], v[36:39], v[8:11], a[24:27]\n" \
"ds_read_b128 v[76:79], v136 offset:19456\n" \
"buffer_load_dwordx4 v129, s[40:43], s52 offen offset:1024 sc1 lds\n" \
"v_mfma_f32_16x16x32_bf16 a[28:31], v[36:39], v[12:15], a[28:31]\n" \
"ds_read_b64_tr_b16 v[100:101], v138 offset:16384\n" \
"v_mfma_f32_16x16x32_bf16 a[32:35], v[40:43], v[0:3], a[32:35]\n" \
"ds_read_b64_tr_b16 v[102:103], v138 offset:24576\n" \
"v_mfma_f32_16x16x32_bf16 a[36:39], v[40:43], v[4:7], a[36:39]\n" \
"ds_read_b64_tr_b16 v[104:105], v139 offset:16384\n" \
"v_mfma_f32_16x16x32_bf16 a[40:43], v[40:43], v[8:11], a[40:43]\n" \
"ds_read_b64_tr_b16 v[106:107], v139 offset:24576\n" \
"v_mfma_f32_16x16x32_bf16 a[44:47], v[40:43], v[12:15], a[44:47]\n" \
"s_add_u32 m0, s57, 0\n" \
"v_mfma_f32_16x16x32_bf16 a[48:51], v[44:47], v[0:3], a[48:51]\n" \
"ds_read_b64_tr_b16 v[108:109], v140 offset:16384\n" \
"buffer_load_dwordx4 v132, s[44:47], s53 offen offset:0 lds\n" \
"v_mfma_f32_16x16x32_bf16 a[52:55], v[44:47], v[4:7], a[52:55]\n" \
"ds_read_b64_tr_b16 v[110:111], v140 offset:24576\n" \
"v_mfma_f32_16x16x32_bf16 a[56:59], v[44:47], v[8:11], a[56:59]\n" \
"ds_read_b64_tr_b16 v[112:113], v137 offset:17408\n" \
"v_mfma_f32_16x16x32_bf16 a[60:63], v[44:47], v[12:15], a[60:63]\n" \
"ds_read_b64_tr_b16 v[114:115], v137 offset:25600\n" \
"v_mfma_f32_16x16x32_bf16 a[64:67], v[48:51], v[0:3], a[64:67]\n" \
"ds_read_b64_tr_b16 v[116:117], v138 offset:17408\n" \
"v_mfma_f32_16x16x32_bf16 a[68:71], v[48:51], v[4:7], a[68:71]\n" \
"buffer_load_dwordx4 v133, s[48:51], s53 offen offset:1024 lds\n" \
"v_mfma_f32_16x16x32_bf16 a[72:75], v[48:51], v[8:11], a[72:75]\n" \
"ds_read_b64_tr_b16 v[118:119], v138 offset:25600\n" \
"v_mfma_f32_16x16x32_bf16 a[76:79], v[48:51], v[12:15], a[76:79]\n" \
"ds_read_b64_tr_b16 v[120:121], v139 offset:17408\n" \
"v_mfma_f32_16x16x32_bf16 a[80:83], v[52:55], v[0:3], a[80:83]\n" \
"ds_read_b64_tr_b16 v[122:123], v139 offset:25600\n" \
"v_mfma_f32_16x16x32_bf16 a[84:87], v[52:55], v[4:7], a[84:87]\n" \
"ds_read_b64_tr_b16 v[124:125], v140 offset:17408\n" \
"v_mfma_f32_16x16x32_bf16 a[88:91], v[52:55], v[8:11], a[88:91]\n" \
"ds_read_b64_tr_b16 v[126:127], v140 offset:25600\n" \
"v_mfma_f32_16x16x32_bf16 a[92:95], v[52:55], v[12:15], a[92:95]\n" \
"buffer_load_dwordx4 v134, s[44:47], s53 offen offset:2048 lds\n" \
"v_mfma_f32_16x16x32_bf16 a[96:99], v[56:59], v[0:3], a[96:99]\n" \
"v_mfma_f32_16x16x32_bf16 a[100:103], v[56:59], v[4:7], a[100:103]\n" \
"v_mfma_f32_16x16x32_bf16 a[104:107], v[56:59], v[8:11], a[104:107]\n" \
"v_mfma_f32_16x16x32_bf16 a[108:111], v[56:59], v[12:15], a[108:111]\n" \
"v_mfma_f32_16x16x32_bf16 a[112:115], v[60:63], v[0:3], a[112:115]\n" \
"v_mfma_f32_16x16x32_bf16 a[116:119], v[60:63], v[4:7], a[116:119]\n" \
"buffer_load_dwordx4 v135, s[48:51], s53 offen offset:3072 lds\n" \
"v_mfma_f32_16x16x32_bf16 a[120:123], v[60:63], v[8:11], a[120:123]\n" \
"v_mfma_f32_16x16x32_bf16 a[124:127], v[60:63], v[12:15], a[124:127]\n" \
"s_add_u32 s52, s52, 1024\n" \
"s_add_u32 s53, s53, s54\n" \
"s_waitcnt vmcnt(12) lgkmcnt(0)\n" \
"s_barrier\n" \
"v_mfma_f32_16x16x32_bf16 a[0:3], v[96:99], v[64:67], a[0:3]\n" \
"ds_read_b64_tr_b16 v[32:33], v137 offset:32768\n" \
"s_add_u32 m0, s56, 16384\n" \
"v_mfma_f32_16x16x32_bf16 a[4:7], v[96:99], v[68:71], a[4:7]\n" \
"ds_read_b64_tr_b16 v[34:35], v137 offset:40960\n" \
"buffer_load_dwordx4 v128, s[40:43], s52 offen offset:0 sc1 lds\n" \
"v_mfma_f32_16x16x32_bf16 a[8:11], v[96:99], v[72:75], a[8:11]\n" \
"ds_read_b128 v[0:3], v136 offset:32768\n" \
"v_mfma_f32_16x16x32_bf16 a[12:15], v[96:99], v[76:79], a[12:15]\n" \
"ds_read_b128 v[4:7], v136 offset:33792\n" \
"v_mfma_f32_16x16x32_bf16 a[16:19], v[100:103], v[64:67], a[16:19]\n" \
"ds_read_b128 v[8:11], v136 offset:34816\n" \
"v_mfma_f32_16x16x32_bf16 a[20:23], v[100:103], v[68:71], a[20:23]\n" \
"v_mfma_f32_16x16x32_bf16 a[24:27], v[100:103], v[72:75], a[24:27]\n" \
"ds_read_b128 v[12:15], v136 offset:35840\n" \
"buffer_load_dwordx4 v129, s[40:43], s52 offen offset:1024 sc1 lds\n" \
"v_mfma_f32_16x16x32_bf16 a[28:31], v[100:103], v[76:79], a[28:31]\n" \
"ds_read_b64_tr_b16 v[36:37], v138 offset:32768\n" \
"v_mfma_f32_16x16x32_bf16 a[32:35], v[104:107], v[64:67], a[32:35]\n" \
"ds_read_b64_tr_b16 v[38:39], v138 offset:40960\n" \
"v_mfma_f32_16x16x32_bf16 a[36:39], v[104:107], v[68:71], a[36:39]\n" \
"ds_read_b64_tr_b16 v[40:41], v139 offset:32768\n" \
"v_mfma_f32_16x16x32_bf16 a[40:43], v[104:107], v[72:75], a[40:43]\n" \
"ds_read_b64_tr_b16 v[42:43], v139 offset:40960\n" \
"v_mfma_f32_16x16x32_bf16 a[44:47], v[104:107], v[76:79], a[44:47]\n" \
"s_add_u32 m0, s57, 16384\n" \
"v_mfma_f32_16x16x32_bf16 a[48:51], v[108:111], v[64:67], a[48:51]\n" \
"ds_read_b64_tr_b16 v[44:45], v140 offset:32768\n" \
"buffer_load_dwordx4 v132, s[44:47], s53 offen offset:0 lds\n" \
"v_mfma_f32_16x16x32_bf16 a[52:55], v[108:111], v[68:71], a[52:55]\n" \
"ds_read_b64_tr_b16 v[46:47], v140 offset:40960\n" \
"v_mfma_f32_16x16x32_bf16 a[56:59], v[108:111], v[72:75], a[56:59]\n" \
"ds_read_b64_tr_b16 v[48:49], v137 offset:33792\n" \
"v_mfma_f32_16x16x32_bf16 a[60:63], v[108:111], v[76:79], a[60:63]\n" \
"ds_read_b64_tr_b16 v[50:51], v137 offset:41984\n" \
"v_mfma_f32_16x16x32_bf16 a[64:67], v[112:115], v[64:67], a[64:67]\n" \
"ds_read_b64_tr_b16 v[52:53], v138 offset:33792\n" \
"v_mfma_f32_16x16x32_bf16 a[68:71], v[112:115], v[68:71], a[68:71]\n" \
"buffer_load_dwordx4 v133, s[48:51], s53 offen offset:1024 lds\n" \
"v_mfma_f32_16x16x32_bf16 a[72:75], v[112:115], v[72:75], a[72:75]\n" \
"ds_read_b64_tr_b16 v[54:55], v138 offset:41984\n" \
"v_mfma_f32_16x16x32_bf16 a[76:79], v[112:115], v[76:79], a[76:79]\n" \
"ds_read_b64_tr_b16 v[56:57], v139 offset:33792\n" \
"v_mfma_f32_16x16x32_bf16 a[80:83], v[116:119], v[64:67], a[80:83]\n" \
"ds_read_b64_tr_b16 v[58:59], v139 offset:41984\n" \
"v_mfma_f32_16x16x32_bf16 a[84:87], v[116:119], v[68:71], a[84:87]\n" \
"ds_read_b64_tr_b16 v[60:61], v140 offset:33792\n" \
"v_mfma_f32_16x16x32_bf16 a[88:91], v[116:119], v[72:75], a[88:91]\n" \
"ds_read_b64_tr_b16 v[62:63], v140 offset:41984\n" \
"v_mfma_f32_16x16x32_bf16 a[92:95], v[116:119], v[76:79], a[92:95]\n" \
"buffer_load_dwordx4 v134, s[44:47], s53 offen offset:2048 lds\n" \
"v_mfma_f32_16x16x32_bf16 a[96:99], v[120:123], v[64:67], a[96:99]\n" \
"v_mfma_f32_16x16x32_bf16 a[100:103], v[120:123], v[68:71], a[100:103]\n" \
"v_mfma_f32_16x16x32_bf16 a[104:107], v[120:123], v[72:75], a[104:107]\n" \
"v_mfma_f32_16x16x32_bf16 a[108:111], v[120:123], v[76:79], a[108:111]\n" \
"v_mfma_f32_16x16x32_bf16 a[112:115], v[124:127], v[64:67], a[112:115]\n" \
"v_mfma_f32_16x16x32_bf16 a[116:119], v[124:127], v[68:71], a[116:119]\n" \
"buffer_load_dwordx4 v135, s[48:51], s53 offen offset:3072 lds\n" \
"v_mfma_f32_16x16x32_bf16 a[120:123], v[124:127], v[72:75], a[120:123]\n" \
"v_mfma_f32_16x16x32_bf16 a[124:127], v[124:127], v[76:79], a[124:127]\n" \
"s_add_u32 s52, s52, 1024\n" \
"s_add_u32 s53, s53, s54\n" \
"s_waitcnt vmcnt(12) lgkmcnt(0)\n" \
"s_barrier\n" \
"v_mfma_f32_16x16x32_bf16 a[0:3], v[32:35], v[0:3], a[0:3]\n" \
"ds_read_b64_tr_b16 v[96:97], v137 offset:49152\n" \
"s_add_u32 m0, s56, 32768\n" \
"v_mfma_f32_16x16x32_bf16 a[4:7], v[32:35], v[4:7], a[4:7]\n" \
"ds_read_b64_tr_b16 v[98:99], v137 offset:57344\n" \
"buffer_load_dwordx4 v128, s[40:43], s52 offen offset:0 sc1 lds\n" \
"v_mfma_f32_16x16x32_bf16 a[8:11], v[32:35], v[8:11], a[8:11]\n" \
"ds_read_b128 v[64:67], v136 offset:49152\n" \
"v_mfma_f32_16x16x32_bf16 a[12:15], v[32:35], v[12:15], a[12:15]\n" \
"ds_read_b128 v[68:71], v136 offset:50176\n" \
"v_mfma_f32_16x16x32_bf16 a[16:19], v[36:39], v[0:3], a[16:19]\n" \
"ds_read_b128 v[72:75], v136 offset:51200\n" \
"v_mfma_f32_16x16x32_bf16 a[20:23], v[36:39], v[4:7], a[20:23]\n" \
"v_mfma_f32_16x16x32_bf16 a[24:27], v[36:39], v[8:11], a[24:27]\n" \
"ds_read_b128 v[76:79], v136 offset:52224\n" \
"buffer_load_dwordx4 v129, s[40:43], s52 offen offset:1024 sc1 lds\n" \
"v_mfma_f32_16x16x32_bf16 a[28:31], v[36:39], v[12:15], a[28:31]\n" \
"ds_read_b64_tr_b16 v[100:101], v138 offset:49152\n" \
"v_mfma_f32_16x16x32_bf16 a[32:35], v[40:43], v[0:3], a[32:35]\n" \
"ds_read_b64_tr_b16 v[102:103], v138 offset:57344\n" \
"v_mfma_f32_16x16x32_bf16 a[36:39], v[40:43], v[4:7], a[36:39]\n" \
"ds_read_b64_tr_b16 v[104:105], v139 offset:49152\n" \
"v_mfma_f32_16x16x32_bf16 a[40:43], v[40:43], v[8:11], a[40:43]\n" \
"ds_read_b64_tr_b16 v[106:107], v139 offset:57344\n" \
"v_mfma_f32_16x16x32_bf16 a[44:47], v[40:43], v[12:15], a[44:47]\n" \
"s_add_u32 m0, s57, 32768\n" \
"v_mfma_f32_16x16x32_bf16 a[48:51], v[44:47], v[0:3], a[48:51]\n" \
"ds_read_b64_tr_b16 v[108:109], v140 offset:49152\n" \
"buffer_load_dwordx4 v132, s[44:47], s53 offen offset:0 lds\n" \
"v_mfma_f32_16x16x32_bf16 a[52:55], v[44:47], v[4:7], a[52:55]\n" \
"ds_read_b64_tr_b16 v[110:111], v140 offset:57344\n" \
"v_mfma_f32_16x16x32_bf16 a[56:59], v[44:47], v[8:11], a[56:59]\n" \
"ds_read_b64_tr_b16 v[112:113], v137 offset:50176\n" \
"v_mfma_f32_16x16x32_bf16 a[60:63], v[44:47], v[12:15], a[60:63]\n" \
"ds_read_b64_tr_b16 v[114:115], v137 offset:58368\n" \
"v_mfma_f32_16x16x32_bf16 a[64:67], v[48:51], v[0:3], a[64:67]\n" \
"ds_read_b64_tr_b16 v[116:117], v138 offset:50176\n" \
"v_mfma_f32_16x16x32_bf16 a[68:71], v[48:51], v[4:7], a[68:71]\n" \
"buffer_load_dwordx4 v133, s[48:51], s53 offen offset:1024 lds\n" \
"v_mfma_f32_16x16x32_bf16 a[72:75], v[48:51], v[8:11], a[72:75]\n" \
"ds_read_b64_tr_b16 v[118:119], v138 offset:58368\n" \
"v_mfma_f32_16x16x32_bf16 a[76:79], v[48:51], v[12:15], a[76:79]\n" \
"ds_read_b64_tr_b16 v[120:121], v139 offset:50176\n" \
"v_mfma_f32_16x16x32_bf16 a[80:83], v[52:55], v[0:3], a[80:83]\n" \
"ds_read_b64_tr_b16 v[122:123], v139 offset:58368\n" \
"v_mfma_f32_16x16x32_bf16 a[84:87], v[52:55], v[4:7], a[84:87]\n" \
"ds_read_b64_tr_b16 v[124:125], v140 offset:50176\n" \
"v_mfma_f32_16x16x32_bf16 a[88:91], v[52:55], v[8:11], a[88:91]\n" \
"ds_read_b64_tr_b16 v[126:127], v140 offset:58368\n" \
"v_mfma_f32_16x16x32_bf16 a[92:95], v[52:55], v[12:15], a[92:95]\n" \
"buffer_load_dwordx4 v134, s[44:47], s53 offen offset:2048 lds\n" \
"v_mfma_f32_16x16x32_bf16 a[96:99], v[56:59], v[0:3], a[96:99]\n" \
"v_mfma_f32_16x16x32_bf16 a[100:103], v[56:59], v[4:7], a[100:103]\n" \
"v_mfma_f32_16x16x32_bf16 a[104:107], v[56:59], v[8:11], a[104:107]\n" \
"v_mfma_f32_16x16x32_bf16 a[108:111], v[56:59], v[12:15], a[108:111]\n" \
"v_mfma_f32_16x16x32_bf16 a[112:115], v[60:63], v[0:3], a[112:115]\n" \
"v_mfma_f32_16x16x32_bf16 a[116:119], v[60:63], v[4:7], a[116:119]\n" \
"buffer_load_dwordx4 v135, s[48:51], s53 offen offset:3072 lds\n" \
"v_mfma_f32_16x16x32_bf16 a[120:123], v[60:63], v[8:11], a[120:123]\n" \
"v_mfma_f32_16x16x32_bf16 a[124:127], v[60:63], v[12:15], a[124:127]\n" \
"s_add_u32 s52, s52, 1024\n" \
"s_add_u32 s53, s53, s54\n" \
"s_waitcnt vmcnt(12) lgkmcnt(0)\n" \
"s_barrier\n" \
"v_mfma_f32_16x16x32_bf16 a[0:3], v[96:99], v[64:67], a[0:3]\n" \
"ds_read_b64_tr_b16 v[32:33], v137 offset:0\n" \
"s_add_u32 m0, s56, 49152\n" \
"v_mfma_f32_16x16x32_bf16 a[4:7], v[96:99], v[68:71], a[4:7]\n" \
"ds_read_b64_tr_b16 v[34:35], v137 offset:8192\n" \
"buffer_load_dwordx4 v128, s[40:43], s52 offen offset:0 sc1 lds\n" \
"v_mfma_f32_16x16x32_bf16 a[8:11], v[96:99], v[72:75], a[8:11]\n" \
"ds_read_b128 v[0:3], v136 offset:0\n" \
"v_mfma_f32_16x16x32_bf16 a[12:15], v[96:99], v[76:79], a[12:15]\n" \
"ds_read_b128 v[4:7], v136 offset:1024\n" \
"v_mfma_f32_16x16x32_bf16 a[16:19], v[100:103], v[64:67], a[16:19]\n" \
"ds_read_b128 v[8:11], v136 offset:2048\n" \
"v_mfma_f32_16x16x32_bf16 a[20:23], v[100:103], v[68:71], a[20:23]\n" \
"v_mfma_f32_16x16x32_bf16 a[24:27], v[100:103], v[72:75], a[24:27]\n" \
"ds_read_b128 v[12:15], v136 offset:3072\n" \
"buffer_load_dwordx4 v129, s[40:43], s52 offen offset:1024 sc1 lds\n" \
"v_mfma_f32_16x16x32_bf16 a[28:31], v[100:103], v[76:79], a[28:31]\n" \
"ds_read_b64_tr_b16 v[36:37], v138 offset:0\n" \
"v_mfma_f32_16x16x32_bf16 a[32:35], v[104:107], v[64:67], a[32:35]\n" \
"ds_read_b64_tr_b16 v[38:39], v138 offset:8192\n" \
"v_mfma_f32_16x16x32_bf16 a[36:39], v[104:107], v[68:71], a[36:39]\n" \
"ds_read_b64_tr_b16 v[40:41], v139 offset:0\n" \
"v_mfma_f32_16x16x32_bf16 a[40:43], v[104:107], v[72:75], a[40:43]\n" \
"ds_read_b64_tr_b16 v[42:43], v139 offset:8192\n" \
"v_mfma_f32_16x16x32_bf16 a[44:47], v[104:107], v[76:79], a[44:47]\n" \
"s_add_u32 m0, s57, 49152\n" \
"v_mfma_f32_16x16x32_bf16 a[48:51], v[108:111], v[64:67], a[48:51]\n" \
"ds_read_b64_tr_b16 v[44:45], v140 offset:0\n" \
"buffer_load_dwordx4 v132, s[44:47], s53 offen offset:0 lds\n" \
"v_mfma_f32_16x16x32_bf16 a[52:55], v[108:111], v[68:71], a[52:55]\n" \
"ds_read_b64_tr_b16 v[46:47], v140 offset:8192\n" \
"v_mfma_f32_16x16x32_bf16 a[56:59], v[108:111], v[72:75], a[56:59]\n" \
"ds_read_b64_tr_b16 v[48:49], v137 offset:1024\n" \
"v_mfma_f32_16x16x32_bf16 a[60:63], v[108:111], v[76:79], a[60:63]\n" \
"ds_read_b64_tr_b16 v[50:51], v137 offset:9216\n" \
"v_mfma_f32_16x16x32_bf16 a[64:67], v[112:115], v[64:67], a[64:67]\n" \
"ds_read_b64_tr_b16 v[52:53], v138 offset:1024\n" \
"v_mfma_f32_16x16x32_bf16 a[68:71], v[112:115], v[68:71], a[68:71]\n" \
"buffer_load_dwordx4 v133, s[48:51], s53 offen offset:1024 lds\n" \
"v_mfma_f32_16x16x32_bf16 a[72:75], v[112:115], v[72:75], a[72:75]\n" \
"ds_read_b64_tr_b16 v[54:55], v138 offset:9216\n" \
"v_mfma_f32_16x16x32_bf16 a[76:79], v[112:115], v[76:79], a[76:79]\n" \
"ds_read_b64_tr_b16 v[56:57], v139 offset:1024\n" \
"v_mfma_f32_16x16x32_bf16 a[80:83], v[116:119], v[64:67], a[80:83]\n" \
"ds_read_b64_tr_b16 v[58:59], v139 offset:9216\n" \
"v_mfma_f32_16x16x32_bf16 a[84:87], v[116:119], v[68:71], a[84:87]\n" \
"ds_read_b64_tr_b16 v[60:61], v140 offset:1024\n" \
"v_mfma_f32_16x16x32_bf16 a[88:91], v[116:119], v[72:75], a[88:91]\n" \
"ds_read_b64_tr_b16 v[62:63], v140 offset:9216\n" \
"v_mfma_f32_16x16x32_bf16 a[92:95], v[116:119], v[76:79], a[92:95]\n" \
"buffer_load_dwordx4 v134, s[44:47], s53 offen offset:2048 lds\n" \
"v_mfma_f32_16x16x32_bf16 a[96:99], v[120:123], v[64:67], a[96:99]\n" \
"v_mfma_f32_16x16x32_bf16 a[100:103], v[120:123], v[68:71], a[100:103]\n" \
"v_mfma_f32_16x16x32_bf16 a[104:107], v[120:123], v[72:75], a[104:107]\n" \
"v_mfma_f32_16x16x32_bf16 a[108:111], v[120:123], v[76:79], a[108:111]\n" \
"v_mfma_f32_16x16x32_bf16 a[112:115], v[124:127], v[64:67], a[112:115]\n" \
"v_mfma_f32_16x16x32_bf16 a[116:119], v[124:127], v[68:71], a[116:119]\n" \
"buffer_load_dwordx4 v135, s[48:51], s53 offen offset:3072 lds\n" \
"v_mfma_f32_16x16x32_bf16 a[120:123], v[124:127], v[72:75], a[120:123]\n" \
"v_mfma_f32_16x16x32_bf16 a[124:127], v[124:127], v[76:79], a[124:127]\n" \
"s_add_u32 s52, s52, 1024\n" \
"s_add_u32 s53, s53, s54\n" \
"s_sub_u32 s55, s55, 1\n" \
"s_cmp_lg_u32 s55, 0\n" \
"s_cbranch_scc1 LG48_loop_%=\n" \
"LG48_tail_%=:\n" \
"s_waitcnt vmcnt(12) lgkmcnt(0)\n" \
"s_barrier\n" \
"v_mfma_f32_16x16x32_bf16 a[0:3], v[32:35], v[0:3], a[0:3]\n" \
"ds_read_b64_tr_b16 v[96:97], v137 offset:16384\n" \
"v_mfma_f32_16x16x32_bf16 a[4:7], v[32:35], v[4:7], a[4:7]\n" \
"ds_read_b64_tr_b16 v[98:99], v137 offset:24576\n" \
"v_mfma_f32_16x16x32_bf16 a[8:11], v[32:35], v[8:11], a[8:11]\n" \
"ds_read_b128 v[64:67], v136 offset:16384\n" \
"v_mfma_f32_16x16x32_bf16 a[12:15], v[32:35], v[12:15], a[12:15]\n" \
"ds_read_b128 v[68:71], v136 offset:17408\n" \
"v_mfma_f32_16x16x32_bf16 a[16:19], v[36:39], v[0:3], a[16:19]\n" \
"ds_read_b128 v[72:75], v136 offset:18432\n" \
"v_mfma_f32_16x16x32_bf16 a[20:23], v[36:39], v[4:7], a[20:23]\n" \
"v_mfma_f32_16x16x32_bf16 a[24:27], v[36:39], v[8:11], a[24:27]\n" \
"ds_read_b128 v[76:79], v136 offset:19456\n" \
"v_mfma_f32_16x16x32_bf16 a[28:31], v[36:39], v[12:15], a[28:31]\n" \
"ds_read_b64_tr_b16 v[100:101], v138 offset:16384\n" \
"v_mfma_f32_16x16x32_bf16 a[32:35], v[40:43], v[0:3], a[32:35]\n" \
"ds_read_b64_tr_b16 v[102:103], v138 offset:24576\n" \
"v_mfma_f32_16x16x32_bf16 a[36:39], v[40:43], v[4:7], a[36:39]\n" \
"ds_read_b64_tr_b16 v[104:105], v139 offset:16384\n" \
"v_mfma_f32_16x16x32_bf16 a[40:43], v[40:43], v[8:11], a[40:43]\n" \
"ds_read_b64_tr_b16 v[106:107], v139 offset:24576\n" \
"v_mfma_f32_16x16x32_bf16 a[44:47], v[40:43], v[12:15], a[44:47]\n" \
"v_mfma_f32_16x16x32_bf16 a[48:51], v[44:47], v[0:3], a[48:51]\n" \
"ds_read_b64_tr_b16 v[108:109], v140 offset:16384\n" \
"v_mfma_f32_16x16x32_bf16 a[52:55], v[44:47], v[4:7], a[52:55]\n" \
"ds_read_b64_tr_b16 v[110:111], v140 offset:24576\n" \
"v_mfma_f32_16x16x32_bf16 a[56:59], v[44:47], v[8:11], a[56:59]\n" \
"ds_read_b64_tr_b16 v[112:113], v137 offset:17408\n" \
"v_mfma_f32_16x16x32_bf16 a[60:63], v[44:47], v[12:15], a[60:63]\n" \
"ds_read_b64_tr_b16 v[114:115], v137 offset:25600\n" \
"v_mfma_f32_16x16x32_bf16 a[64:67], v[48:51], v[0:3], a[64:67]\n" \
"ds_read_b64_tr_b16 v[116:117], v138 offset:17408\n" \
"v_mfma_f32_16x16x32_bf16 a[68:71], v[48:51], v[4:7], a[68:71]\n" \
"v_mfma_f32_16x16x32_bf16 a[72:75], v[48:51], v[8:11], a[72:75]\n" \
"ds_read_b64_tr_b16 v[118:119], v138 offset:25600\n" \
"v_mfma_f32_16x16x32_bf16 a[76:79], v[48:51], v[12:15], a[76:79]\n" \
"ds_read_b64_tr_b16 v[120:121], v139 offset:17408\n" \
"v_mfma_f32_16x16x32_bf16 a[80:83], v[52:55], v[0:3], a[80:83]\n" \
"ds_read_b64_tr_b16 v[122:123], v139 offset:25600\n" \
"v_mfma_f32_16x16x32_bf16 a[84:87], v[52:55], v[4:7], a[84:87]\n" \
"ds_read_b64_tr_b16 v[124:125], v140 offset:17408\n" \
"v_mfma_f32_16x16x32_bf16 a[88:91], v[52:55], v[8:11], a[88:91]\n" \
"ds_read_b64_tr_b16 v[126:127], v140 offset:25600\n" \
"v_mfma_f32_16x16x32_bf16 a[92:95], v[52:55], v[12:15], a[92:95]\n" \
"v_mfma_f32_16x16x32_bf16 a[96:99], v[56:59], v[0:3], a[96:99]\n" \
"v_mfma_f32_16x16x32_bf16 a[100:103], v[56:59], v[4:7], a[100:103]\n" \
"v_mfma_f32_16x16x32_bf16 a[104:107], v[56:59], v[8:11], a[104:107]\n" \
"v_mfma_f32_16x16x32_bf16 a[108:111], v[56:59], v[12:15], a[108:111]\n" \
"v_mfma_f32_16x16x32_bf16 a[112:115], v[60:63], v[0:3], a[112:115]\n" \
"v_mfma_f32_16x16x32_bf16 a[116:119], v[60:63], v[4:7], a[116:119]\n" \
"v_mfma_f32_16x16x32_bf16 a[120:123], v[60:63], v[8:11], a[120:123]\n" \
"v_mfma_f32_16x16x32_bf16 a[124:127], v[60:63], v[12:15], a[124:127]\n" \
"s_waitcnt vmcnt(6) lgkmcnt(0)\n" \
"s_barrier\n" \
"v_mfma_f32_16x16x32_bf16 a[0:3], v[96:99], v[64:67], a[0:3]\n" \
"ds_read_b64_tr_b16 v[32:33], v137 offset:32768\n" \
"v_mfma_f32_16x16x32_bf16 a[4:7], v[96:99], v[68:71], a[4:7]\n" \
"ds_read_b64_tr_b16 v[34:35], v137 offset:40960\n" \
"v_mfma_f32_16x16x32_bf16 a[8:11], v[96:99], v[72:75], a[8:11]\n" \
"ds_read_b128 v[0:3], v136 offset:32768\n" \
"v_mfma_f32_16x16x32_bf16 a[12:15], v[96:99], v[76:79], a[12:15]\n" \
"ds_read_b128 v[4:7], v136 offset:33792\n" \
"v_mfma_f32_16x16x32_bf16 a[16:19], v[100:103], v[64:67], a[16:19]\n" \
"ds_read_b128 v[8:11], v136 offset:34816\n" \
"v_mfma_f32_16x16x32_bf16 a[20:23], v[100:103], v[68:71], a[20:23]\n" \
"v_mfma_f32_16x16x32_bf16 a[24:27], v[100:103], v[72:75], a[24:27]\n" \
"ds_read_b128 v[12:15], v136 offset:35840\n" \
"v_mfma_f32_16x16x32_bf16 a[28:31], v[100:103], v[76:79], a[28:31]\n" \
"ds_read_b64_tr_b16 v[36:37], v138 offset:32768\n" \
"v_mfma_f32_16x16x32_bf16 a[32:35], v[104:107], v[64:67], a[32:35]\n" \
"ds_read_b64_tr_b16 v[38:39], v138 offset:40960\n" \
"v_mfma_f32_16x16x32_bf16 a[36:39], v[104:107], v[68:71], a[36:39]\n" \
"ds_read_b64_tr_b16 v[40:41], v139 offset:32768\n" \
"v_mfma_f32_16x16x32_bf16 a[40:43], v[104:107], v[72:75], a[40:43]\n" \
"ds_read_b64_tr_b16 v[42:43], v139 offset:40960\n" \
"v_mfma_f32_16x16x32_bf16 a[44:47], v[104:107], v[76:79], a[44:47]\n" \
"v_mfma_f32_16x16x32_bf16 a[48:51], v[108:111], v[64:67], a[48:51]\n" \
"ds_read_b64_tr_b16 v[44:45], v140 offset:32768\n" \
"v_mfma_f32_16x16x32_bf16 a[52:55], v[108:111], v[68:71], a[52:55]\n" \
"ds_read_b64_tr_b16 v[46:47], v140 offset:40960\n" \
"v_mfma_f32_16x16x32_bf16 a[56:59], v[108:111], v[72:75], a[56:59]\n" \
"ds_read_b64_tr_b16 v[48:49], v137 offset:33792\n" \
"v_mfma_f32_16x16x32_bf16 a[60:63], v[108:111], v[76:79], a[60:63]\n" \
"ds_read_b64_tr_b16 v[50:51], v137 offset:41984\n" \
"v_mfma_f32_16x16x32_bf16 a[64:67], v[112:115], v[64:67], a[64:67]\n" \
"ds_read_b64_tr_b16 v[52:53], v138 offset:33792\n" \
"v_mfma_f32_16x16x32_bf16 a[68:71], v[112:115], v[68:71], a[68:71]\n" \
"v_mfma_f32_16x16x32_bf16 a[72:75], v[112:115], v[72:75], a[72:75]\n" \
"ds_read_b64_tr_b16 v[54:55], v138 offset:41984\n" \
"v_mfma_f32_16x16x32_bf16 a[76:79], v[112:115], v[76:79], a[76:79]\n" \
"ds_read_b64_tr_b16 v[56:57], v139 offset:33792\n" \
"v_mfma_f32_16x16x32_bf16 a[80:83], v[116:119], v[64:67], a[80:83]\n" \
"ds_read_b64_tr_b16 v[58:59], v139 offset:41984\n" \
"v_mfma_f32_16x16x32_bf16 a[84:87], v[116:119], v[68:71], a[84:87]\n" \
"ds_read_b64_tr_b16 v[60:61], v140 offset:33792\n" \
"v_mfma_f32_16x16x32_bf16 a[88:91], v[116:119], v[72:75], a[88:91]\n" \
"ds_read_b64_tr_b16 v[62:63], v140 offset:41984\n" \
"v_mfma_f32_16x16x32_bf16 a[92:95], v[116:119], v[76:79], a[92:95]\n" \
"v_mfma_f32_16x16x32_bf16 a[96:99], v[120:123], v[64:67], a[96:99]\n" \
"v_mfma_f32_16x16x32_bf16 a[100:103], v[120:123], v[68:71], a[100:103]\n" \
"v_mfma_f32_16x16x32_bf16 a[104:107], v[120:123], v[72:75], a[104:107]\n" \
"v_mfma_f32_16x16x32_bf16 a[108:111], v[120:123], v[76:79], a[108:111]\n" \
"v_mfma_f32_16x16x32_bf16 a[112:115], v[124:127], v[64:67], a[112:115]\n" \
"v_mfma_f32_16x16x32_bf16 a[116:119], v[124:127], v[68:71], a[116:119]\n" \
"v_mfma_f32_16x16x32_bf16 a[120:123], v[124:127], v[72:75], a[120:123]\n" \
"v_mfma_f32_16x16x32_bf16 a[124:127], v[124:127], v[76:79], a[124:127]\n" \
"s_waitcnt vmcnt(0) lgkmcnt(0)\n" \
"s_barrier\n" \
"v_mfma_f32_16x16x32_bf16 a[0:3], v[32:35], v[0:3], a[0:3]\n" \
"ds_read_b64_tr_b16 v[96:97], v137 offset:49152\n" \
"v_mfma_f32_16x16x32_bf16 a[4:7], v[32:35], v[4:7], a[4:7]\n" \
"ds_read_b64_tr_b16 v[98:99], v137 offset:57344\n" \
"v_mfma_f32_16x16x32_bf16 a[8:11], v[32:35], v[8:11], a[8:11]\n" \
"ds_read_b128 v[64:67], v136 offset:49152\n" \
"v_mfma_f32_16x16x32_bf16 a[12:15], v[32:35], v[12:15], a[12:15]\n" \
"ds_read_b128 v[68:71], v136 offset:50176\n" \
"v_mfma_f32_16x16x32_bf16 a[16:19], v[36:39], v[0:3], a[16:19]\n" \
"ds_read_b128 v[72:75], v136 offset:51200\n" \
"v_mfma_f32_16x16x32_bf16 a[20:23], v[36:39], v[4:7], a[20:23]\n" \
"v_mfma_f32_16x16x32_bf16 a[24:27], v[36:39], v[8:11], a[24:27]\n" \
"ds_read_b128 v[76:79], v136 offset:52224\n" \
"v_mfma_f32_16x16x32_bf16 a[28:31], v[36:39], v[12:15], a[28:31]\n" \
"ds_read_b64_tr_b16 v[100:101], v138 offset:49152\n" \
"v_mfma_f32_16x16x32_bf16 a[32:35], v[40:43], v[0:3], a[32:35]\n" \
"ds_read_b64_tr_b16 v[102:103], v138 offset:57344\n" \
"v_mfma_f32_16x16x32_bf16 a[36:39], v[40:43], v[4:7], a[36:39]\n" \
"ds_read_b64_tr_b16 v[104:105], v139 offset:49152\n" \
"v_mfma_f32_16x16x32_bf16 a[40:43], v[40:43], v[8:11], a[40:43]\n" \
"ds_read_b64_tr_b16 v[106:107], v139 offset:57344\n" \
"v_mfma_f32_16x16x32_bf16 a[44:47], v[40:43], v[12:15], a[44:47]\n" \
"v_mfma_f32_16x16x32_bf16 a[48:51], v[44:47], v[0:3], a[48:51]\n" \
"ds_read_b64_tr_b16 v[108:109], v140 offset:49152\n" \
"v_mfma_f32_16x16x32_bf16 a[52:55], v[44:47], v[4:7], a[52:55]\n" \
"ds_read_b64_tr_b16 v[110:111], v140 offset:57344\n" \
"v_mfma_f32_16x16x32_bf16 a[56:59], v[44:47], v[8:11], a[56:59]\n" \
"ds_read_b64_tr_b16 v[112:113], v137 offset:50176\n" \
"v_mfma_f32_16x16x32_bf16 a[60:63], v[44:47], v[12:15], a[60:63]\n" \
"ds_read_b64_tr_b16 v[114:115], v137 offset:58368\n" \
"v_mfma_f32_16x16x32_bf16 a[64:67], v[48:51], v[0:3], a[64:67]\n" \
"ds_read_b64_tr_b16 v[116:117], v138 offset:50176\n" \
"v_mfma_f32_16x16x32_bf16 a[68:71], v[48:51], v[4:7], a[68:71]\n" \
"v_mfma_f32_16x16x32_bf16 a[72:75], v[48:51], v[8:11], a[72:75]\n" \
"ds_read_b64_tr_b16 v[118:119], v138 offset:58368\n" \
"v_mfma_f32_16x16x32_bf16 a[76:79], v[48:51], v[12:15], a[76:79]\n" \
"ds_read_b64_tr_b16 v[120:121], v139 offset:50176\n" \
"v_mfma_f32_16x16x32_bf16 a[80:83], v[52:55], v[0:3], a[80:83]\n" \
"ds_read_b64_tr_b16 v[122:123], v139 offset:58368\n" \
"v_mfma_f32_16x16x32_bf16 a[84:87], v[52:55], v[4:7], a[84:87]\n" \
"ds_read_b64_tr_b16 v[124:125], v140 offset:50176\n" \
"v_mfma_f32_16x16x32_bf16 a[88:91], v[52:55], v[8:11], a[88:91]\n" \
"ds_read_b64_tr_b16 v[126:127], v140 offset:58368\n" \
"v_mfma_f32_16x16x32_bf16 a[92:95], v[52:55], v[12:15], a[92:95]\n" \
"v_mfma_f32_16x16x32_bf16 a[96:99], v[56:59], v[0:3], a[96:99]\n" \
"v_mfma_f32_16x16x32_bf16 a[100:103], v[56:59], v[4:7], a[100:103]\n" \
"v_mfma_f32_16x16x32_bf16 a[104:107], v[56:59], v[8:11], a[104:107]\n" \
"v_mfma_f32_16x16x32_bf16 a[108:111], v[56:59], v[12:15], a[108:111]\n" \
"v_mfma_f32_16x16x32_bf16 a[112:115], v[60:63], v[0:3], a[112:115]\n" \
"v_mfma_f32_16x16x32_bf16 a[116:119], v[60:63], v[4:7], a[116:119]\n" \
"v_mfma_f32_16x16x32_bf16 a[120:123], v[60:63], v[8:11], a[120:123]\n" \
"v_mfma_f32_16x16x32_bf16 a[124:127], v[60:63], v[12:15], a[124:127]\n" \
"s_waitcnt lgkmcnt(0)\n" \
"v_mfma_f32_16x16x32_bf16 a[0:3], v[96:99], v[64:67], a[0:3]\n" \
"v_mfma_f32_16x16x32_bf16 a[4:7], v[96:99], v[68:71], a[4:7]\n" \
"v_mfma_f32_16x16x32_bf16 a[8:11], v[96:99], v[72:75], a[8:11]\n" \
"v_mfma_f32_16x16x32_bf16 a[12:15], v[96:99], v[76:79], a[12:15]\n" \
"v_mfma_f32_16x16x32_bf16 a[16:19], v[100:103], v[64:67], a[16:19]\n" \
"v_mfma_f32_16x16x32_bf16 a[20:23], v[100:103], v[68:71], a[20:23]\n" \
"v_mfma_f32_16x16x32_bf16 a[24:27], v[100:103], v[72:75], a[24:27]\n" \
"v_mfma_f32_16x16x32_bf16 a[28:31], v[100:103], v[76:79], a[28:31]\n" \
"v_mfma_f32_16x16x32_bf16 a[32:35], v[104:107], v[64:67], a[32:35]\n" \
"v_mfma_f32_16x16x32_bf16 a[36:39], v[104:107], v[68:71], a[36:39]\n" \
"v_mfma_f32_16x16x32_bf16 a[40:43], v[104:107], v[72:75], a[40:43]\n" \
"v_mfma_f32_16x16x32_bf16 a[44:47], v[104:107], v[76:79], a[44:47]\n" \
"v_mfma_f32_16x16x32_bf16 a[48:51], v[108:111], v[64:67], a[48:51]\n" \
"v_mfma_f32_16x16x32_bf16 a[52:55], v[108:111], v[68:71], a[52:55]\n" \
"v_mfma_f32_16x16x32_bf16 a[56:59], v[108:111], v[72:75], a[56:59]\n" \
"v_mfma_f32_16x16x32_bf16 a[60:63], v[108:111], v[76:79], a[60:63]\n" \
"v_mfma_f32_16x16x32_bf16 a[64:67], v[112:115], v[64:67], a[64:67]\n" \
"v_mfma_f32_16x16x32_bf16 a[68:71], v[112:115], v[68:71], a[68:71]\n" \
"v_mfma_f32_16x16x32_bf16 a[72:75], v[112:115], v[72:75], a[72:75]\n" \
"v_mfma_f32_16x16x32_bf16 a[76:79], v[112:115], v[76:79], a[76:79]\n" \
"v_mfma_f32_16x16x32_bf16 a[80:83], v[116:119], v[64:67], a[80:83]\n" \
"v_mfma_f32_16x16x32_bf16 a[84:87], v[116:119], v[68:71], a[84:87]\n" \
"v_mfma_f32_16x16x32_bf16 a[88:91], v[116:119], v[72:75], a[88:91]\n" \
"v_mfma_f32_16x16x32_bf16 a[92:95], v[116:119], v[76:79], a[92:95]\n" \
"v_mfma_f32_16x16x32_bf16 a[96:99], v[120:123], v[64:67], a[96:99]\n" \
"v_mfma_f32_16x16x32_bf16 a[100:103], v[120:123], v[68:71], a[100:103]\n" \
"v_mfma_f32_16x16x32_bf16 a[104:107], v[120:123], v[72:75], a[104:107]\n" \
"v_mfma_f32_16x16x32_bf16 a[108:111], v[120:123], v[76:79], a[108:111]\n" \
"v_mfma_f32_16x16x32_bf16 a[112:115], v[124:127], v[64:67], a[112:115]\n" \
"v_mfma_f32_16x16x32_bf16 a[116:119], v[124:127], v[68:71], a[116:119]\n" \
"v_mfma_f32_16x16x32_bf16 a[120:123], v[124:127], v[72:75], a[120:123]\n" \
"v_mfma_f32_16x16x32_bf16 a[124:127], v[124:127], v[76:79], a[124:127]\n" \
"s_nop 7\n" \
"LG48_eF_%=:\n" \
"v_accvgpr_read_b32 v0, a0\n" \
"v_accvgpr_read_b32 v1, a1\n" \
"v_accvgpr_read_b32 v2, a2\n" \
"v_accvgpr_read_b32 v3, a3\n" \
"v_accvgpr_read_b32 v4, a16\n" \
"v_accvgpr_read_b32 v5, a17\n" \
"v_accvgpr_read_b32 v6, a18\n" \
"v_accvgpr_read_b32 v7, a19\n" \
"v_cvt_pk_bf16_f32 v0, v0, v1\n" \
"v_cvt_pk_bf16_f32 v1, v2, v3\n" \
"v_cvt_pk_bf16_f32 v2, v4, v5\n" \
"v_cvt_pk_bf16_f32 v3, v6, v7\n" \
"buffer_store_dwordx4 v[0:3], v141, s[60:63], s64 offen offset:0 sc1\n" \
"v_accvgpr_read_b32 v8, a32\n" \
"v_accvgpr_read_b32 v9, a33\n" \
"v_accvgpr_read_b32 v10, a34\n" \
"v_accvgpr_read_b32 v11, a35\n" \
"v_accvgpr_read_b32 v12, a48\n" \
"v_accvgpr_read_b32 v13, a49\n" \
"v_accvgpr_read_b32 v14, a50\n" \
"v_accvgpr_read_b32 v15, a51\n" \
"v_cvt_pk_bf16_f32 v8, v8, v9\n" \
"v_cvt_pk_bf16_f32 v9, v10, v11\n" \
"v_cvt_pk_bf16_f32 v10, v12, v13\n" \
"v_cvt_pk_bf16_f32 v11, v14, v15\n" \
"buffer_store_dwordx4 v[8:11], v141, s[60:63], s64 offen offset:1024 sc1\n" \
"v_accvgpr_read_b32 v16, a64\n" \
"v_accvgpr_read_b32 v17, a65\n" \
"v_accvgpr_read_b32 v18, a66\n" \
"v_accvgpr_read_b32 v19, a67\n" \
"v_accvgpr_read_b32 v20, a80\n" \
"v_accvgpr_read_b32 v21, a81\n" \
"v_accvgpr_read_b32 v22, a82\n" \
"v_accvgpr_read_b32 v23, a83\n" \
"v_cvt_pk_bf16_f32 v16, v16, v17\n" \
"v_cvt_pk_bf16_f32 v17, v18, v19\n" \
"v_cvt_pk_bf16_f32 v18, v20, v21\n" \
"v_cvt_pk_bf16_f32 v19, v22, v23\n" \
"buffer_store_dwordx4 v[16:19], v141, s[60:63], s64 offen offset:2048 sc1\n" \
"v_accvgpr_read_b32 v24, a96\n" \
"v_accvgpr_read_b32 v25, a97\n" \
"v_accvgpr_read_b32 v26, a98\n" \
"v_accvgpr_read_b32 v27, a99\n" \
"v_accvgpr_read_b32 v28, a112\n" \
"v_accvgpr_read_b32 v29, a113\n" \
"v_accvgpr_read_b32 v30, a114\n" \
"v_accvgpr_read_b32 v31, a115\n" \
"v_cvt_pk_bf16_f32 v24, v24, v25\n" \
"v_cvt_pk_bf16_f32 v25, v26, v27\n" \
"v_cvt_pk_bf16_f32 v26, v28, v29\n" \
"v_cvt_pk_bf16_f32 v27, v30, v31\n" \
"buffer_store_dwordx4 v[24:27], v141, s[60:63], s64 offen offset:3072 sc1\n" \
"s_add_u32 s64, s64, s65\n" \
"v_accvgpr_read_b32 v32, a4\n" \
"v_accvgpr_read_b32 v33, a5\n" \
"v_accvgpr_read_b32 v34, a6\n" \
"v_accvgpr_read_b32 v35, a7\n" \
"v_accvgpr_read_b32 v36, a20\n" \
"v_accvgpr_read_b32 v37, a21\n" \
"v_accvgpr_read_b32 v38, a22\n" \
"v_accvgpr_read_b32 v39, a23\n" \
"v_cvt_pk_bf16_f32 v32, v32, v33\n" \
"v_cvt_pk_bf16_f32 v33, v34, v35\n" \
"v_cvt_pk_bf16_f32 v34, v36, v37\n" \
"v_cvt_pk_bf16_f32 v35, v38, v39\n" \
"buffer_store_dwordx4 v[32:35], v141, s[60:63], s64 offen offset:0 sc1\n" \
"v_accvgpr_read_b32 v40, a36\n" \
"v_accvgpr_read_b32 v41, a37\n" \
"v_accvgpr_read_b32 v42, a38\n" \
"v_accvgpr_read_b32 v43, a39\n" \
"v_accvgpr_read_b32 v44, a52\n" \
"v_accvgpr_read_b32 v45, a53\n" \
"v_accvgpr_read_b32 v46, a54\n" \
"v_accvgpr_read_b32 v47, a55\n" \
"v_cvt_pk_bf16_f32 v40, v40, v41\n" \
"v_cvt_pk_bf16_f32 v41, v42, v43\n" \
"v_cvt_pk_bf16_f32 v42, v44, v45\n" \
"v_cvt_pk_bf16_f32 v43, v46, v47\n" \
"buffer_store_dwordx4 v[40:43], v141, s[60:63], s64 offen offset:1024 sc1\n" \
"v_accvgpr_read_b32 v48, a68\n" \
"v_accvgpr_read_b32 v49, a69\n" \
"v_accvgpr_read_b32 v50, a70\n" \
"v_accvgpr_read_b32 v51, a71\n" \
"v_accvgpr_read_b32 v52, a84\n" \
"v_accvgpr_read_b32 v53, a85\n" \
"v_accvgpr_read_b32 v54, a86\n" \
"v_accvgpr_read_b32 v55, a87\n" \
"v_cvt_pk_bf16_f32 v48, v48, v49\n" \
"v_cvt_pk_bf16_f32 v49, v50, v51\n" \
"v_cvt_pk_bf16_f32 v50, v52, v53\n" \
"v_cvt_pk_bf16_f32 v51, v54, v55\n" \
"buffer_store_dwordx4 v[48:51], v141, s[60:63], s64 offen offset:2048 sc1\n" \
"v_accvgpr_read_b32 v56, a100\n" \
"v_accvgpr_read_b32 v57, a101\n" \
"v_accvgpr_read_b32 v58, a102\n" \
"v_accvgpr_read_b32 v59, a103\n" \
"v_accvgpr_read_b32 v60, a116\n" \
"v_accvgpr_read_b32 v61, a117\n" \
"v_accvgpr_read_b32 v62, a118\n" \
"v_accvgpr_read_b32 v63, a119\n" \
"v_cvt_pk_bf16_f32 v56, v56, v57\n" \
"v_cvt_pk_bf16_f32 v57, v58, v59\n" \
"v_cvt_pk_bf16_f32 v58, v60, v61\n" \
"v_cvt_pk_bf16_f32 v59, v62, v63\n" \
"buffer_store_dwordx4 v[56:59], v141, s[60:63], s64 offen offset:3072 sc1\n" \
"s_add_u32 s64, s64, s65\n" \
"v_accvgpr_read_b32 v0, a8\n" \
"v_accvgpr_read_b32 v1, a9\n" \
"v_accvgpr_read_b32 v2, a10\n" \
"v_accvgpr_read_b32 v3, a11\n" \
"v_accvgpr_read_b32 v4, a24\n" \
"v_accvgpr_read_b32 v5, a25\n" \
"v_accvgpr_read_b32 v6, a26\n" \
"v_accvgpr_read_b32 v7, a27\n" \
"v_cvt_pk_bf16_f32 v0, v0, v1\n" \
"v_cvt_pk_bf16_f32 v1, v2, v3\n" \
"v_cvt_pk_bf16_f32 v2, v4, v5\n" \
"v_cvt_pk_bf16_f32 v3, v6, v7\n" \
"buffer_store_dwordx4 v[0:3], v141, s[60:63], s64 offen offset:0 sc1\n" \
"v_accvgpr_read_b32 v8, a40\n" \
"v_accvgpr_read_b32 v9, a41\n" \
"v_accvgpr_read_b32 v10, a42\n" \
"v_accvgpr_read_b32 v11, a43\n" \
"v_accvgpr_read_b32 v12, a56\n" \
"v_accvgpr_read_b32 v13, a57\n" \
"v_accvgpr_read_b32 v14, a58\n" \
"v_accvgpr_read_b32 v15, a59\n" \
"v_cvt_pk_bf16_f32 v8, v8, v9\n" \
"v_cvt_pk_bf16_f32 v9, v10, v11\n" \
"v_cvt_pk_bf16_f32 v10, v12, v13\n" \
"v_cvt_pk_bf16_f32 v11, v14, v15\n" \
"buffer_store_dwordx4 v[8:11], v141, s[60:63], s64 offen offset:1024 sc1\n" \
"v_accvgpr_read_b32 v16, a72\n" \
"v_accvgpr_read_b32 v17, a73\n" \
"v_accvgpr_read_b32 v18, a74\n" \
"v_accvgpr_read_b32 v19, a75\n" \
"v_accvgpr_read_b32 v20, a88\n" \
"v_accvgpr_read_b32 v21, a89\n" \
"v_accvgpr_read_b32 v22, a90\n" \
"v_accvgpr_read_b32 v23, a91\n" \
"v_cvt_pk_bf16_f32 v16, v16, v17\n" \
"v_cvt_pk_bf16_f32 v17, v18, v19\n" \
"v_cvt_pk_bf16_f32 v18, v20, v21\n" \
"v_cvt_pk_bf16_f32 v19, v22, v23\n" \
"buffer_store_dwordx4 v[16:19], v141, s[60:63], s64 offen offset:2048 sc1\n" \
"v_accvgpr_read_b32 v24, a104\n" \
"v_accvgpr_read_b32 v25, a105\n" \
"v_accvgpr_read_b32 v26, a106\n" \
"v_accvgpr_read_b32 v27, a107\n" \
"v_accvgpr_read_b32 v28, a120\n" \
"v_accvgpr_read_b32 v29, a121\n" \
"v_accvgpr_read_b32 v30, a122\n" \
"v_accvgpr_read_b32 v31, a123\n" \
"v_cvt_pk_bf16_f32 v24, v24, v25\n" \
"v_cvt_pk_bf16_f32 v25, v26, v27\n" \
"v_cvt_pk_bf16_f32 v26, v28, v29\n" \
"v_cvt_pk_bf16_f32 v27, v30, v31\n" \
"buffer_store_dwordx4 v[24:27], v141, s[60:63], s64 offen offset:3072 sc1\n" \
"s_add_u32 s64, s64, s65\n" \
"v_accvgpr_read_b32 v32, a12\n" \
"v_accvgpr_read_b32 v33, a13\n" \
"v_accvgpr_read_b32 v34, a14\n" \
"v_accvgpr_read_b32 v35, a15\n" \
"v_accvgpr_read_b32 v36, a28\n" \
"v_accvgpr_read_b32 v37, a29\n" \
"v_accvgpr_read_b32 v38, a30\n" \
"v_accvgpr_read_b32 v39, a31\n" \
"v_cvt_pk_bf16_f32 v32, v32, v33\n" \
"v_cvt_pk_bf16_f32 v33, v34, v35\n" \
"v_cvt_pk_bf16_f32 v34, v36, v37\n" \
"v_cvt_pk_bf16_f32 v35, v38, v39\n" \
"buffer_store_dwordx4 v[32:35], v141, s[60:63], s64 offen offset:0 sc1\n" \
"v_accvgpr_read_b32 v40, a44\n" \
"v_accvgpr_read_b32 v41, a45\n" \
"v_accvgpr_read_b32 v42, a46\n" \
"v_accvgpr_read_b32 v43, a47\n" \
"v_accvgpr_read_b32 v44, a60\n" \
"v_accvgpr_read_b32 v45, a61\n" \
"v_accvgpr_read_b32 v46, a62\n" \
"v_accvgpr_read_b32 v47, a63\n" \
"v_cvt_pk_bf16_f32 v40, v40, v41\n" \
"v_cvt_pk_bf16_f32 v41, v42, v43\n" \
"v_cvt_pk_bf16_f32 v42, v44, v45\n" \
"v_cvt_pk_bf16_f32 v43, v46, v47\n" \
"buffer_store_dwordx4 v[40:43], v141, s[60:63], s64 offen offset:1024 sc1\n" \
"v_accvgpr_read_b32 v48, a76\n" \
"v_accvgpr_read_b32 v49, a77\n" \
"v_accvgpr_read_b32 v50, a78\n" \
"v_accvgpr_read_b32 v51, a79\n" \
"v_accvgpr_read_b32 v52, a92\n" \
"v_accvgpr_read_b32 v53, a93\n" \
"v_accvgpr_read_b32 v54, a94\n" \
"v_accvgpr_read_b32 v55, a95\n" \
"v_cvt_pk_bf16_f32 v48, v48, v49\n" \
"v_cvt_pk_bf16_f32 v49, v50, v51\n" \
"v_cvt_pk_bf16_f32 v50, v52, v53\n" \
"v_cvt_pk_bf16_f32 v51, v54, v55\n" \
"buffer_store_dwordx4 v[48:51], v141, s[60:63], s64 offen offset:2048 sc1\n" \
"v_accvgpr_read_b32 v56, a108\n" \
"v_accvgpr_read_b32 v57, a109\n" \
"v_accvgpr_read_b32 v58, a110\n" \
"v_accvgpr_read_b32 v59, a111\n" \
"v_accvgpr_read_b32 v60, a124\n" \
"v_accvgpr_read_b32 v61, a125\n" \
"v_accvgpr_read_b32 v62, a126\n" \
"v_accvgpr_read_b32 v63, a127\n" \
"v_cvt_pk_bf16_f32 v56, v56, v57\n" \
"v_cvt_pk_bf16_f32 v57, v58, v59\n" \
"v_cvt_pk_bf16_f32 v58, v60, v61\n" \
"v_cvt_pk_bf16_f32 v59, v62, v63\n" \
"buffer_store_dwordx4 v[56:59], v141, s[60:63], s64 offen offset:3072 sc1\n" \
"s_add_u32 s64, s64, s65\n" \
"LG48_end_%=:\n" \
"s_nop 1\n"
#define ASM_G48S \
"s_add_u32 m0, s56, 0\n" \
"s_nop 0\n" \
"buffer_load_dwordx4 v128, s[40:43], s52 offen offset:0 sc1 lds\n" \
"buffer_load_dwordx4 v129, s[40:43], s52 offen offset:1024 sc1 lds\n" \
"s_add_u32 m0, s57, 0\n" \
"s_nop 0\n" \
"buffer_load_dwordx4 v132, s[44:47], s53 offen offset:0 lds\n" \
"buffer_load_dwordx4 v133, s[48:51], s53 offen offset:1024 lds\n" \
"buffer_load_dwordx4 v134, s[44:47], s53 offen offset:2048 lds\n" \
"buffer_load_dwordx4 v135, s[48:51], s53 offen offset:3072 lds\n" \
"s_add_u32 s52, s52, 1024\n" \
"s_add_u32 s53, s53, s54\n" \
"s_add_u32 m0, s56, 16384\n" \
"s_nop 0\n" \
"buffer_load_dwordx4 v128, s[40:43], s52 offen offset:0 sc1 lds\n" \
"buffer_load_dwordx4 v129, s[40:43], s52 offen offset:1024 sc1 lds\n" \
"s_add_u32 m0, s57, 16384\n" \
"s_nop 0\n" \
"buffer_load_dwordx4 v132, s[44:47], s53 offen offset:0 lds\n" \
"buffer_load_dwordx4 v133, s[48:51], s53 offen offset:1024 lds\n" \
"buffer_load_dwordx4 v134, s[44:47], s53 offen offset:2048 lds\n" \
"buffer_load_dwordx4 v135, s[48:51], s53 offen offset:3072 lds\n" \
"s_add_u32 s52, s52, 1024\n" \
"s_add_u32 s53, s53, s54\n" \
"s_add_u32 m0, s56, 32768\n" \
"s_nop 0\n" \
"buffer_load_dwordx4 v128, s[40:43], s52 offen offset:0 sc1 lds\n" \
"buffer_load_dwordx4 v129, s[40:43], s52 offen offset:1024 sc1 lds\n" \
"s_add_u32 m0, s57, 32768\n" \
"s_nop 0\n" \
"buffer_load_dwordx4 v132, s[44:47], s53 offen offset:0 lds\n" \
"buffer_load_dwordx4 v133, s[48:51], s53 offen offset:1024 lds\n" \
"buffer_load_dwordx4 v134, s[44:47], s53 offen offset:2048 lds\n" \
"buffer_load_dwordx4 v135, s[48:51], s53 offen offset:3072 lds\n" \
"s_add_u32 s52, s52, 1024\n" \
"s_add_u32 s53, s53, s54\n" \
"s_add_u32 m0, s56, 49152\n" \
"s_nop 0\n" \
"buffer_load_dwordx4 v128, s[40:43], s52 offen offset:0 sc1 lds\n" \
"buffer_load_dwordx4 v129, s[40:43], s52 offen offset:1024 sc1 lds\n" \
"s_add_u32 m0, s57, 49152\n" \
"s_nop 0\n" \
"buffer_load_dwordx4 v132, s[44:47], s53 offen offset:0 lds\n" \
"buffer_load_dwordx4 v133, s[48:51], s53 offen offset:1024 lds\n" \
"buffer_load_dwordx4 v134, s[44:47], s53 offen offset:2048 lds\n" \
"buffer_load_dwordx4 v135, s[48:51], s53 offen offset:3072 lds\n" \
"s_add_u32 s52, s52, 1024\n" \
"s_add_u32 s53, s53, s54\n" \
"v_accvgpr_write_b32 a0, 0\n" \
"v_accvgpr_write_b32 a1, 0\n" \
"v_accvgpr_write_b32 a2, 0\n" \
"v_accvgpr_write_b32 a3, 0\n" \
"v_accvgpr_write_b32 a4, 0\n" \
"v_accvgpr_write_b32 a5, 0\n" \
"v_accvgpr_write_b32 a6, 0\n" \
"v_accvgpr_write_b32 a7, 0\n" \
"v_accvgpr_write_b32 a8, 0\n" \
"v_accvgpr_write_b32 a9, 0\n" \
"v_accvgpr_write_b32 a10, 0\n" \
"v_accvgpr_write_b32 a11, 0\n" \
"v_accvgpr_write_b32 a12, 0\n" \
"v_accvgpr_write_b32 a13, 0\n" \
"v_accvgpr_write_b32 a14, 0\n" \
"v_accvgpr_write_b32 a15, 0\n" \
"v_accvgpr_write_b32 a16, 0\n" \
"v_accvgpr_write_b32 a17, 0\n" \
"v_accvgpr_write_b32 a18, 0\n" \
"v_accvgpr_write_b32 a19, 0\n" \
"v_accvgpr_write_b32 a20, 0\n" \
"v_accvgpr_write_b32 a21, 0\n" \
"v_accvgpr_write_b32 a22, 0\n" \
"v_accvgpr_write_b32 a23, 0\n" \
"v_accvgpr_write_b32 a24, 0\n" \
"v_accvgpr_write_b32 a25, 0\n" \
"v_accvgpr_write_b32 a26, 0\n" \
"v_accvgpr_write_b32 a27, 0\n" \
"v_accvgpr_write_b32 a28, 0\n" \
"v_accvgpr_write_b32 a29, 0\n" \
"v_accvgpr_write_b32 a30, 0\n" \
"v_accvgpr_write_b32 a31, 0\n" \
"v_accvgpr_write_b32 a32, 0\n" \
"v_accvgpr_write_b32 a33, 0\n" \
"v_accvgpr_write_b32 a34, 0\n" \
"v_accvgpr_write_b32 a35, 0\n" \
"v_accvgpr_write_b32 a36, 0\n" \
"v_accvgpr_write_b32 a37, 0\n" \
"v_accvgpr_write_b32 a38, 0\n" \
"v_accvgpr_write_b32 a39, 0\n" \
"v_accvgpr_write_b32 a40, 0\n" \
"v_accvgpr_write_b32 a41, 0\n" \
"v_accvgpr_write_b32 a42, 0\n" \
"v_accvgpr_write_b32 a43, 0\n" \
"v_accvgpr_write_b32 a44, 0\n" \
"v_accvgpr_write_b32 a45, 0\n" \
"v_accvgpr_write_b32 a46, 0\n" \
"v_accvgpr_write_b32 a47, 0\n" \
"v_accvgpr_write_b32 a48, 0\n" \
"v_accvgpr_write_b32 a49, 0\n" \
"v_accvgpr_write_b32 a50, 0\n" \
"v_accvgpr_write_b32 a51, 0\n" \
"v_accvgpr_write_b32 a52, 0\n" \
"v_accvgpr_write_b32 a53, 0\n" \
"v_accvgpr_write_b32 a54, 0\n" \
"v_accvgpr_write_b32 a55, 0\n" \
"v_accvgpr_write_b32 a56, 0\n" \
"v_accvgpr_write_b32 a57, 0\n" \
"v_accvgpr_write_b32 a58, 0\n" \
"v_accvgpr_write_b32 a59, 0\n" \
"v_accvgpr_write_b32 a60, 0\n" \
"v_accvgpr_write_b32 a61, 0\n" \
"v_accvgpr_write_b32 a62, 0\n" \
"v_accvgpr_write_b32 a63, 0\n" \
"v_accvgpr_write_b32 a64, 0\n" \
"v_accvgpr_write_b32 a65, 0\n" \
"v_accvgpr_write_b32 a66, 0\n" \
"v_accvgpr_write_b32 a67, 0\n" \
"v_accvgpr_write_b32 a68, 0\n" \
"v_accvgpr_write_b32 a69, 0\n" \
"v_accvgpr_write_b32 a70, 0\n" \
"v_accvgpr_write_b32 a71, 0\n" \
"v_accvgpr_write_b32 a72, 0\n" \
"v_accvgpr_write_b32 a73, 0\n" \
"v_accvgpr_write_b32 a74, 0\n" \
"v_accvgpr_write_b32 a75, 0\n" \
"v_accvgpr_write_b32 a76, 0\n" \
"v_accvgpr_write_b32 a77, 0\n" \
"v_accvgpr_write_b32 a78, 0\n" \
"v_accvgpr_write_b32 a79, 0\n" \
"v_accvgpr_write_b32 a80, 0\n" \
"v_accvgpr_write_b32 a81, 0\n" \
"v_accvgpr_write_b32 a82, 0\n" \
"v_accvgpr_write_b32 a83, 0\n" \
"v_accvgpr_write_b32 a84, 0\n" \
"v_accvgpr_write_b32 a85, 0\n" \
"v_accvgpr_write_b32 a86, 0\n" \
"v_accvgpr_write_b32 a87, 0\n" \
"v_accvgpr_write_b32 a88, 0\n" \
"v_accvgpr_write_b32 a89, 0\n" \
"v_accvgpr_write_b32 a90, 0\n" \
"v_accvgpr_write_b32 a91, 0\n" \
"v_accvgpr_write_b32 a92, 0\n" \
"v_accvgpr_write_b32 a93, 0\n" \
"v_accvgpr_write_b32 a94, 0\n" \
"v_accvgpr_write_b32 a95, 0\n" \
"v_accvgpr_write_b32 a96, 0\n" \
"v_accvgpr_write_b32 a97, 0\n" \
"v_accvgpr_write_b32 a98, 0\n" \
"v_accvgpr_write_b32 a99, 0\n" \
"v_accvgpr_write_b32 a100, 0\n" \
"v_accvgpr_write_b32 a101, 0\n" \
"v_accvgpr_write_b32 a102, 0\n" \
"v_accvgpr_write_b32 a103, 0\n" \
"v_accvgpr_write_b32 a104, 0\n" \
"v_accvgpr_write_b32 a105, 0\n" \
"v_accvgpr_write_b32 a106, 0\n" \
"v_accvgpr_write_b32 a107, 0\n" \
"v_accvgpr_write_b32 a108, 0\n" \
"v_accvgpr_write_b32 a109, 0\n" \
"v_accvgpr_write_b32 a110, 0\n" \
"v_accvgpr_write_b32 a111, 0\n" \
"v_accvgpr_write_b32 a112, 0\n" \
"v_accvgpr_write_b32 a113, 0\n" \
"v_accvgpr_write_b32 a114, 0\n" \
"v_accvgpr_write_b32 a115, 0\n" \
"v_accvgpr_write_b32 a116, 0\n" \
"v_accvgpr_write_b32 a117, 0\n" \
"v_accvgpr_write_b32 a118, 0\n" \
"v_accvgpr_write_b32 a119, 0\n" \
"v_accvgpr_write_b32 a120, 0\n" \
"v_accvgpr_write_b32 a121, 0\n" \
"v_accvgpr_write_b32 a122, 0\n" \
"v_accvgpr_write_b32 a123, 0\n" \
"v_accvgpr_write_b32 a124, 0\n" \
"v_accvgpr_write_b32 a125, 0\n" \
"v_accvgpr_write_b32 a126, 0\n" \
"v_accvgpr_write_b32 a127, 0\n" \
"s_waitcnt vmcnt(18)\n" \
"s_barrier\n" \
"ds_read_b64_tr_b16 v[32:33], v137 offset:0\n" \
"ds_read_b64_tr_b16 v[34:35], v137 offset:8192\n" \
"ds_read_b128 v[0:3], v136 offset:0\n" \
"ds_read_b128 v[4:7], v136 offset:1024\n" \
"ds_read_b128 v[8:11], v136 offset:2048\n" \
"ds_read_b128 v[12:15], v136 offset:3072\n" \
"ds_read_b64_tr_b16 v[36:37], v138 offset:0\n" \
"ds_read_b64_tr_b16 v[38:39], v138 offset:8192\n" \
"ds_read_b64_tr_b16 v[40:41], v139 offset:0\n" \
"ds_read_b64_tr_b16 v[42:43], v139 offset:8192\n" \
"ds_read_b64_tr_b16 v[44:45], v140 offset:0\n" \
"ds_read_b64_tr_b16 v[46:47], v140 offset:8192\n" \
"ds_read_b64_tr_b16 v[48:49], v137 offset:1024\n" \
"ds_read_b64_tr_b16 v[50:51], v137 offset:9216\n" \
"ds_read_b64_tr_b16 v[52:53], v138 offset:1024\n" \
"ds_read_b64_tr_b16 v[54:55], v138 offset:9216\n" \
"ds_read_b64_tr_b16 v[56:57], v139 offset:1024\n" \
"ds_read_b64_tr_b16 v[58:59], v139 offset:9216\n" \
"ds_read_b64_tr_b16 v[60:61], v140 offset:1024\n" \
"ds_read_b64_tr_b16 v[62:63], v140 offset:9216\n" \
"s_cmp_eq_u32 s55, 0\n" \
"s_cbranch_scc1 LG48S_tail_%=\n" \
"LG48S_loop_%=:\n" \
"s_waitcnt vmcnt(12) lgkmcnt(0)\n" \
"s_barrier\n" \
"v_mfma_f32_16x16x32_bf16 a[0:3], v[0:3], v[32:35], a[0:3]\n" \
"ds_read_b64_tr_b16 v[96:97], v137 offset:16384\n" \
"s_add_u32 m0, s56, 0\n" \
"v_mfma_f32_16x16x32_bf16 a[4:7], v[4:7], v[32:35], a[4:7]\n" \
"ds_read_b64_tr_b16 v[98:99], v137 offset:24576\n" \
"buffer_load_dwordx4 v128, s[40:43], s52 offen offset:0 sc1 lds\n" \
"v_mfma_f32_16x16x32_bf16 a[8:11], v[8:11], v[32:35], a[8:11]\n" \
"ds_read_b128 v[64:67], v136 offset:16384\n" \
"v_mfma_f32_16x16x32_bf16 a[12:15], v[12:15], v[32:35], a[12:15]\n" \
"ds_read_b128 v[68:71], v136 offset:17408\n" \
"v_mfma_f32_16x16x32_bf16 a[16:19], v[0:3], v[36:39], a[16:19]\n" \
"ds_read_b128 v[72:75], v136 offset:18432\n" \
"v_mfma_f32_16x16x32_bf16 a[20:23], v[4:7], v[36:39], a[20:23]\n" \
"v_mfma_f32_16x16x32_bf16 a[24:27], v[8:11], v[36:39], a[24:27]\n" \
"ds_read_b128 v[76:79], v136 offset:19456\n" \
"buffer_load_dwordx4 v129, s[40:43], s52 offen offset:1024 sc1 lds\n" \
"v_mfma_f32_16x16x32_bf16 a[28:31], v[12:15], v[36:39], a[28:31]\n" \
"ds_read_b64_tr_b16 v[100:101], v138 offset:16384\n" \
"v_mfma_f32_16x16x32_bf16 a[32:35], v[0:3], v[40:43], a[32:35]\n" \
"ds_read_b64_tr_b16 v[102:103], v138 offset:24576\n" \
"v_mfma_f32_16x16x32_bf16 a[36:39], v[4:7], v[40:43], a[36:39]\n" \
"ds_read_b64_tr_b16 v[104:105], v139 offset:16384\n" \
"v_mfma_f32_16x16x32_bf16 a[40:43], v[8:11], v[40:43], a[40:43]\n" \
"ds_read_b64_tr_b16 v[106:107], v139 offset:24576\n" \
"v_mfma_f32_16x16x32_bf16 a[44:47], v[12:15], v[40:43], a[44:47]\n" \
"s_add_u32 m0, s57, 0\n" \
"v_mfma_f32_16x16x32_bf16 a[48:51], v[0:3], v[44:47], a[48:51]\n" \
"ds_read_b64_tr_b16 v[108:109], v140 offset:16384\n" \
"buffer_load_dwordx4 v132, s[44:47], s53 offen offset:0 lds\n" \
"v_mfma_f32_16x16x32_bf16 a[52:55], v[4:7], v[44:47], a[52:55]\n" \
"ds_read_b64_tr_b16 v[110:111], v140 offset:24576\n" \
"v_mfma_f32_16x16x32_bf16 a[56:59], v[8:11], v[44:47], a[56:59]\n" \
"ds_read_b64_tr_b16 v[112:113], v137 offset:17408\n" \
"v_mfma_f32_16x16x32_bf16 a[60:63], v[12:15], v[44:47], a[60:63]\n" \
"ds_read_b64_tr_b16 v[114:115], v137 offset:25600\n" \
"v_mfma_f32_16x16x32_bf16 a[64:67], v[0:3], v[48:51], a[64:67]\n" \
"ds_read_b64_tr_b16 v[116:117], v138 offset:17408\n" \
"v_mfma_f32_16x16x32_bf16 a[68:71], v[4:7], v[48:51], a[68:71]\n" \
"buffer_load_dwordx4 v133, s[48:51], s53 offen offset:1024 lds\n" \
"v_mfma_f32_16x16x32_bf16 a[72:75], v[8:11], v[48:51], a[72:75]\n" \
"ds_read_b64_tr_b16 v[118:119], v138 offset:25600\n" \
"v_mfma_f32_16x16x32_bf16 a[76:79], v[12:15], v[48:51], a[76:79]\n" \
"ds_read_b64_tr_b16 v[120:121], v139 offset:17408\n" \
"v_mfma_f32_16x16x32_bf16 a[80:83], v[0:3], v[52:55], a[80:83]\n" \
"ds_read_b64_tr_b16 v[122:123], v139 offset:25600\n" \
"v_mfma_f32_16x16x32_bf16 a[84:87], v[4:7], v[52:55], a[84:87]\n" \
"ds_read_b64_tr_b16 v[124:125], v140 offset:17408\n" \
"v_mfma_f32_16x16x32_bf16 a[88:91], v[8:11], v[52:55], a[88:91]\n" \
"ds_read_b64_tr_b16 v[126:127], v140 offset:25600\n" \
"v_mfma_f32_16x16x32_bf16 a[92:95], v[12:15], v[52:55], a[92:95]\n" \
"buffer_load_dwordx4 v134, s[44:47], s53 offen offset:2048 lds\n" \
"v_mfma_f32_16x16x32_bf16 a[96:99], v[0:3], v[56:59], a[96:99]\n" \
"v_mfma_f32_16x16x32_bf16 a[100:103], v[4:7], v[56:59], a[100:103]\n" \
"v_mfma_f32_16x16x32_bf16 a[104:107], v[8:11], v[56:59], a[104:107]\n" \
"v_mfma_f32_16x16x32_bf16 a[108:111], v[12:15], v[56:59], a[108:111]\n" \
"v_mfma_f32_16x16x32_bf16 a[112:115], v[0:3], v[60:63], a[112:115]\n" \
"v_mfma_f32_16x16x32_bf16 a[116:119], v[4:7], v[60:63], a[116:119]\n" \
"buffer_load_dwordx4 v135, s[48:51], s53 offen offset:3072 lds\n" \
"v_mfma_f32_16x16x32_bf16 a[120:123], v[8:11], v[60:63], a[120:123]\n" \
"v_mfma_f32_16x16x32_bf16 a[124:127], v[12:15], v[60:63], a[124:127]\n" \
"s_add_u32 s52, s52, 1024\n" \
"s_add_u32 s53, s53, s54\n" \
"s_waitcnt vmcnt(12) lgkmcnt(0)\n" \
"s_barrier\n" \
"v_mfma_f32_16x16x32_bf16 a[0:3], v[64:67], v[96:99], a[0:3]\n" \
"ds_read_b64_tr_b16 v[32:33], v137 offset:32768\n" \
"s_add_u32 m0, s56, 16384\n" \
"v_mfma_f32_16x16x32_bf16 a[4:7], v[68:71], v[96:99], a[4:7]\n" \
"ds_read_b64_tr_b16 v[34:35], v137 offset:40960\n" \
"buffer_load_dwordx4 v128, s[40:43], s52 offen offset:0 sc1 lds\n" \
"v_mfma_f32_16x16x32_bf16 a[8:11], v[72:75], v[96:99], a[8:11]\n" \
"ds_read_b128 v[0:3], v136 offset:32768\n" \
"v_mfma_f32_16x16x32_bf16 a[12:15], v[76:79], v[96:99], a[12:15]\n" \
"ds_read_b128 v[4:7], v136 offset:33792\n" \
"v_mfma_f32_16x16x32_bf16 a[16:19], v[64:67], v[100:103], a[16:19]\n" \
"ds_read_b128 v[8:11], v136 offset:34816\n" \
"v_mfma_f32_16x16x32_bf16 a[20:23], v[68:71], v[100:103], a[20:23]\n" \
"v_mfma_f32_16x16x32_bf16 a[24:27], v[72:75], v[100:103], a[24:27]\n" \
"ds_read_b128 v[12:15], v136 offset:35840\n" \
"buffer_load_dwordx4 v129, s[40:43], s52 offen offset:1024 sc1 lds\n" \
"v_mfma_f32_16x16x32_bf16 a[28:31], v[76:79], v[100:103], a[28:31]\n" \
"ds_read_b64_tr_b16 v[36:37], v138 offset:32768\n" \
"v_mfma_f32_16x16x32_bf16 a[32:35], v[64:67], v[104:107], a[32:35]\n" \
"ds_read_b64_tr_b16 v[38:39], v138 offset:40960\n" \
"v_mfma_f32_16x16x32_bf16 a[36:39], v[68:71], v[104:107], a[36:39]\n" \
"ds_read_b64_tr_b16 v[40:41], v139 offset:32768\n" \
"v_mfma_f32_16x16x32_bf16 a[40:43], v[72:75], v[104:107], a[40:43]\n" \
"ds_read_b64_tr_b16 v[42:43], v139 offset:40960\n" \
"v_mfma_f32_16x16x32_bf16 a[44:47], v[76:79], v[104:107], a[44:47]\n" \
"s_add_u32 m0, s57, 16384\n" \
"v_mfma_f32_16x16x32_bf16 a[48:51], v[64:67], v[108:111], a[48:51]\n" \
"ds_read_b64_tr_b16 v[44:45], v140 offset:32768\n" \
"buffer_load_dwordx4 v132, s[44:47], s53 offen offset:0 lds\n" \
"v_mfma_f32_16x16x32_bf16 a[52:55], v[68:71], v[108:111], a[52:55]\n" \
"ds_read_b64_tr_b16 v[46:47], v140 offset:40960\n" \
"v_mfma_f32_16x16x32_bf16 a[56:59], v[72:75], v[108:111], a[56:59]\n" \
"ds_read_b64_tr_b16 v[48:49], v137 offset:33792\n" \
"v_mfma_f32_16x16x32_bf16 a[60:63], v[76:79], v[108:111], a[60:63]\n" \
"ds_read_b64_tr_b16 v[50:51], v137 offset:41984\n" \
"v_mfma_f32_16x16x32_bf16 a[64:67], v[64:67], v[112:115], a[64:67]\n" \
"ds_read_b64_tr_b16 v[52:53], v138 offset:33792\n" \
"v_mfma_f32_16x16x32_bf16 a[68:71], v[68:71], v[112:115], a[68:71]\n" \
"buffer_load_dwordx4 v133, s[48:51], s53 offen offset:1024 lds\n" \
"v_mfma_f32_16x16x32_bf16 a[72:75], v[72:75], v[112:115], a[72:75]\n" \
"ds_read_b64_tr_b16 v[54:55], v138 offset:41984\n" \
"v_mfma_f32_16x16x32_bf16 a[76:79], v[76:79], v[112:115], a[76:79]\n" \
"ds_read_b64_tr_b16 v[56:57], v139 offset:33792\n" \
"v_mfma_f32_16x16x32_bf16 a[80:83], v[64:67], v[116:119], a[80:83]\n" \
"ds_read_b64_tr_b16 v[58:59], v139 offset:41984\n" \
"v_mfma_f32_16x16x32_bf16 a[84:87], v[68:71], v[116:119], a[84:87]\n" \
"ds_read_b64_tr_b16 v[60:61], v140 offset:33792\n" \
"v_mfma_f32_16x16x32_bf16 a[88:91], v[72:75], v[116:119], a[88:91]\n" \
"ds_read_b64_tr_b16 v[62:63], v140 offset:41984\n" \
"v_mfma_f32_16x16x32_bf16 a[92:95], v[76:79], v[116:119], a[92:95]\n" \
"buffer_load_dwordx4 v134, s[44:47], s53 offen offset:2048 lds\n" \
"v_mfma_f32_16x16x32_bf16 a[96:99], v[64:67], v[120:123], a[96:99]\n" \
"v_mfma_f32_16x16x32_bf16 a[100:103], v[68:71], v[120:123], a[100:103]\n" \
"v_mfma_f32_16x16x32_bf16 a[104:107], v[72:75], v[120:123], a[104:107]\n" \
"v_mfma_f32_16x16x32_bf16 a[108:111], v[76:79], v[120:123], a[108:111]\n" \
"v_mfma_f32_16x16x32_bf16 a[112:115], v[64:67], v[124:127], a[112:115]\n" \
"v_mfma_f32_16x16x32_bf16 a[116:119], v[68:71], v[124:127], a[116:119]\n" \
"buffer_load_dwordx4 v135, s[48:51], s53 offen offset:3072 lds\n" \
"v_mfma_f32_16x16x32_bf16 a[120:123], v[72:75], v[124:127], a[120:123]\n" \
"v_mfma_f32_16x16x32_bf16 a[124:127], v[76:79], v[124:127], a[124:127]\n" \
"s_add_u32 s52, s52, 1024\n" \
"s_add_u32 s53, s53, s54\n" \
"s_waitcnt vmcnt(12) lgkmcnt(0)\n" \
"s_barrier\n" \
"v_mfma_f32_16x16x32_bf16 a[0:3], v[0:3], v[32:35], a[0:3]\n" \
"ds_read_b64_tr_b16 v[96:97], v137 offset:49152\n" \
"s_add_u32 m0, s56, 32768\n" \
"v_mfma_f32_16x16x32_bf16 a[4:7], v[4:7], v[32:35], a[4:7]\n" \
"ds_read_b64_tr_b16 v[98:99], v137 offset:57344\n" \
"buffer_load_dwordx4 v128, s[40:43], s52 offen offset:0 sc1 lds\n" \
"v_mfma_f32_16x16x32_bf16 a[8:11], v[8:11], v[32:35], a[8:11]\n" \
"ds_read_b128 v[64:67], v136 offset:49152\n" \
"v_mfma_f32_16x16x32_bf16 a[12:15], v[12:15], v[32:35], a[12:15]\n" \
"ds_read_b128 v[68:71], v136 offset:50176\n" \
"v_mfma_f32_16x16x32_bf16 a[16:19], v[0:3], v[36:39], a[16:19]\n" \
"ds_read_b128 v[72:75], v136 offset:51200\n" \
"v_mfma_f32_16x16x32_bf16 a[20:23], v[4:7], v[36:39], a[20:23]\n" \
"v_mfma_f32_16x16x32_bf16 a[24:27], v[8:11], v[36:39], a[24:27]\n" \
"ds_read_b128 v[76:79], v136 offset:52224\n" \
"buffer_load_dwordx4 v129, s[40:43], s52 offen offset:1024 sc1 lds\n" \
"v_mfma_f32_16x16x32_bf16 a[28:31], v[12:15], v[36:39], a[28:31]\n" \
"ds_read_b64_tr_b16 v[100:101], v138 offset:49152\n" \
"v_mfma_f32_16x16x32_bf16 a[32:35], v[0:3], v[40:43], a[32:35]\n" \
"ds_read_b64_tr_b16 v[102:103], v138 offset:57344\n" \
"v_mfma_f32_16x16x32_bf16 a[36:39], v[4:7], v[40:43], a[36:39]\n" \
"ds_read_b64_tr_b16 v[104:105], v139 offset:49152\n" \
"v_mfma_f32_16x16x32_bf16 a[40:43], v[8:11], v[40:43], a[40:43]\n" \
"ds_read_b64_tr_b16 v[106:107], v139 offset:57344\n" \
"v_mfma_f32_16x16x32_bf16 a[44:47], v[12:15], v[40:43], a[44:47]\n" \
"s_add_u32 m0, s57, 32768\n" \
"v_mfma_f32_16x16x32_bf16 a[48:51], v[0:3], v[44:47], a[48:51]\n" \
"ds_read_b64_tr_b16 v[108:109], v140 offset:49152\n" \
"buffer_load_dwordx4 v132, s[44:47], s53 offen offset:0 lds\n" \
"v_mfma_f32_16x16x32_bf16 a[52:55], v[4:7], v[44:47], a[52:55]\n" \
"ds_read_b64_tr_b16 v[110:111], v140 offset:57344\n" \
"v_mfma_f32_16x16x32_bf16 a[56:59], v[8:11], v[44:47], a[56:59]\n" \
"ds_read_b64_tr_b16 v[112:113], v137 offset:50176\n" \
"v_mfma_f32_16x16x32_bf16 a[60:63], v[12:15], v[44:47], a[60:63]\n" \
"ds_read_b64_tr_b16 v[114:115], v137 offset:58368\n" \
"v_mfma_f32_16x16x32_bf16 a[64:67], v[0:3], v[48:51], a[64:67]\n" \
"ds_read_b64_tr_b16 v[116:117], v138 offset:50176\n" \
"v_mfma_f32_16x16x32_bf16 a[68:71], v[4:7], v[48:51], a[68:71]\n" \
"buffer_load_dwordx4 v133, s[48:51], s53 offen offset:1024 lds\n" \
"v_mfma_f32_16x16x32_bf16 a[72:75], v[8:11], v[48:51], a[72:75]\n" \
"ds_read_b64_tr_b16 v[118:119], v138 offset:58368\n" \
"v_mfma_f32_16x16x32_bf16 a[76:79], v[12:15], v[48:51], a[76:79]\n" \
"ds_read_b64_tr_b16 v[120:121], v139 offset:50176\n" \
"v_mfma_f32_16x16x32_bf16 a[80:83], v[0:3], v[52:55], a[80:83]\n" \
"ds_read_b64_tr_b16 v[122:123], v139 offset:58368\n" \
"v_mfma_f32_16x16x32_bf16 a[84:87], v[4:7], v[52:55], a[84:87]\n" \
"ds_read_b64_tr_b16 v[124:125], v140 offset:50176\n" \
"v_mfma_f32_16x16x32_bf16 a[88:91], v[8:11], v[52:55], a[88:91]\n" \
"ds_read_b64_tr_b16 v[126:127], v140 offset:58368\n" \
"v_mfma_f32_16x16x32_bf16 a[92:95], v[12:15], v[52:55], a[92:95]\n" \
"buffer_load_dwordx4 v134, s[44:47], s53 offen offset:2048 lds\n" \
"v_mfma_f32_16x16x32_bf16 a[96:99], v[0:3], v[56:59], a[96:99]\n" \
"v_mfma_f32_16x16x32_bf16 a[100:103], v[4:7], v[56:59], a[100:103]\n" \
"v_mfma_f32_16x16x32_bf16 a[104:107], v[8:11], v[56:59], a[104:107]\n" \
"v_mfma_f32_16x16x32_bf16 a[108:111], v[12:15], v[56:59], a[108:111]\n" \
"v_mfma_f32_16x16x32_bf16 a[112:115], v[0:3], v[60:63], a[112:115]\n" \
"v_mfma_f32_16x16x32_bf16 a[116:119], v[4:7], v[60:63], a[116:119]\n" \
"buffer_load_dwordx4 v135, s[48:51], s53 offen offset:3072 lds\n" \
"v_mfma_f32_16x16x32_bf16 a[120:123], v[8:11], v[60:63], a[120:123]\n" \
"v_mfma_f32_16x16x32_bf16 a[124:127], v[12:15], v[60:63], a[124:127]\n" \
"s_add_u32 s52, s52, 1024\n" \
"s_add_u32 s53, s53, s54\n" \
"s_waitcnt vmcnt(12) lgkmcnt(0)\n" \
"s_barrier\n" \
"v_mfma_f32_16x16x32_bf16 a[0:3], v[64:67], v[96:99], a[0:3]\n" \
"ds_read_b64_tr_b16 v[32:33], v137 offset:0\n" \
"s_add_u32 m0, s56, 49152\n" \
"v_mfma_f32_16x16x32_bf16 a[4:7], v[68:71], v[96:99], a[4:7]\n" \
"ds_read_b64_tr_b16 v[34:35], v137 offset:8192\n" \
"buffer_load_dwordx4 v128, s[40:43], s52 offen offset:0 sc1 lds\n" \
"v_mfma_f32_16x16x32_bf16 a[8:11], v[72:75], v[96:99], a[8:11]\n" \
"ds_read_b128 v[0:3], v136 offset:0\n" \
"v_mfma_f32_16x16x32_bf16 a[12:15], v[76:79], v[96:99], a[12:15]\n" \
"ds_read_b128 v[4:7], v136 offset:1024\n" \
"v_mfma_f32_16x16x32_bf16 a[16:19], v[64:67], v[100:103], a[16:19]\n" \
"ds_read_b128 v[8:11], v136 offset:2048\n" \
"v_mfma_f32_16x16x32_bf16 a[20:23], v[68:71], v[100:103], a[20:23]\n" \
"v_mfma_f32_16x16x32_bf16 a[24:27], v[72:75], v[100:103], a[24:27]\n" \
"ds_read_b128 v[12:15], v136 offset:3072\n" \
"buffer_load_dwordx4 v129, s[40:43], s52 offen offset:1024 sc1 lds\n" \
"v_mfma_f32_16x16x32_bf16 a[28:31], v[76:79], v[100:103], a[28:31]\n" \
"ds_read_b64_tr_b16 v[36:37], v138 offset:0\n" \
"v_mfma_f32_16x16x32_bf16 a[32:35], v[64:67], v[104:107], a[32:35]\n" \
"ds_read_b64_tr_b16 v[38:39], v138 offset:8192\n" \
"v_mfma_f32_16x16x32_bf16 a[36:39], v[68:71], v[104:107], a[36:39]\n" \
"ds_read_b64_tr_b16 v[40:41], v139 offset:0\n" \
"v_mfma_f32_16x16x32_bf16 a[40:43], v[72:75], v[104:107], a[40:43]\n" \
"ds_read_b64_tr_b16 v[42:43], v139 offset:8192\n" \
"v_mfma_f32_16x16x32_bf16 a[44:47], v[76:79], v[104:107], a[44:47]\n" \
"s_add_u32 m0, s57, 49152\n" \
"v_mfma_f32_16x16x32_bf16 a[48:51], v[64:67], v[108:111], a[48:51]\n" \
"ds_read_b64_tr_b16 v[44:45], v140 offset:0\n" \
"buffer_load_dwordx4 v132, s[44:47], s53 offen offset:0 lds\n" \
"v_mfma_f32_16x16x32_bf16 a[52:55], v[68:71], v[108:111], a[52:55]\n" \
"ds_read_b64_tr_b16 v[46:47], v140 offset:8192\n" \
"v_mfma_f32_16x16x32_bf16 a[56:59], v[72:75], v[108:111], a[56:59]\n" \
"ds_read_b64_tr_b16 v[48:49], v137 offset:1024\n" \
"v_mfma_f32_16x16x32_bf16 a[60:63], v[76:79], v[108:111], a[60:63]\n" \
"ds_read_b64_tr_b16 v[50:51], v137 offset:9216\n" \
"v_mfma_f32_16x16x32_bf16 a[64:67], v[64:67], v[112:115], a[64:67]\n" \
"ds_read_b64_tr_b16 v[52:53], v138 offset:1024\n" \
"v_mfma_f32_16x16x32_bf16 a[68:71], v[68:71], v[112:115], a[68:71]\n" \
"buffer_load_dwordx4 v133, s[48:51], s53 offen offset:1024 lds\n" \
"v_mfma_f32_16x16x32_bf16 a[72:75], v[72:75], v[112:115], a[72:75]\n" \
"ds_read_b64_tr_b16 v[54:55], v138 offset:9216\n" \
"v_mfma_f32_16x16x32_bf16 a[76:79], v[76:79], v[112:115], a[76:79]\n" \
"ds_read_b64_tr_b16 v[56:57], v139 offset:1024\n" \
"v_mfma_f32_16x16x32_bf16 a[80:83], v[64:67], v[116:119], a[80:83]\n" \
"ds_read_b64_tr_b16 v[58:59], v139 offset:9216\n" \
"v_mfma_f32_16x16x32_bf16 a[84:87], v[68:71], v[116:119], a[84:87]\n" \
"ds_read_b64_tr_b16 v[60:61], v140 offset:1024\n" \
"v_mfma_f32_16x16x32_bf16 a[88:91], v[72:75], v[116:119], a[88:91]\n" \
"ds_read_b64_tr_b16 v[62:63], v140 offset:9216\n" \
"v_mfma_f32_16x16x32_bf16 a[92:95], v[76:79], v[116:119], a[92:95]\n" \
"buffer_load_dwordx4 v134, s[44:47], s53 offen offset:2048 lds\n" \
"v_mfma_f32_16x16x32_bf16 a[96:99], v[64:67], v[120:123], a[96:99]\n" \
"v_mfma_f32_16x16x32_bf16 a[100:103], v[68:71], v[120:123], a[100:103]\n" \
"v_mfma_f32_16x16x32_bf16 a[104:107], v[72:75], v[120:123], a[104:107]\n" \
"v_mfma_f32_16x16x32_bf16 a[108:111], v[76:79], v[120:123], a[108:111]\n" \
"v_mfma_f32_16x16x32_bf16 a[112:115], v[64:67], v[124:127], a[112:115]\n" \
"v_mfma_f32_16x16x32_bf16 a[116:119], v[68:71], v[124:127], a[116:119]\n" \
"buffer_load_dwordx4 v135, s[48:51], s53 offen offset:3072 lds\n" \
"v_mfma_f32_16x16x32_bf16 a[120:123], v[72:75], v[124:127], a[120:123]\n" \
"v_mfma_f32_16x16x32_bf16 a[124:127], v[76:79], v[124:127], a[124:127]\n" \
"s_add_u32 s52, s52, 1024\n" \
"s_add_u32 s53, s53, s54\n" \
"s_sub_u32 s55, s55, 1\n" \
"s_cmp_lg_u32 s55, 0\n" \
"s_cbranch_scc1 LG48S_loop_%=\n" \
"LG48S_tail_%=:\n" \
"s_waitcnt vmcnt(12) lgkmcnt(0)\n" \
"s_barrier\n" \
"v_mfma_f32_16x16x32_bf16 a[0:3], v[0:3], v[32:35], a[0:3]\n" \
"ds_read_b64_tr_b16 v[96:97], v137 offset:16384\n" \
"v_mfma_f32_16x16x32_bf16 a[4:7], v[4:7], v[32:35], a[4:7]\n" \
"ds_read_b64_tr_b16 v[98:99], v137 offset:24576\n" \
"v_mfma_f32_16x16x32_bf16 a[8:11], v[8:11], v[32:35], a[8:11]\n" \
"ds_read_b128 v[64:67], v136 offset:16384\n" \
"v_mfma_f32_16x16x32_bf16 a[12:15], v[12:15], v[32:35], a[12:15]\n" \
"ds_read_b128 v[68:71], v136 offset:17408\n" \
"v_mfma_f32_16x16x32_bf16 a[16:19], v[0:3], v[36:39], a[16:19]\n" \
"ds_read_b128 v[72:75], v136 offset:18432\n" \
"v_mfma_f32_16x16x32_bf16 a[20:23], v[4:7], v[36:39], a[20:23]\n" \
"v_mfma_f32_16x16x32_bf16 a[24:27], v[8:11], v[36:39], a[24:27]\n" \
"ds_read_b128 v[76:79], v136 offset:19456\n" \
"v_mfma_f32_16x16x32_bf16 a[28:31], v[12:15], v[36:39], a[28:31]\n" \
"ds_read_b64_tr_b16 v[100:101], v138 offset:16384\n" \
"v_mfma_f32_16x16x32_bf16 a[32:35], v[0:3], v[40:43], a[32:35]\n" \
"ds_read_b64_tr_b16 v[102:103], v138 offset:24576\n" \
"v_mfma_f32_16x16x32_bf16 a[36:39], v[4:7], v[40:43], a[36:39]\n" \
"ds_read_b64_tr_b16 v[104:105], v139 offset:16384\n" \
"v_mfma_f32_16x16x32_bf16 a[40:43], v[8:11], v[40:43], a[40:43]\n" \
"ds_read_b64_tr_b16 v[106:107], v139 offset:24576\n" \
"v_mfma_f32_16x16x32_bf16 a[44:47], v[12:15], v[40:43], a[44:47]\n" \
"v_mfma_f32_16x16x32_bf16 a[48:51], v[0:3], v[44:47], a[48:51]\n" \
"ds_read_b64_tr_b16 v[108:109], v140 offset:16384\n" \
"v_mfma_f32_16x16x32_bf16 a[52:55], v[4:7], v[44:47], a[52:55]\n" \
"ds_read_b64_tr_b16 v[110:111], v140 offset:24576\n" \
"v_mfma_f32_16x16x32_bf16 a[56:59], v[8:11], v[44:47], a[56:59]\n" \
"ds_read_b64_tr_b16 v[112:113], v137 offset:17408\n" \
"v_mfma_f32_16x16x32_bf16 a[60:63], v[12:15], v[44:47], a[60:63]\n" \
"ds_read_b64_tr_b16 v[114:115], v137 offset:25600\n" \
"v_mfma_f32_16x16x32_bf16 a[64:67], v[0:3], v[48:51], a[64:67]\n" \
"ds_read_b64_tr_b16 v[116:117], v138 offset:17408\n" \
"v_mfma_f32_16x16x32_bf16 a[68:71], v[4:7], v[48:51], a[68:71]\n" \
"v_mfma_f32_16x16x32_bf16 a[72:75], v[8:11], v[48:51], a[72:75]\n" \
"ds_read_b64_tr_b16 v[118:119], v138 offset:25600\n" \
"v_mfma_f32_16x16x32_bf16 a[76:79], v[12:15], v[48:51], a[76:79]\n" \
"ds_read_b64_tr_b16 v[120:121], v139 offset:17408\n" \
"v_mfma_f32_16x16x32_bf16 a[80:83], v[0:3], v[52:55], a[80:83]\n" \
"ds_read_b64_tr_b16 v[122:123], v139 offset:25600\n" \
"v_mfma_f32_16x16x32_bf16 a[84:87], v[4:7], v[52:55], a[84:87]\n" \
"ds_read_b64_tr_b16 v[124:125], v140 offset:17408\n" \
"v_mfma_f32_16x16x32_bf16 a[88:91], v[8:11], v[52:55], a[88:91]\n" \
"ds_read_b64_tr_b16 v[126:127], v140 offset:25600\n" \
"v_mfma_f32_16x16x32_bf16 a[92:95], v[12:15], v[52:55], a[92:95]\n" \
"v_mfma_f32_16x16x32_bf16 a[96:99], v[0:3], v[56:59], a[96:99]\n" \
"v_mfma_f32_16x16x32_bf16 a[100:103], v[4:7], v[56:59], a[100:103]\n" \
"v_mfma_f32_16x16x32_bf16 a[104:107], v[8:11], v[56:59], a[104:107]\n" \
"v_mfma_f32_16x16x32_bf16 a[108:111], v[12:15], v[56:59], a[108:111]\n" \
"v_mfma_f32_16x16x32_bf16 a[112:115], v[0:3], v[60:63], a[112:115]\n" \
"v_mfma_f32_16x16x32_bf16 a[116:119], v[4:7], v[60:63], a[116:119]\n" \
"v_mfma_f32_16x16x32_bf16 a[120:123], v[8:11], v[60:63], a[120:123]\n" \
"v_mfma_f32_16x16x32_bf16 a[124:127], v[12:15], v[60:63], a[124:127]\n" \
"s_waitcnt vmcnt(6) lgkmcnt(0)\n" \
"s_barrier\n" \
"v_mfma_f32_16x16x32_bf16 a[0:3], v[64:67], v[96:99], a[0:3]\n" \
"ds_read_b64_tr_b16 v[32:33], v137 offset:32768\n" \
"v_mfma_f32_16x16x32_bf16 a[4:7], v[68:71], v[96:99], a[4:7]\n" \
"ds_read_b64_tr_b16 v[34:35], v137 offset:40960\n" \
"v_mfma_f32_16x16x32_bf16 a[8:11], v[72:75], v[96:99], a[8:11]\n" \
"ds_read_b128 v[0:3], v136 offset:32768\n" \
"v_mfma_f32_16x16x32_bf16 a[12:15], v[76:79], v[96:99], a[12:15]\n" \
"ds_read_b128 v[4:7], v136 offset:33792\n" \
"v_mfma_f32_16x16x32_bf16 a[16:19], v[64:67], v[100:103], a[16:19]\n" \
"ds_read_b128 v[8:11], v136 offset:34816\n" \
"v_mfma_f32_16x16x32_bf16 a[20:23], v[68:71], v[100:103], a[20:23]\n" \
"v_mfma_f32_16x16x32_bf16 a[24:27], v[72:75], v[100:103], a[24:27]\n" \
"ds_read_b128 v[12:15], v136 offset:35840\n" \
"v_mfma_f32_16x16x32_bf16 a[28:31], v[76:79], v[100:103], a[28:31]\n" \
"ds_read_b64_tr_b16 v[36:37], v138 offset:32768\n" \
"v_mfma_f32_16x16x32_bf16 a[32:35], v[64:67], v[104:107], a[32:35]\n" \
"ds_read_b64_tr_b16 v[38:39], v138 offset:40960\n" \
"v_mfma_f32_16x16x32_bf16 a[36:39], v[68:71], v[104:107], a[36:39]\n" \
"ds_read_b64_tr_b16 v[40:41], v139 offset:32768\n" \
"v_mfma_f32_16x16x32_bf16 a[40:43], v[72:75], v[104:107], a[40:43]\n" \
"ds_read_b64_tr_b16 v[42:43], v139 offset:40960\n" \
"v_mfma_f32_16x16x32_bf16 a[44:47], v[76:79], v[104:107], a[44:47]\n" \
"v_mfma_f32_16x16x32_bf16 a[48:51], v[64:67], v[108:111], a[48:51]\n" \
"ds_read_b64_tr_b16 v[44:45], v140 offset:32768\n" \
"v_mfma_f32_16x16x32_bf16 a[52:55], v[68:71], v[108:111], a[52:55]\n" \
"ds_read_b64_tr_b16 v[46:47], v140 offset:40960\n" \
"v_mfma_f32_16x16x32_bf16 a[56:59], v[72:75], v[108:111], a[56:59]\n" \
"ds_read_b64_tr_b16 v[48:49], v137 offset:33792\n" \
"v_mfma_f32_16x16x32_bf16 a[60:63], v[76:79], v[108:111], a[60:63]\n" \
"ds_read_b64_tr_b16 v[50:51], v137 offset:41984\n" \
"v_mfma_f32_16x16x32_bf16 a[64:67], v[64:67], v[112:115], a[64:67]\n" \
"ds_read_b64_tr_b16 v[52:53], v138 offset:33792\n" \
"v_mfma_f32_16x16x32_bf16 a[68:71], v[68:71], v[112:115], a[68:71]\n" \
"v_mfma_f32_16x16x32_bf16 a[72:75], v[72:75], v[112:115], a[72:75]\n" \
"ds_read_b64_tr_b16 v[54:55], v138 offset:41984\n" \
"v_mfma_f32_16x16x32_bf16 a[76:79], v[76:79], v[112:115], a[76:79]\n" \
"ds_read_b64_tr_b16 v[56:57], v139 offset:33792\n" \
"v_mfma_f32_16x16x32_bf16 a[80:83], v[64:67], v[116:119], a[80:83]\n" \
"ds_read_b64_tr_b16 v[58:59], v139 offset:41984\n" \
"v_mfma_f32_16x16x32_bf16 a[84:87], v[68:71], v[116:119], a[84:87]\n" \
"ds_read_b64_tr_b16 v[60:61], v140 offset:33792\n" \
"v_mfma_f32_16x16x32_bf16 a[88:91], v[72:75], v[116:119], a[88:91]\n" \
"ds_read_b64_tr_b16 v[62:63], v140 offset:41984\n" \
"v_mfma_f32_16x16x32_bf16 a[92:95], v[76:79], v[116:119], a[92:95]\n" \
"v_mfma_f32_16x16x32_bf16 a[96:99], v[64:67], v[120:123], a[96:99]\n" \
"v_mfma_f32_16x16x32_bf16 a[100:103], v[68:71], v[120:123], a[100:103]\n" \
"v_mfma_f32_16x16x32_bf16 a[104:107], v[72:75], v[120:123], a[104:107]\n" \
"v_mfma_f32_16x16x32_bf16 a[108:111], v[76:79], v[120:123], a[108:111]\n" \
"v_mfma_f32_16x16x32_bf16 a[112:115], v[64:67], v[124:127], a[112:115]\n" \
"v_mfma_f32_16x16x32_bf16 a[116:119], v[68:71], v[124:127], a[116:119]\n" \
"v_mfma_f32_16x16x32_bf16 a[120:123], v[72:75], v[124:127], a[120:123]\n" \
"v_mfma_f32_16x16x32_bf16 a[124:127], v[76:79], v[124:127], a[124:127]\n" \
"s_waitcnt vmcnt(0) lgkmcnt(0)\n" \
"s_barrier\n" \
"v_mfma_f32_16x16x32_bf16 a[0:3], v[0:3], v[32:35], a[0:3]\n" \
"ds_read_b64_tr_b16 v[96:97], v137 offset:49152\n" \
"v_mfma_f32_16x16x32_bf16 a[4:7], v[4:7], v[32:35], a[4:7]\n" \
"ds_read_b64_tr_b16 v[98:99], v137 offset:57344\n" \
"v_mfma_f32_16x16x32_bf16 a[8:11], v[8:11], v[32:35], a[8:11]\n" \
"ds_read_b128 v[64:67], v136 offset:49152\n" \
"v_mfma_f32_16x16x32_bf16 a[12:15], v[12:15], v[32:35], a[12:15]\n" \
"ds_read_b128 v[68:71], v136 offset:50176\n" \
"v_mfma_f32_16x16x32_bf16 a[16:19], v[0:3], v[36:39], a[16:19]\n" \
"ds_read_b128 v[72:75], v136 offset:51200\n" \
"v_mfma_f32_16x16x32_bf16 a[20:23], v[4:7], v[36:39], a[20:23]\n" \
"v_mfma_f32_16x16x32_bf16 a[24:27], v[8:11], v[36:39], a[24:27]\n" \
"ds_read_b128 v[76:79], v136 offset:52224\n" \
"v_mfma_f32_16x16x32_bf16 a[28:31], v[12:15], v[36:39], a[28:31]\n" \
"ds_read_b64_tr_b16 v[100:101], v138 offset:49152\n" \
"v_mfma_f32_16x16x32_bf16 a[32:35], v[0:3], v[40:43], a[32:35]\n" \
"ds_read_b64_tr_b16 v[102:103], v138 offset:57344\n" \
"v_mfma_f32_16x16x32_bf16 a[36:39], v[4:7], v[40:43], a[36:39]\n" \
"ds_read_b64_tr_b16 v[104:105], v139 offset:49152\n" \
"v_mfma_f32_16x16x32_bf16 a[40:43], v[8:11], v[40:43], a[40:43]\n" \
"ds_read_b64_tr_b16 v[106:107], v139 offset:57344\n" \
"v_mfma_f32_16x16x32_bf16 a[44:47], v[12:15], v[40:43], a[44:47]\n" \
"v_mfma_f32_16x16x32_bf16 a[48:51], v[0:3], v[44:47], a[48:51]\n" \
"ds_read_b64_tr_b16 v[108:109], v140 offset:49152\n" \
"v_mfma_f32_16x16x32_bf16 a[52:55], v[4:7], v[44:47], a[52:55]\n" \
"ds_read_b64_tr_b16 v[110:111], v140 offset:57344\n" \
"v_mfma_f32_16x16x32_bf16 a[56:59], v[8:11], v[44:47], a[56:59]\n" \
"ds_read_b64_tr_b16 v[112:113], v137 offset:50176\n" \
"v_mfma_f32_16x16x32_bf16 a[60:63], v[12:15], v[44:47], a[60:63]\n" \
"ds_read_b64_tr_b16 v[114:115], v137 offset:58368\n" \
"v_mfma_f32_16x16x32_bf16 a[64:67], v[0:3], v[48:51], a[64:67]\n" \
"ds_read_b64_tr_b16 v[116:117], v138 offset:50176\n" \
"v_mfma_f32_16x16x32_bf16 a[68:71], v[4:7], v[48:51], a[68:71]\n" \
"v_mfma_f32_16x16x32_bf16 a[72:75], v[8:11], v[48:51], a[72:75]\n" \
"ds_read_b64_tr_b16 v[118:119], v138 offset:58368\n" \
"v_mfma_f32_16x16x32_bf16 a[76:79], v[12:15], v[48:51], a[76:79]\n" \
"ds_read_b64_tr_b16 v[120:121], v139 offset:50176\n" \
"v_mfma_f32_16x16x32_bf16 a[80:83], v[0:3], v[52:55], a[80:83]\n" \
"ds_read_b64_tr_b16 v[122:123], v139 offset:58368\n" \
"v_mfma_f32_16x16x32_bf16 a[84:87], v[4:7], v[52:55], a[84:87]\n" \
"ds_read_b64_tr_b16 v[124:125], v140 offset:50176\n" \
"v_mfma_f32_16x16x32_bf16 a[88:91], v[8:11], v[52:55], a[88:91]\n" \
"ds_read_b64_tr_b16 v[126:127], v140 offset:58368\n" \
"v_mfma_f32_16x16x32_bf16 a[92:95], v[12:15], v[52:55], a[92:95]\n" \
"v_mfma_f32_16x16x32_bf16 a[96:99], v[0:3], v[56:59], a[96:99]\n" \
"v_mfma_f32_16x16x32_bf16 a[100:103], v[4:7], v[56:59], a[100:103]\n" \
"v_mfma_f32_16x16x32_bf16 a[104:107], v[8:11], v[56:59], a[104:107]\n" \
"v_mfma_f32_16x16x32_bf16 a[108:111], v[12:15], v[56:59], a[108:111]\n" \
"v_mfma_f32_16x16x32_bf16 a[112:115], v[0:3], v[60:63], a[112:115]\n" \
"v_mfma_f32_16x16x32_bf16 a[116:119], v[4:7], v[60:63], a[116:119]\n" \
"v_mfma_f32_16x16x32_bf16 a[120:123], v[8:11], v[60:63], a[120:123]\n" \
"v_mfma_f32_16x16x32_bf16 a[124:127], v[12:15], v[60:63], a[124:127]\n" \
"s_waitcnt lgkmcnt(0)\n" \
"v_mfma_f32_16x16x32_bf16 a[0:3], v[64:67], v[96:99], a[0:3]\n" \
"v_mfma_f32_16x16x32_bf16 a[4:7], v[68:71], v[96:99], a[4:7]\n" \
"v_mfma_f32_16x16x32_bf16 a[8:11], v[72:75], v[96:99], a[8:11]\n" \
"v_mfma_f32_16x16x32_bf16 a[12:15], v[76:79], v[96:99], a[12:15]\n" \
"v_mfma_f32_16x16x32_bf16 a[16:19], v[64:67], v[100:103], a[16:19]\n" \
"v_mfma_f32_16x16x32_bf16 a[20:23], v[68:71], v[100:103], a[20:23]\n" \
"v_mfma_f32_16x16x32_bf16 a[24:27], v[72:75], v[100:103], a[24:27]\n" \
"v_mfma_f32_16x16x32_bf16 a[28:31], v[76:79], v[100:103], a[28:31]\n" \
"v_mfma_f32_16x16x32_bf16 a[32:35], v[64:67], v[104:107], a[32:35]\n" \
"v_mfma_f32_16x16x32_bf16 a[36:39], v[68:71], v[104:107], a[36:39]\n" \
"v_mfma_f32_16x16x32_bf16 a[40:43], v[72:75], v[104:107], a[40:43]\n" \
"v_mfma_f32_16x16x32_bf16 a[44:47], v[76:79], v[104:107], a[44:47]\n" \
"v_mfma_f32_16x16x32_bf16 a[48:51], v[64:67], v[108:111], a[48:51]\n" \
"v_mfma_f32_16x16x32_bf16 a[52:55], v[68:71], v[108:111], a[52:55]\n" \
"v_mfma_f32_16x16x32_bf16 a[56:59], v[72:75], v[108:111], a[56:59]\n" \
"v_mfma_f32_16x16x32_bf16 a[60:63], v[76:79], v[108:111], a[60:63]\n" \
"v_mfma_f32_16x16x32_bf16 a[64:67], v[64:67], v[112:115], a[64:67]\n" \
"v_mfma_f32_16x16x32_bf16 a[68:71], v[68:71], v[112:115], a[68:71]\n" \
"v_mfma_f32_16x16x32_bf16 a[72:75], v[72:75], v[112:115], a[72:75]\n" \
"v_mfma_f32_16x16x32_bf16 a[76:79], v[76:79], v[112:115], a[76:79]\n" \
"v_mfma_f32_16x16x32_bf16 a[80:83], v[64:67], v[116:119], a[80:83]\n" \
"v_mfma_f32_16x16x32_bf16 a[84:87], v[68:71], v[116:119], a[84:87]\n" \
"v_mfma_f32_16x16x32_bf16 a[88:91], v[72:75], v[116:119], a[88:91]\n" \
"v_mfma_f32_16x16x32_bf16 a[92:95], v[76:79], v[116:119], a[92:95]\n" \
"v_mfma_f32_16x16x32_bf16 a[96:99], v[64:67], v[120:123], a[96:99]\n" \
"v_mfma_f32_16x16x32_bf16 a[100:103], v[68:71], v[120:123], a[100:103]\n" \
"v_mfma_f32_16x16x32_bf16 a[104:107], v[72:75], v[120:123], a[104:107]\n" \
"v_mfma_f32_16x16x32_bf16 a[108:111], v[76:79], v[120:123], a[108:111]\n" \
"v_mfma_f32_16x16x32_bf16 a[112:115], v[64:67], v[124:127], a[112:115]\n" \
"v_mfma_f32_16x16x32_bf16 a[116:119], v[68:71], v[124:127], a[116:119]\n" \
"v_mfma_f32_16x16x32_bf16 a[120:123], v[72:75], v[124:127], a[120:123]\n" \
"v_mfma_f32_16x16x32_bf16 a[124:127], v[76:79], v[124:127], a[124:127]\n" \
"s_nop 7\n" \
"LG48S_eV_%=:\n" \
"s_add_u32 s66, s64, 4096\n" \
"v_accvgpr_read_b32 v0, a0\n" \
"v_accvgpr_read_b32 v1, a1\n" \
"v_accvgpr_read_b32 v2, a2\n" \
"v_accvgpr_read_b32 v3, a3\n" \
"v_accvgpr_read_b32 v4, a4\n" \
"v_accvgpr_read_b32 v5, a5\n" \
"v_accvgpr_read_b32 v6, a6\n" \
"v_accvgpr_read_b32 v7, a7\n" \
"v_cvt_pk_bf16_f32 v0, v0, v1\n" \
"v_cvt_pk_bf16_f32 v1, v2, v3\n" \
"v_cvt_pk_bf16_f32 v2, v4, v5\n" \
"v_cvt_pk_bf16_f32 v3, v6, v7\n" \
"buffer_store_dwordx4 v[0:3], v141, s[60:63], s64 offen offset:0 sc1\n" \
"v_accvgpr_read_b32 v8, a16\n" \
"v_accvgpr_read_b32 v9, a17\n" \
"v_accvgpr_read_b32 v10, a18\n" \
"v_accvgpr_read_b32 v11, a19\n" \
"v_accvgpr_read_b32 v12, a20\n" \
"v_accvgpr_read_b32 v13, a21\n" \
"v_accvgpr_read_b32 v14, a22\n" \
"v_accvgpr_read_b32 v15, a23\n" \
"v_cvt_pk_bf16_f32 v8, v8, v9\n" \
"v_cvt_pk_bf16_f32 v9, v10, v11\n" \
"v_cvt_pk_bf16_f32 v10, v12, v13\n" \
"v_cvt_pk_bf16_f32 v11, v14, v15\n" \
"buffer_store_dwordx4 v[8:11], v141, s[60:63], s64 offen offset:1024 sc1\n" \
"v_accvgpr_read_b32 v16, a32\n" \
"v_accvgpr_read_b32 v17, a33\n" \
"v_accvgpr_read_b32 v18, a34\n" \
"v_accvgpr_read_b32 v19, a35\n" \
"v_accvgpr_read_b32 v20, a36\n" \
"v_accvgpr_read_b32 v21, a37\n" \
"v_accvgpr_read_b32 v22, a38\n" \
"v_accvgpr_read_b32 v23, a39\n" \
"v_cvt_pk_bf16_f32 v16, v16, v17\n" \
"v_cvt_pk_bf16_f32 v17, v18, v19\n" \
"v_cvt_pk_bf16_f32 v18, v20, v21\n" \
"v_cvt_pk_bf16_f32 v19, v22, v23\n" \
"buffer_store_dwordx4 v[16:19], v141, s[60:63], s64 offen offset:2048 sc1\n" \
"v_accvgpr_read_b32 v24, a48\n" \
"v_accvgpr_read_b32 v25, a49\n" \
"v_accvgpr_read_b32 v26, a50\n" \
"v_accvgpr_read_b32 v27, a51\n" \
"v_accvgpr_read_b32 v28, a52\n" \
"v_accvgpr_read_b32 v29, a53\n" \
"v_accvgpr_read_b32 v30, a54\n" \
"v_accvgpr_read_b32 v31, a55\n" \
"v_cvt_pk_bf16_f32 v24, v24, v25\n" \
"v_cvt_pk_bf16_f32 v25, v26, v27\n" \
"v_cvt_pk_bf16_f32 v26, v28, v29\n" \
"v_cvt_pk_bf16_f32 v27, v30, v31\n" \
"buffer_store_dwordx4 v[24:27], v141, s[60:63], s64 offen offset:3072 sc1\n" \
"v_accvgpr_read_b32 v32, a64\n" \
"v_accvgpr_read_b32 v33, a65\n" \
"v_accvgpr_read_b32 v34, a66\n" \
"v_accvgpr_read_b32 v35, a67\n" \
"v_accvgpr_read_b32 v36, a68\n" \
"v_accvgpr_read_b32 v37, a69\n" \
"v_accvgpr_read_b32 v38, a70\n" \
"v_accvgpr_read_b32 v39, a71\n" \
"v_cvt_pk_bf16_f32 v32, v32, v33\n" \
"v_cvt_pk_bf16_f32 v33, v34, v35\n" \
"v_cvt_pk_bf16_f32 v34, v36, v37\n" \
"v_cvt_pk_bf16_f32 v35, v38, v39\n" \
"buffer_store_dwordx4 v[32:35], v141, s[60:63], s66 offen offset:0 sc1\n" \
"v_accvgpr_read_b32 v40, a80\n" \
"v_accvgpr_read_b32 v41, a81\n" \
"v_accvgpr_read_b32 v42, a82\n" \
"v_accvgpr_read_b32 v43, a83\n" \
"v_accvgpr_read_b32 v44, a84\n" \
"v_accvgpr_read_b32 v45, a85\n" \
"v_accvgpr_read_b32 v46, a86\n" \
"v_accvgpr_read_b32 v47, a87\n" \
"v_cvt_pk_bf16_f32 v40, v40, v41\n" \
"v_cvt_pk_bf16_f32 v41, v42, v43\n" \
"v_cvt_pk_bf16_f32 v42, v44, v45\n" \
"v_cvt_pk_bf16_f32 v43, v46, v47\n" \
"buffer_store_dwordx4 v[40:43], v141, s[60:63], s66 offen offset:1024 sc1\n" \
"v_accvgpr_read_b32 v48, a96\n" \
"v_accvgpr_read_b32 v49, a97\n" \
"v_accvgpr_read_b32 v50, a98\n" \
"v_accvgpr_read_b32 v51, a99\n" \
"v_accvgpr_read_b32 v52, a100\n" \
"v_accvgpr_read_b32 v53, a101\n" \
"v_accvgpr_read_b32 v54, a102\n" \
"v_accvgpr_read_b32 v55, a103\n" \
"v_cvt_pk_bf16_f32 v48, v48, v49\n" \
"v_cvt_pk_bf16_f32 v49, v50, v51\n" \
"v_cvt_pk_bf16_f32 v50, v52, v53\n" \
"v_cvt_pk_bf16_f32 v51, v54, v55\n" \
"buffer_store_dwordx4 v[48:51], v141, s[60:63], s66 offen offset:2048 sc1\n" \
"v_accvgpr_read_b32 v56, a112\n" \
"v_accvgpr_read_b32 v57, a113\n" \
"v_accvgpr_read_b32 v58, a114\n" \
"v_accvgpr_read_b32 v59, a115\n" \
"v_accvgpr_read_b32 v60, a116\n" \
"v_accvgpr_read_b32 v61, a117\n" \
"v_accvgpr_read_b32 v62, a118\n" \
"v_accvgpr_read_b32 v63, a119\n" \
"v_cvt_pk_bf16_f32 v56, v56, v57\n" \
"v_cvt_pk_bf16_f32 v57, v58, v59\n" \
"v_cvt_pk_bf16_f32 v58, v60, v61\n" \
"v_cvt_pk_bf16_f32 v59, v62, v63\n" \
"buffer_store_dwordx4 v[56:59], v141, s[60:63], s66 offen offset:3072 sc1\n" \
"s_add_u32 s64, s64, s65\n" \
"s_add_u32 s66, s64, 4096\n" \
"v_accvgpr_read_b32 v0, a8\n" \
"v_accvgpr_read_b32 v1, a9\n" \
"v_accvgpr_read_b32 v2, a10\n" \
"v_accvgpr_read_b32 v3, a11\n" \
"v_accvgpr_read_b32 v4, a12\n" \
"v_accvgpr_read_b32 v5, a13\n" \
"v_accvgpr_read_b32 v6, a14\n" \
"v_accvgpr_read_b32 v7, a15\n" \
"v_cvt_pk_bf16_f32 v0, v0, v1\n" \
"v_cvt_pk_bf16_f32 v1, v2, v3\n" \
"v_cvt_pk_bf16_f32 v2, v4, v5\n" \
"v_cvt_pk_bf16_f32 v3, v6, v7\n" \
"buffer_store_dwordx4 v[0:3], v141, s[60:63], s64 offen offset:0 sc1\n" \
"v_accvgpr_read_b32 v8, a24\n" \
"v_accvgpr_read_b32 v9, a25\n" \
"v_accvgpr_read_b32 v10, a26\n" \
"v_accvgpr_read_b32 v11, a27\n" \
"v_accvgpr_read_b32 v12, a28\n" \
"v_accvgpr_read_b32 v13, a29\n" \
"v_accvgpr_read_b32 v14, a30\n" \
"v_accvgpr_read_b32 v15, a31\n" \
"v_cvt_pk_bf16_f32 v8, v8, v9\n" \
"v_cvt_pk_bf16_f32 v9, v10, v11\n" \
"v_cvt_pk_bf16_f32 v10, v12, v13\n" \
"v_cvt_pk_bf16_f32 v11, v14, v15\n" \
"buffer_store_dwordx4 v[8:11], v141, s[60:63], s64 offen offset:1024 sc1\n" \
"v_accvgpr_read_b32 v16, a40\n" \
"v_accvgpr_read_b32 v17, a41\n" \
"v_accvgpr_read_b32 v18, a42\n" \
"v_accvgpr_read_b32 v19, a43\n" \
"v_accvgpr_read_b32 v20, a44\n" \
"v_accvgpr_read_b32 v21, a45\n" \
"v_accvgpr_read_b32 v22, a46\n" \
"v_accvgpr_read_b32 v23, a47\n" \
"v_cvt_pk_bf16_f32 v16, v16, v17\n" \
"v_cvt_pk_bf16_f32 v17, v18, v19\n" \
"v_cvt_pk_bf16_f32 v18, v20, v21\n" \
"v_cvt_pk_bf16_f32 v19, v22, v23\n" \
"buffer_store_dwordx4 v[16:19], v141, s[60:63], s64 offen offset:2048 sc1\n" \
"v_accvgpr_read_b32 v24, a56\n" \
"v_accvgpr_read_b32 v25, a57\n" \
"v_accvgpr_read_b32 v26, a58\n" \
"v_accvgpr_read_b32 v27, a59\n" \
"v_accvgpr_read_b32 v28, a60\n" \
"v_accvgpr_read_b32 v29, a61\n" \
"v_accvgpr_read_b32 v30, a62\n" \
"v_accvgpr_read_b32 v31, a63\n" \
"v_cvt_pk_bf16_f32 v24, v24, v25\n" \
"v_cvt_pk_bf16_f32 v25, v26, v27\n" \
"v_cvt_pk_bf16_f32 v26, v28, v29\n" \
"v_cvt_pk_bf16_f32 v27, v30, v31\n" \
"buffer_store_dwordx4 v[24:27], v141, s[60:63], s64 offen offset:3072 sc1\n" \
"v_accvgpr_read_b32 v32, a72\n" \
"v_accvgpr_read_b32 v33, a73\n" \
"v_accvgpr_read_b32 v34, a74\n" \
"v_accvgpr_read_b32 v35, a75\n" \
"v_accvgpr_read_b32 v36, a76\n" \
"v_accvgpr_read_b32 v37, a77\n" \
"v_accvgpr_read_b32 v38, a78\n" \
"v_accvgpr_read_b32 v39, a79\n" \
"v_cvt_pk_bf16_f32 v32, v32, v33\n" \
"v_cvt_pk_bf16_f32 v33, v34, v35\n" \
"v_cvt_pk_bf16_f32 v34, v36, v37\n" \
"v_cvt_pk_bf16_f32 v35, v38, v39\n" \
"buffer_store_dwordx4 v[32:35], v141, s[60:63], s66 offen offset:0 sc1\n" \
"v_accvgpr_read_b32 v40, a88\n" \
"v_accvgpr_read_b32 v41, a89\n" \
"v_accvgpr_read_b32 v42, a90\n" \
"v_accvgpr_read_b32 v43, a91\n" \
"v_accvgpr_read_b32 v44, a92\n" \
"v_accvgpr_read_b32 v45, a93\n" \
"v_accvgpr_read_b32 v46, a94\n" \
"v_accvgpr_read_b32 v47, a95\n" \
"v_cvt_pk_bf16_f32 v40, v40, v41\n" \
"v_cvt_pk_bf16_f32 v41, v42, v43\n" \
"v_cvt_pk_bf16_f32 v42, v44, v45\n" \
"v_cvt_pk_bf16_f32 v43, v46, v47\n" \
"buffer_store_dwordx4 v[40:43], v141, s[60:63], s66 offen offset:1024 sc1\n" \
"v_accvgpr_read_b32 v48, a104\n" \
"v_accvgpr_read_b32 v49, a105\n" \
"v_accvgpr_read_b32 v50, a106\n" \
"v_accvgpr_read_b32 v51, a107\n" \
"v_accvgpr_read_b32 v52, a108\n" \
"v_accvgpr_read_b32 v53, a109\n" \
"v_accvgpr_read_b32 v54, a110\n" \
"v_accvgpr_read_b32 v55, a111\n" \
"v_cvt_pk_bf16_f32 v48, v48, v49\n" \
"v_cvt_pk_bf16_f32 v49, v50, v51\n" \
"v_cvt_pk_bf16_f32 v50, v52, v53\n" \
"v_cvt_pk_bf16_f32 v51, v54, v55\n" \
"buffer_store_dwordx4 v[48:51], v141, s[60:63], s66 offen offset:2048 sc1\n" \
"v_accvgpr_read_b32 v56, a120\n" \
"v_accvgpr_read_b32 v57, a121\n" \
"v_accvgpr_read_b32 v58, a122\n" \
"v_accvgpr_read_b32 v59, a123\n" \
"v_accvgpr_read_b32 v60, a124\n" \
"v_accvgpr_read_b32 v61, a125\n" \
"v_accvgpr_read_b32 v62, a126\n" \
"v_accvgpr_read_b32 v63, a127\n" \
"v_cvt_pk_bf16_f32 v56, v56, v57\n" \
"v_cvt_pk_bf16_f32 v57, v58, v59\n" \
"v_cvt_pk_bf16_f32 v58, v60, v61\n" \
"v_cvt_pk_bf16_f32 v59, v62, v63\n" \
"buffer_store_dwordx4 v[56:59], v141, s[60:63], s66 offen offset:3072 sc1\n" \
"s_add_u32 s64, s64, s65\n" \
"LG48S_end_%=:\n" \
"s_nop 1\n"
#define ASM_SP \
"s_add_u32 m0, s56, 0\n" \
"s_nop 0\n" \
"buffer_load_dwordx4 v0, s[36:39], s53 offen lds\n" \
"buffer_load_dwordx4 v1, s[40:43], s53 offen offset:1024 lds\n" \
"s_add_u32 s53, s53, s54\n" \
"s_add_u32 m0, s56, 8192\n" \
"s_nop 0\n" \
"buffer_load_dwordx4 v0, s[36:39], s53 offen lds\n" \
"buffer_load_dwordx4 v1, s[40:43], s53 offen offset:1024 lds\n" \
"s_add_u32 s53, s53, s54\n" \
"s_add_u32 m0, s56, 16384\n" \
"s_nop 0\n" \
"buffer_load_dwordx4 v0, s[36:39], s53 offen lds\n" \
"buffer_load_dwordx4 v1, s[40:43], s53 offen offset:1024 lds\n" \
"s_add_u32 s53, s53, s54\n" \
"s_add_u32 m0, s56, 24576\n" \
"s_nop 0\n" \
"buffer_load_dwordx4 v0, s[36:39], s53 offen lds\n" \
"buffer_load_dwordx4 v1, s[40:43], s53 offen offset:1024 lds\n" \
"s_add_u32 s53, s53, s54\n" \
"s_add_u32 m0, s56, 32768\n" \
"s_nop 0\n" \
"buffer_load_dwordx4 v0, s[36:39], s53 offen lds\n" \
"buffer_load_dwordx4 v1, s[40:43], s53 offen offset:1024 lds\n" \
"s_add_u32 s53, s53, s54\n" \
"s_add_u32 m0, s56, 40960\n" \
"s_nop 0\n" \
"buffer_load_dwordx4 v0, s[36:39], s53 offen lds\n" \
"buffer_load_dwordx4 v1, s[40:43], s53 offen offset:1024 lds\n" \
"s_add_u32 s53, s53, s54\n" \
"s_add_u32 m0, s56, 49152\n" \
"s_nop 0\n" \
"buffer_load_dwordx4 v0, s[36:39], s53 offen lds\n" \
"buffer_load_dwordx4 v1, s[40:43], s53 offen offset:1024 lds\n" \
"s_add_u32 s53, s53, s54\n" \
"s_add_u32 m0, s56, 57344\n" \
"s_nop 0\n" \
"buffer_load_dwordx4 v0, s[36:39], s53 offen lds\n" \
"buffer_load_dwordx4 v1, s[40:43], s53 offen offset:1024 lds\n" \
"s_add_u32 s53, s53, s54\n" \
"s_add_u32 m0, s56, 65536\n" \
"s_nop 0\n" \
"buffer_load_dwordx4 v0, s[36:39], s53 offen lds\n" \
"buffer_load_dwordx4 v1, s[40:43], s53 offen offset:1024 lds\n" \
"s_add_u32 s53, s53, s54\n" \
"s_add_u32 m0, s56, 73728\n" \
"s_nop 0\n" \
"buffer_load_dwordx4 v0, s[36:39], s53 offen lds\n" \
"buffer_load_dwordx4 v1, s[40:43], s53 offen offset:1024 lds\n" \
"s_add_u32 s53, s53, s54\n" \
"s_add_u32 m0, s56, 81920\n" \
"s_nop 0\n" \
"buffer_load_dwordx4 v0, s[36:39], s53 offen lds\n" \
"buffer_load_dwordx4 v1, s[40:43], s53 offen offset:1024 lds\n" \
"s_add_u32 s53, s53, s54\n" \
"s_add_u32 m0, s56, 90112\n" \
"s_nop 0\n" \
"buffer_load_dwordx4 v0, s[36:39], s53 offen lds\n" \
"buffer_load_dwordx4 v1, s[40:43], s53 offen offset:1024 lds\n" \
"s_add_u32 s53, s53, s54\n" \
"s_add_u32 m0, s56, 98304\n" \
"s_nop 0\n" \
"buffer_load_dwordx4 v0, s[36:39], s53 offen lds\n" \
"buffer_load_dwordx4 v1, s[40:43], s53 offen offset:1024 lds\n" \
"s_add_u32 s53, s53, s54\n" \
"s_add_u32 m0, s56, 106496\n" \
"s_nop 0\n" \
"buffer_load_dwordx4 v0, s[36:39], s53 offen lds\n" \
"buffer_load_dwordx4 v1, s[40:43], s53 offen offset:1024 lds\n" \
"s_add_u32 s53, s53, s54\n" \
"s_add_u32 m0, s56, 114688\n" \
"s_nop 0\n" \
"buffer_load_dwordx4 v0, s[36:39], s53 offen lds\n" \
"buffer_load_dwordx4 v1, s[40:43], s53 offen offset:1024 lds\n" \
"s_add_u32 s53, s53, s54\n" \
"s_add_u32 m0, s56, 122880\n" \
"s_nop 0\n" \
"buffer_load_dwordx4 v0, s[36:39], s53 offen lds\n" \
"buffer_load_dwordx4 v1, s[40:43], s53 offen offset:1024 lds\n" \
"s_add_u32 s53, s53, s54\n" \
"s_waitcnt vmcnt(16)\n" \
"s_barrier\n" \
"ds_read_b64_tr_b16 a[0:1], v4 offset:0\n" \
"ds_read_b64_tr_b16 a[2:3], v4 offset:4096\n" \
"ds_read_b64_tr_b16 a[4:5], v5 offset:0\n" \
"ds_read_b64_tr_b16 a[6:7], v5 offset:4096\n" \
"ds_read_b64_tr_b16 a[8:9], v4 offset:8192\n" \
"ds_read_b64_tr_b16 a[10:11], v4 offset:12288\n" \
"ds_read_b64_tr_b16 a[12:13], v5 offset:8192\n" \
"ds_read_b64_tr_b16 a[14:15], v5 offset:12288\n" \
"ds_read_b64_tr_b16 a[16:17], v4 offset:16384\n" \
"ds_read_b64_tr_b16 a[18:19], v4 offset:20480\n" \
"ds_read_b64_tr_b16 a[20:21], v5 offset:16384\n" \
"ds_read_b64_tr_b16 a[22:23], v5 offset:20480\n" \
"ds_read_b64_tr_b16 a[24:25], v4 offset:24576\n" \
"ds_read_b64_tr_b16 a[26:27], v4 offset:28672\n" \
"ds_read_b64_tr_b16 a[28:29], v5 offset:24576\n" \
"ds_read_b64_tr_b16 a[30:31], v5 offset:28672\n" \
"ds_read_b64_tr_b16 a[32:33], v4 offset:32768\n" \
"ds_read_b64_tr_b16 a[34:35], v4 offset:36864\n" \
"ds_read_b64_tr_b16 a[36:37], v5 offset:32768\n" \
"ds_read_b64_tr_b16 a[38:39], v5 offset:36864\n" \
"ds_read_b64_tr_b16 a[40:41], v4 offset:40960\n" \
"ds_read_b64_tr_b16 a[42:43], v4 offset:45056\n" \
"ds_read_b64_tr_b16 a[44:45], v5 offset:40960\n" \
"ds_read_b64_tr_b16 a[46:47], v5 offset:45056\n" \
"ds_read_b64_tr_b16 a[48:49], v4 offset:49152\n" \
"ds_read_b64_tr_b16 a[50:51], v4 offset:53248\n" \
"ds_read_b64_tr_b16 a[52:53], v5 offset:49152\n" \
"ds_read_b64_tr_b16 a[54:55], v5 offset:53248\n" \
"ds_read_b64_tr_b16 a[56:57], v4 offset:57344\n" \
"ds_read_b64_tr_b16 a[58:59], v4 offset:61440\n" \
"ds_read_b64_tr_b16 a[60:61], v5 offset:57344\n" \
"ds_read_b64_tr_b16 a[62:63], v5 offset:61440\n" \
"s_waitcnt lgkmcnt(0)\n" \
"s_barrier\n" \
"s_add_u32 m0, s56, 0\n" \
"s_nop 0\n" \
"buffer_load_dwordx4 v0, s[36:39], s53 offen lds\n" \
"buffer_load_dwordx4 v1, s[40:43], s53 offen offset:1024 lds\n" \
"s_add_u32 s53, s53, s54\n" \
"s_add_u32 m0, s56, 8192\n" \
"s_nop 0\n" \
"buffer_load_dwordx4 v0, s[36:39], s53 offen lds\n" \
"buffer_load_dwordx4 v1, s[40:43], s53 offen offset:1024 lds\n" \
"s_add_u32 s53, s53, s54\n" \
"s_add_u32 m0, s56, 16384\n" \
"s_nop 0\n" \
"buffer_load_dwordx4 v0, s[36:39], s53 offen lds\n" \
"buffer_load_dwordx4 v1, s[40:43], s53 offen offset:1024 lds\n" \
"s_add_u32 s53, s53, s54\n" \
"s_add_u32 m0, s56, 24576\n" \
"s_nop 0\n" \
"buffer_load_dwordx4 v0, s[36:39], s53 offen lds\n" \
"buffer_load_dwordx4 v1, s[40:43], s53 offen offset:1024 lds\n" \
"s_add_u32 s53, s53, s54\n" \
"s_add_u32 m0, s56, 32768\n" \
"s_nop 0\n" \
"buffer_load_dwordx4 v0, s[36:39], s53 offen lds\n" \
"buffer_load_dwordx4 v1, s[40:43], s53 offen offset:1024 lds\n" \
"s_add_u32 s53, s53, s54\n" \
"s_add_u32 m0, s56, 40960\n" \
"s_nop 0\n" \
"buffer_load_dwordx4 v0, s[36:39], s53 offen lds\n" \
"buffer_load_dwordx4 v1, s[40:43], s53 offen offset:1024 lds\n" \
"s_add_u32 s53, s53, s54\n" \
"s_add_u32 m0, s56, 49152\n" \
"s_nop 0\n" \
"buffer_load_dwordx4 v0, s[36:39], s53 offen lds\n" \
"buffer_load_dwordx4 v1, s[40:43], s53 offen offset:1024 lds\n" \
"s_add_u32 s53, s53, s54\n" \
"s_add_u32 m0, s56, 57344\n" \
"s_nop 0\n" \
"buffer_load_dwordx4 v0, s[36:39], s53 offen lds\n" \
"buffer_load_dwordx4 v1, s[40:43], s53 offen offset:1024 lds\n" \
"s_add_u32 s53, s53, s54\n" \
"s_waitcnt vmcnt(16)\n" \
"s_barrier\n" \
"ds_read_b64_tr_b16 a[64:65], v6 offset:0\n" \
"ds_read_b64_tr_b16 a[66:67], v6 offset:4096\n" \
"ds_read_b64_tr_b16 a[68:69], v7 offset:0\n" \
"ds_read_b64_tr_b16 a[70:71], v7 offset:4096\n" \
"ds_read_b64_tr_b16 a[72:73], v6 offset:8192\n" \
"ds_read_b64_tr_b16 a[74:75], v6 offset:12288\n" \
"ds_read_b64_tr_b16 a[76:77], v7 offset:8192\n" \
"ds_read_b64_tr_b16 a[78:79], v7 offset:12288\n" \
"ds_read_b64_tr_b16 a[80:81], v6 offset:16384\n" \
"ds_read_b64_tr_b16 a[82:83], v6 offset:20480\n" \
"ds_read_b64_tr_b16 a[84:85], v7 offset:16384\n" \
"ds_read_b64_tr_b16 a[86:87], v7 offset:20480\n" \
"ds_read_b64_tr_b16 a[88:89], v6 offset:24576\n" \
"ds_read_b64_tr_b16 a[90:91], v6 offset:28672\n" \
"ds_read_b64_tr_b16 a[92:93], v7 offset:24576\n" \
"ds_read_b64_tr_b16 a[94:95], v7 offset:28672\n" \
"ds_read_b64_tr_b16 a[96:97], v6 offset:32768\n" \
"ds_read_b64_tr_b16 a[98:99], v6 offset:36864\n" \
"ds_read_b64_tr_b16 a[100:101], v7 offset:32768\n" \
"ds_read_b64_tr_b16 a[102:103], v7 offset:36864\n" \
"ds_read_b64_tr_b16 a[104:105], v6 offset:40960\n" \
"ds_read_b64_tr_b16 a[106:107], v6 offset:45056\n" \
"ds_read_b64_tr_b16 a[108:109], v7 offset:40960\n" \
"ds_read_b64_tr_b16 a[110:111], v7 offset:45056\n" \
"ds_read_b64_tr_b16 a[112:113], v6 offset:49152\n" \
"ds_read_b64_tr_b16 a[114:115], v6 offset:53248\n" \
"ds_read_b64_tr_b16 a[116:117], v7 offset:49152\n" \
"ds_read_b64_tr_b16 a[118:119], v7 offset:53248\n" \
"ds_read_b64_tr_b16 a[120:121], v6 offset:57344\n" \
"ds_read_b64_tr_b16 a[122:123], v6 offset:61440\n" \
"ds_read_b64_tr_b16 a[124:125], v7 offset:57344\n" \
"ds_read_b64_tr_b16 a[126:127], v7 offset:61440\n" \
"s_waitcnt lgkmcnt(0)\n" \
"s_barrier\n" \
"s_add_u32 m0, s56, 65536\n" \
"s_nop 0\n" \
"buffer_load_dwordx4 v0, s[36:39], s53 offen lds\n" \
"buffer_load_dwordx4 v1, s[40:43], s53 offen offset:1024 lds\n" \
"s_add_u32 s53, s53, s54\n" \
"s_add_u32 m0, s56, 73728\n" \
"s_nop 0\n" \
"buffer_load_dwordx4 v0, s[36:39], s53 offen lds\n" \
"buffer_load_dwordx4 v1, s[40:43], s53 offen offset:1024 lds\n" \
"s_add_u32 s53, s53, s54\n" \
"s_add_u32 m0, s56, 81920\n" \
"s_nop 0\n" \
"buffer_load_dwordx4 v0, s[36:39], s53 offen lds\n" \
"buffer_load_dwordx4 v1, s[40:43], s53 offen offset:1024 lds\n" \
"s_add_u32 s53, s53, s54\n" \
"s_add_u32 m0, s56, 90112\n" \
"s_nop 0\n" \
"buffer_load_dwordx4 v0, s[36:39], s53 offen lds\n" \
"buffer_load_dwordx4 v1, s[40:43], s53 offen offset:1024 lds\n" \
"s_add_u32 s53, s53, s54\n" \
"s_add_u32 m0, s56, 98304\n" \
"s_nop 0\n" \
"buffer_load_dwordx4 v0, s[36:39], s53 offen lds\n" \
"buffer_load_dwordx4 v1, s[40:43], s53 offen offset:1024 lds\n" \
"s_add_u32 s53, s53, s54\n" \
"s_add_u32 m0, s56, 106496\n" \
"s_nop 0\n" \
"buffer_load_dwordx4 v0, s[36:39], s53 offen lds\n" \
"buffer_load_dwordx4 v1, s[40:43], s53 offen offset:1024 lds\n" \
"s_add_u32 s53, s53, s54\n" \
"s_add_u32 m0, s56, 114688\n" \
"s_nop 0\n" \
"buffer_load_dwordx4 v0, s[36:39], s53 offen lds\n" \
"buffer_load_dwordx4 v1, s[40:43], s53 offen offset:1024 lds\n" \
"s_add_u32 s53, s53, s54\n" \
"s_add_u32 m0, s56, 122880\n" \
"s_nop 0\n" \
"buffer_load_dwordx4 v0, s[36:39], s53 offen lds\n" \
"buffer_load_dwordx4 v1, s[40:43], s53 offen offset:1024 lds\n" \
"s_add_u32 s53, s53, s54\n" \
"s_waitcnt vmcnt(16)\n" \
"s_barrier\n" \
"ds_read_b64_tr_b16 a[128:129], v4 offset:0\n" \
"ds_read_b64_tr_b16 a[130:131], v4 offset:4096\n" \
"ds_read_b64_tr_b16 a[132:133], v5 offset:0\n" \
"ds_read_b64_tr_b16 a[134:135], v5 offset:4096\n" \
"ds_read_b64_tr_b16 a[136:137], v4 offset:8192\n" \
"ds_read_b64_tr_b16 a[138:139], v4 offset:12288\n" \
"ds_read_b64_tr_b16 a[140:141], v5 offset:8192\n" \
"ds_read_b64_tr_b16 a[142:143], v5 offset:12288\n" \
"ds_read_b64_tr_b16 a[144:145], v4 offset:16384\n" \
"ds_read_b64_tr_b16 a[146:147], v4 offset:20480\n" \
"ds_read_b64_tr_b16 a[148:149], v5 offset:16384\n" \
"ds_read_b64_tr_b16 a[150:151], v5 offset:20480\n" \
"ds_read_b64_tr_b16 a[152:153], v4 offset:24576\n" \
"ds_read_b64_tr_b16 a[154:155], v4 offset:28672\n" \
"ds_read_b64_tr_b16 a[156:157], v5 offset:24576\n" \
"ds_read_b64_tr_b16 a[158:159], v5 offset:28672\n" \
"ds_read_b64_tr_b16 a[160:161], v4 offset:32768\n" \
"ds_read_b64_tr_b16 a[162:163], v4 offset:36864\n" \
"ds_read_b64_tr_b16 a[164:165], v5 offset:32768\n" \
"ds_read_b64_tr_b16 a[166:167], v5 offset:36864\n" \
"ds_read_b64_tr_b16 a[168:169], v4 offset:40960\n" \
"ds_read_b64_tr_b16 a[170:171], v4 offset:45056\n" \
"ds_read_b64_tr_b16 a[172:173], v5 offset:40960\n" \
"ds_read_b64_tr_b16 a[174:175], v5 offset:45056\n" \
"ds_read_b64_tr_b16 a[176:177], v4 offset:49152\n" \
"ds_read_b64_tr_b16 a[178:179], v4 offset:53248\n" \
"ds_read_b64_tr_b16 a[180:181], v5 offset:49152\n" \
"ds_read_b64_tr_b16 a[182:183], v5 offset:53248\n" \
"ds_read_b64_tr_b16 a[184:185], v4 offset:57344\n" \
"ds_read_b64_tr_b16 a[186:187], v4 offset:61440\n" \
"ds_read_b64_tr_b16 a[188:189], v5 offset:57344\n" \
"ds_read_b64_tr_b16 a[190:191], v5 offset:61440\n" \
"s_waitcnt lgkmcnt(0)\n" \
"s_waitcnt vmcnt(0)\n" \
"s_barrier\n" \
"ds_read_b64_tr_b16 a[192:193], v6 offset:0\n" \
"ds_read_b64_tr_b16 a[194:195], v6 offset:4096\n" \
"ds_read_b64_tr_b16 a[196:197], v7 offset:0\n" \
"ds_read_b64_tr_b16 a[198:199], v7 offset:4096\n" \
"ds_read_b64_tr_b16 a[200:201], v6 offset:8192\n" \
"ds_read_b64_tr_b16 a[202:203], v6 offset:12288\n" \
"ds_read_b64_tr_b16 a[204:205], v7 offset:8192\n" \
"ds_read_b64_tr_b16 a[206:207], v7 offset:12288\n" \
"ds_read_b64_tr_b16 a[208:209], v6 offset:16384\n" \
"ds_read_b64_tr_b16 a[210:211], v6 offset:20480\n" \
"ds_read_b64_tr_b16 a[212:213], v7 offset:16384\n" \
"ds_read_b64_tr_b16 a[214:215], v7 offset:20480\n" \
"ds_read_b64_tr_b16 a[216:217], v6 offset:24576\n" \
"ds_read_b64_tr_b16 a[218:219], v6 offset:28672\n" \
"ds_read_b64_tr_b16 a[220:221], v7 offset:24576\n" \
"ds_read_b64_tr_b16 a[222:223], v7 offset:28672\n" \
"ds_read_b64_tr_b16 a[224:225], v6 offset:32768\n" \
"ds_read_b64_tr_b16 a[226:227], v6 offset:36864\n" \
"ds_read_b64_tr_b16 a[228:229], v7 offset:32768\n" \
"ds_read_b64_tr_b16 a[230:231], v7 offset:36864\n" \
"ds_read_b64_tr_b16 a[232:233], v6 offset:40960\n" \
"ds_read_b64_tr_b16 a[234:235], v6 offset:45056\n" \
"ds_read_b64_tr_b16 a[236:237], v7 offset:40960\n" \
"ds_read_b64_tr_b16 a[238:239], v7 offset:45056\n" \
"ds_read_b64_tr_b16 a[240:241], v6 offset:49152\n" \
"ds_read_b64_tr_b16 a[242:243], v6 offset:53248\n" \
"ds_read_b64_tr_b16 a[244:245], v7 offset:49152\n" \
"ds_read_b64_tr_b16 a[246:247], v7 offset:53248\n" \
"ds_read_b64_tr_b16 a[248:249], v6 offset:57344\n" \
"ds_read_b64_tr_b16 a[250:251], v6 offset:61440\n" \
"ds_read_b64_tr_b16 a[252:253], v7 offset:57344\n" \
"ds_read_b64_tr_b16 a[254:255], v7 offset:61440\n" \
"s_waitcnt lgkmcnt(0)\n" \
"s_barrier\n" \
"s_mov_b32 s65, 0\n" \
"s_mov_b32 s69, 0\n" \
"Lsp_wait_%=:\n" \
"s_mov_b32 s58, 400000\n" \
"s_and_b32 s59, s49, s71\n" \
"s_mul_i32 s59, s59, s72\n" \
"s_add_u32 s52, s73, s59\n" \
"Lsp_poll_%=:\n" \
"buffer_load_dword v126, v122, s[44:47], s52 offen sc1\n" \
"s_waitcnt vmcnt(0)\n" \
"v_cmp_ge_u32 vcc, s49, v126\n" \
"s_cbranch_vccz Lsp_go_%=\n" \
"s_sleep 1\n" \
"s_sub_u32 s58, s58, 1\n" \
"s_cmp_lg_u32 s58, 0\n" \
"s_cbranch_scc1 Lsp_poll_%=\n" \
"s_mov_b32 s65, 1\n" \
"s_branch Lsp_end_%=\n" \
"Lsp_go_%=:\n" \
"s_and_b32 s59, s49, 1\n" \
"s_lshl_b32 s59, s59, 16\n" \
"s_add_u32 s59, s59, s57\n" \
"s_add_u32 m0, s59, 0\n" \
"s_add_u32 s64, s50, 0\n" \
"buffer_load_dwordx4 v120, s[44:47], s64 offen offset:0 sc1 lds\n" \
"buffer_load_dwordx4 v120, s[44:47], s64 offen offset:1024 sc1 lds\n" \
"buffer_load_dwordx4 v120, s[44:47], s64 offen offset:2048 sc1 lds\n" \
"buffer_load_dwordx4 v120, s[44:47], s64 offen offset:3072 sc1 lds\n" \
"s_add_u32 m0, s59, 8192\n" \
"s_add_u32 s64, s50, 8192\n" \
"buffer_load_dwordx4 v120, s[44:47], s64 offen offset:0 sc1 lds\n" \
"buffer_load_dwordx4 v120, s[44:47], s64 offen offset:1024 sc1 lds\n" \
"buffer_load_dwordx4 v120, s[44:47], s64 offen offset:2048 sc1 lds\n" \
"buffer_load_dwordx4 v120, s[44:47], s64 offen offset:3072 sc1 lds\n" \
"s_add_u32 m0, s59, 16384\n" \
"s_add_u32 s64, s50, 16384\n" \
"buffer_load_dwordx4 v120, s[44:47], s64 offen offset:0 sc1 lds\n" \
"buffer_load_dwordx4 v120, s[44:47], s64 offen offset:1024 sc1 lds\n" \
"buffer_load_dwordx4 v120, s[44:47], s64 offen offset:2048 sc1 lds\n" \
"buffer_load_dwordx4 v120, s[44:47], s64 offen offset:3072 sc1 lds\n" \
"s_add_u32 m0, s59, 24576\n" \
"s_add_u32 s64, s50, 24576\n" \
"buffer_load_dwordx4 v120, s[44:47], s64 offen offset:0 sc1 lds\n" \
"buffer_load_dwordx4 v120, s[44:47], s64 offen offset:1024 sc1 lds\n" \
"buffer_load_dwordx4 v120, s[44:47], s64 offen offset:2048 sc1 lds\n" \
"buffer_load_dwordx4 v120, s[44:47], s64 offen offset:3072 sc1 lds\n" \
"s_add_u32 s50, s50, s51\n" \
"s_add_u32 s49, s49, 1\n" \
"Lsp_seg_%=:\n" \
"s_and_b32 s59, s48, 1\n" \
"s_lshl_b32 s59, s59, 16\n" \
"v_add_u32 v124, s59, v120\n" \
"s_and_b32 s59, s49, s71\n" \
"s_mul_i32 s59, s59, s72\n" \
"s_add_u32 s52, s73, s59\n" \
"s_waitcnt vmcnt(12)\n" \
"s_barrier\n" \
"ds_read_b128 v[16:19], v124 offset:0\n" \
"ds_read_b128 v[20:23], v124 offset:32768\n" \
"ds_read_b128 v[24:27], v124 offset:1024\n" \
"ds_read_b128 v[28:31], v124 offset:33792\n" \
"ds_read_b128 v[32:35], v124 offset:2048\n" \
"ds_read_b128 v[36:39], v124 offset:34816\n" \
"s_waitcnt lgkmcnt(4)\n" \
"v_mfma_f32_16x16x32_bf16 v[0:3], a[0:3], v[16:19], 0\n" \
"ds_read_b128 v[40:43], v124 offset:3072\n" \
"v_mfma_f32_16x16x32_bf16 v[4:7], a[4:7], v[16:19], 0\n" \
"v_mfma_f32_16x16x32_bf16 v[8:11], a[0:3], v[20:23], 0\n" \
"ds_read_b128 v[44:47], v124 offset:35840\n" \
"v_mfma_f32_16x16x32_bf16 v[12:15], a[4:7], v[20:23], 0\n" \
"s_waitcnt lgkmcnt(4)\n" \
"v_mfma_f32_16x16x32_bf16 v[0:3], a[8:11], v[24:27], v[0:3]\n" \
"ds_read_b128 v[16:19], v124 offset:4096\n" \
"v_mfma_f32_16x16x32_bf16 v[4:7], a[12:15], v[24:27], v[4:7]\n" \
"v_mfma_f32_16x16x32_bf16 v[8:11], a[8:11], v[28:31], v[8:11]\n" \
"ds_read_b128 v[20:23], v124 offset:36864\n" \
"v_mfma_f32_16x16x32_bf16 v[12:15], a[12:15], v[28:31], v[12:15]\n" \
"s_waitcnt lgkmcnt(4)\n" \
"v_mfma_f32_16x16x32_bf16 v[0:3], a[16:19], v[32:35], v[0:3]\n" \
"ds_read_b128 v[24:27], v124 offset:5120\n" \
"v_mfma_f32_16x16x32_bf16 v[4:7], a[20:23], v[32:35], v[4:7]\n" \
"v_mfma_f32_16x16x32_bf16 v[8:11], a[16:19], v[36:39], v[8:11]\n" \
"ds_read_b128 v[28:31], v124 offset:37888\n" \
"v_mfma_f32_16x16x32_bf16 v[12:15], a[20:23], v[36:39], v[12:15]\n" \
"s_waitcnt lgkmcnt(4)\n" \
"v_mfma_f32_16x16x32_bf16 v[0:3], a[24:27], v[40:43], v[0:3]\n" \
"ds_read_b128 v[32:35], v124 offset:6144\n" \
"v_mfma_f32_16x16x32_bf16 v[4:7], a[28:31], v[40:43], v[4:7]\n" \
"v_mfma_f32_16x16x32_bf16 v[8:11], a[24:27], v[44:47], v[8:11]\n" \
"ds_read_b128 v[36:39], v124 offset:38912\n" \
"v_mfma_f32_16x16x32_bf16 v[12:15], a[28:31], v[44:47], v[12:15]\n" \
"s_waitcnt lgkmcnt(4)\n" \
"v_mfma_f32_16x16x32_bf16 v[0:3], a[32:35], v[16:19], v[0:3]\n" \
"ds_read_b128 v[40:43], v124 offset:7168\n" \
"v_mfma_f32_16x16x32_bf16 v[4:7], a[36:39], v[16:19], v[4:7]\n" \
"v_mfma_f32_16x16x32_bf16 v[8:11], a[32:35], v[20:23], v[8:11]\n" \
"ds_read_b128 v[44:47], v124 offset:39936\n" \
"v_mfma_f32_16x16x32_bf16 v[12:15], a[36:39], v[20:23], v[12:15]\n" \
"s_waitcnt lgkmcnt(4)\n" \
"v_mfma_f32_16x16x32_bf16 v[0:3], a[40:43], v[24:27], v[0:3]\n" \
"s_waitcnt vmcnt(8)\n" \
"s_barrier\n" \
"buffer_load_dword v126, v122, s[44:47], s52 offen sc1\n" \
"ds_read_b128 v[16:19], v124 offset:8192\n" \
"v_mfma_f32_16x16x32_bf16 v[4:7], a[44:47], v[24:27], v[4:7]\n" \
"v_mfma_f32_16x16x32_bf16 v[8:11], a[40:43], v[28:31], v[8:11]\n" \
"ds_read_b128 v[20:23], v124 offset:40960\n" \
"v_mfma_f32_16x16x32_bf16 v[12:15], a[44:47], v[28:31], v[12:15]\n" \
"s_waitcnt lgkmcnt(4)\n" \
"v_mfma_f32_16x16x32_bf16 v[0:3], a[48:51], v[32:35], v[0:3]\n" \
"ds_read_b128 v[24:27], v124 offset:9216\n" \
"v_mfma_f32_16x16x32_bf16 v[4:7], a[52:55], v[32:35], v[4:7]\n" \
"v_mfma_f32_16x16x32_bf16 v[8:11], a[48:51], v[36:39], v[8:11]\n" \
"ds_read_b128 v[28:31], v124 offset:41984\n" \
"v_mfma_f32_16x16x32_bf16 v[12:15], a[52:55], v[36:39], v[12:15]\n" \
"s_waitcnt lgkmcnt(4)\n" \
"v_mfma_f32_16x16x32_bf16 v[0:3], a[56:59], v[40:43], v[0:3]\n" \
"ds_read_b128 v[32:35], v124 offset:10240\n" \
"v_mfma_f32_16x16x32_bf16 v[4:7], a[60:63], v[40:43], v[4:7]\n" \
"v_mfma_f32_16x16x32_bf16 v[8:11], a[56:59], v[44:47], v[8:11]\n" \
"ds_read_b128 v[36:39], v124 offset:43008\n" \
"v_mfma_f32_16x16x32_bf16 v[12:15], a[60:63], v[44:47], v[12:15]\n" \
"s_waitcnt lgkmcnt(4)\n" \
"v_mfma_f32_16x16x32_bf16 v[0:3], a[64:67], v[16:19], v[0:3]\n" \
"ds_read_b128 v[40:43], v124 offset:11264\n" \
"v_mfma_f32_16x16x32_bf16 v[4:7], a[68:71], v[16:19], v[4:7]\n" \
"v_mfma_f32_16x16x32_bf16 v[8:11], a[64:67], v[20:23], v[8:11]\n" \
"ds_read_b128 v[44:47], v124 offset:44032\n" \
"v_mfma_f32_16x16x32_bf16 v[12:15], a[68:71], v[20:23], v[12:15]\n" \
"s_waitcnt lgkmcnt(4)\n" \
"v_mfma_f32_16x16x32_bf16 v[0:3], a[72:75], v[24:27], v[0:3]\n" \
"ds_read_b128 v[16:19], v124 offset:12288\n" \
"v_mfma_f32_16x16x32_bf16 v[4:7], a[76:79], v[24:27], v[4:7]\n" \
"v_mfma_f32_16x16x32_bf16 v[8:11], a[72:75], v[28:31], v[8:11]\n" \
"ds_read_b128 v[20:23], v124 offset:45056\n" \
"v_mfma_f32_16x16x32_bf16 v[12:15], a[76:79], v[28:31], v[12:15]\n" \
"s_waitcnt lgkmcnt(4)\n" \
"v_mfma_f32_16x16x32_bf16 v[0:3], a[80:83], v[32:35], v[0:3]\n" \
"ds_read_b128 v[24:27], v124 offset:13312\n" \
"v_mfma_f32_16x16x32_bf16 v[4:7], a[84:87], v[32:35], v[4:7]\n" \
"v_mfma_f32_16x16x32_bf16 v[8:11], a[80:83], v[36:39], v[8:11]\n" \
"ds_read_b128 v[28:31], v124 offset:46080\n" \
"v_mfma_f32_16x16x32_bf16 v[12:15], a[84:87], v[36:39], v[12:15]\n" \
"s_waitcnt lgkmcnt(4)\n" \
"v_mfma_f32_16x16x32_bf16 v[0:3], a[88:91], v[40:43], v[0:3]\n" \
"ds_read_b128 v[32:35], v124 offset:14336\n" \
"v_mfma_f32_16x16x32_bf16 v[4:7], a[92:95], v[40:43], v[4:7]\n" \
"v_mfma_f32_16x16x32_bf16 v[8:11], a[88:91], v[44:47], v[8:11]\n" \
"ds_read_b128 v[36:39], v124 offset:47104\n" \
"v_mfma_f32_16x16x32_bf16 v[12:15], a[92:95], v[44:47], v[12:15]\n" \
"s_waitcnt lgkmcnt(4)\n" \
"v_mfma_f32_16x16x32_bf16 v[0:3], a[96:99], v[16:19], v[0:3]\n" \
"ds_read_b128 v[40:43], v124 offset:15360\n" \
"v_mfma_f32_16x16x32_bf16 v[4:7], a[100:103], v[16:19], v[4:7]\n" \
"v_mfma_f32_16x16x32_bf16 v[8:11], a[96:99], v[20:23], v[8:11]\n" \
"ds_read_b128 v[44:47], v124 offset:48128\n" \
"v_mfma_f32_16x16x32_bf16 v[12:15], a[100:103], v[20:23], v[12:15]\n" \
"s_waitcnt lgkmcnt(4)\n" \
"v_mfma_f32_16x16x32_bf16 v[0:3], a[104:107], v[24:27], v[0:3]\n" \
"s_waitcnt vmcnt(5)\n" \
"s_barrier\n" \
"ds_read_b128 v[16:19], v124 offset:16384\n" \
"v_mfma_f32_16x16x32_bf16 v[4:7], a[108:111], v[24:27], v[4:7]\n" \
"v_mfma_f32_16x16x32_bf16 v[8:11], a[104:107], v[28:31], v[8:11]\n" \
"ds_read_b128 v[20:23], v124 offset:49152\n" \
"v_mfma_f32_16x16x32_bf16 v[12:15], a[108:111], v[28:31], v[12:15]\n" \
"s_waitcnt lgkmcnt(4)\n" \
"v_mfma_f32_16x16x32_bf16 v[0:3], a[112:115], v[32:35], v[0:3]\n" \
"ds_read_b128 v[24:27], v124 offset:17408\n" \
"v_mfma_f32_16x16x32_bf16 v[4:7], a[116:119], v[32:35], v[4:7]\n" \
"v_mfma_f32_16x16x32_bf16 v[8:11], a[112:115], v[36:39], v[8:11]\n" \
"ds_read_b128 v[28:31], v124 offset:50176\n" \
"v_mfma_f32_16x16x32_bf16 v[12:15], a[116:119], v[36:39], v[12:15]\n" \
"s_waitcnt lgkmcnt(4)\n" \
"v_mfma_f32_16x16x32_bf16 v[0:3], a[120:123], v[40:43], v[0:3]\n" \
"ds_read_b128 v[32:35], v124 offset:18432\n" \
"v_mfma_f32_16x16x32_bf16 v[4:7], a[124:127], v[40:43], v[4:7]\n" \
"v_mfma_f32_16x16x32_bf16 v[8:11], a[120:123], v[44:47], v[8:11]\n" \
"ds_read_b128 v[36:39], v124 offset:51200\n" \
"v_mfma_f32_16x16x32_bf16 v[12:15], a[124:127], v[44:47], v[12:15]\n" \
"s_waitcnt lgkmcnt(4)\n" \
"v_mfma_f32_16x16x32_bf16 v[0:3], a[128:131], v[16:19], v[0:3]\n" \
"ds_read_b128 v[40:43], v124 offset:19456\n" \
"v_mfma_f32_16x16x32_bf16 v[4:7], a[132:135], v[16:19], v[4:7]\n" \
"v_mfma_f32_16x16x32_bf16 v[8:11], a[128:131], v[20:23], v[8:11]\n" \
"ds_read_b128 v[44:47], v124 offset:52224\n" \
"v_mfma_f32_16x16x32_bf16 v[12:15], a[132:135], v[20:23], v[12:15]\n" \
"s_waitcnt lgkmcnt(4)\n" \
"v_mfma_f32_16x16x32_bf16 v[0:3], a[136:139], v[24:27], v[0:3]\n" \
"ds_read_b128 v[16:19], v124 offset:20480\n" \
"v_mfma_f32_16x16x32_bf16 v[4:7], a[140:143], v[24:27], v[4:7]\n" \
"v_mfma_f32_16x16x32_bf16 v[8:11], a[136:139], v[28:31], v[8:11]\n" \
"ds_read_b128 v[20:23], v124 offset:53248\n" \
"v_mfma_f32_16x16x32_bf16 v[12:15], a[140:143], v[28:31], v[12:15]\n" \
"s_waitcnt lgkmcnt(4)\n" \
"v_mfma_f32_16x16x32_bf16 v[0:3], a[144:147], v[32:35], v[0:3]\n" \
"ds_read_b128 v[24:27], v124 offset:21504\n" \
"v_mfma_f32_16x16x32_bf16 v[4:7], a[148:151], v[32:35], v[4:7]\n" \
"v_mfma_f32_16x16x32_bf16 v[8:11], a[144:147], v[36:39], v[8:11]\n" \
"ds_read_b128 v[28:31], v124 offset:54272\n" \
"v_mfma_f32_16x16x32_bf16 v[12:15], a[148:151], v[36:39], v[12:15]\n" \
"s_waitcnt lgkmcnt(4)\n" \
"v_mfma_f32_16x16x32_bf16 v[0:3], a[152:155], v[40:43], v[0:3]\n" \
"ds_read_b128 v[32:35], v124 offset:22528\n" \
"v_mfma_f32_16x16x32_bf16 v[4:7], a[156:159], v[40:43], v[4:7]\n" \
"v_mfma_f32_16x16x32_bf16 v[8:11], a[152:155], v[44:47], v[8:11]\n" \
"ds_read_b128 v[36:39], v124 offset:55296\n" \
"v_mfma_f32_16x16x32_bf16 v[12:15], a[156:159], v[44:47], v[12:15]\n" \
"s_waitcnt lgkmcnt(4)\n" \
"v_mfma_f32_16x16x32_bf16 v[0:3], a[160:163], v[16:19], v[0:3]\n" \
"ds_read_b128 v[40:43], v124 offset:23552\n" \
"v_mfma_f32_16x16x32_bf16 v[4:7], a[164:167], v[16:19], v[4:7]\n" \
"v_mfma_f32_16x16x32_bf16 v[8:11], a[160:163], v[20:23], v[8:11]\n" \
"ds_read_b128 v[44:47], v124 offset:56320\n" \
"v_mfma_f32_16x16x32_bf16 v[12:15], a[164:167], v[20:23], v[12:15]\n" \
"s_waitcnt lgkmcnt(4)\n" \
"v_mfma_f32_16x16x32_bf16 v[0:3], a[168:171], v[24:27], v[0:3]\n" \
"s_waitcnt vmcnt(0)\n" \
"v_cmp_ge_u32 vcc, s49, v126\n" \
"s_cmp_eq_u64 vcc, 0\n" \
"s_cselect_b32 s59, 1, 0\n" \
"s_cmp_lt_u32 s49, 64\n" \
"s_cselect_b32 s59, s59, 0\n" \
"v_mov_b32 v126, s59\n" \
"ds_write_b32 v123, v126\n" \
"s_waitcnt lgkmcnt(0)\n" \
"s_barrier\n" \
"v_mov_b32 v126, 133120\n" \
"ds_read_b32 v126, v126\n" \
"s_cmp_eq_u32 s69, 0\n" \
"s_cbranch_scc1 Lsp_nopub_%=\n" \
"v_mov_b32 v127, s48\n" \
"buffer_store_dword v127, off, s[44:47], s55 sc1\n" \
"s_mov_b32 s69, 0\n" \
"Lsp_nopub_%=:\n" \
"ds_read_b128 v[16:19], v124 offset:24576\n" \
"v_mfma_f32_16x16x32_bf16 v[4:7], a[172:175], v[24:27], v[4:7]\n" \
"v_mfma_f32_16x16x32_bf16 v[8:11], a[168:171], v[28:31], v[8:11]\n" \
"ds_read_b128 v[20:23], v124 offset:57344\n" \
"v_mfma_f32_16x16x32_bf16 v[12:15], a[172:175], v[28:31], v[12:15]\n" \
"v_mfma_f32_16x16x32_bf16 v[0:3], a[176:179], v[32:35], v[0:3]\n" \
"ds_read_b128 v[24:27], v124 offset:25600\n" \
"v_mfma_f32_16x16x32_bf16 v[4:7], a[180:183], v[32:35], v[4:7]\n" \
"v_mfma_f32_16x16x32_bf16 v[8:11], a[176:179], v[36:39], v[8:11]\n" \
"ds_read_b128 v[28:31], v124 offset:58368\n" \
"v_mfma_f32_16x16x32_bf16 v[12:15], a[180:183], v[36:39], v[12:15]\n" \
"v_mfma_f32_16x16x32_bf16 v[0:3], a[184:187], v[40:43], v[0:3]\n" \
"ds_read_b128 v[32:35], v124 offset:26624\n" \
"v_mfma_f32_16x16x32_bf16 v[4:7], a[188:191], v[40:43], v[4:7]\n" \
"v_mfma_f32_16x16x32_bf16 v[8:11], a[184:187], v[44:47], v[8:11]\n" \
"ds_read_b128 v[36:39], v124 offset:59392\n" \
"v_mfma_f32_16x16x32_bf16 v[12:15], a[188:191], v[44:47], v[12:15]\n" \
"s_waitcnt lgkmcnt(4)\n" \
"v_mfma_f32_16x16x32_bf16 v[0:3], a[192:195], v[16:19], v[0:3]\n" \
"ds_read_b128 v[40:43], v124 offset:27648\n" \
"v_mfma_f32_16x16x32_bf16 v[4:7], a[196:199], v[16:19], v[4:7]\n" \
"v_mfma_f32_16x16x32_bf16 v[8:11], a[192:195], v[20:23], v[8:11]\n" \
"ds_read_b128 v[44:47], v124 offset:60416\n" \
"v_mfma_f32_16x16x32_bf16 v[12:15], a[196:199], v[20:23], v[12:15]\n" \
"v_readfirstlane_b32 s68, v126\n" \
"s_cmp_eq_u32 s68, 0\n" \
"s_cbranch_scc1 Lsp_nopre_%=\n" \
"s_and_b32 s59, s49, 1\n" \
"s_lshl_b32 s59, s59, 16\n" \
"s_add_u32 s59, s59, s57\n" \
"s_add_u32 m0, s59, 0\n" \
"s_add_u32 s64, s50, 0\n" \
"buffer_load_dwordx4 v120, s[44:47], s64 offen offset:0 sc1 lds\n" \
"buffer_load_dwordx4 v120, s[44:47], s64 offen offset:1024 sc1 lds\n" \
"buffer_load_dwordx4 v120, s[44:47], s64 offen offset:2048 sc1 lds\n" \
"buffer_load_dwordx4 v120, s[44:47], s64 offen offset:3072 sc1 lds\n" \
"s_add_u32 m0, s59, 8192\n" \
"s_add_u32 s64, s50, 8192\n" \
"buffer_load_dwordx4 v120, s[44:47], s64 offen offset:0 sc1 lds\n" \
"buffer_load_dwordx4 v120, s[44:47], s64 offen offset:1024 sc1 lds\n" \
"buffer_load_dwordx4 v120, s[44:47], s64 offen offset:2048 sc1 lds\n" \
"buffer_load_dwordx4 v120, s[44:47], s64 offen offset:3072 sc1 lds\n" \
"s_add_u32 m0, s59, 16384\n" \
"s_add_u32 s64, s50, 16384\n" \
"buffer_load_dwordx4 v120, s[44:47], s64 offen offset:0 sc1 lds\n" \
"buffer_load_dwordx4 v120, s[44:47], s64 offen offset:1024 sc1 lds\n" \
"buffer_load_dwordx4 v120, s[44:47], s64 offen offset:2048 sc1 lds\n" \
"buffer_load_dwordx4 v120, s[44:47], s64 offen offset:3072 sc1 lds\n" \
"s_add_u32 m0, s59, 24576\n" \
"s_add_u32 s64, s50, 24576\n" \
"buffer_load_dwordx4 v120, s[44:47], s64 offen offset:0 sc1 lds\n" \
"buffer_load_dwordx4 v120, s[44:47], s64 offen offset:1024 sc1 lds\n" \
"buffer_load_dwordx4 v120, s[44:47], s64 offen offset:2048 sc1 lds\n" \
"buffer_load_dwordx4 v120, s[44:47], s64 offen offset:3072 sc1 lds\n" \
"s_add_u32 s50, s50, s51\n" \
"s_add_u32 s49, s49, 1\n" \
"Lsp_nopre_%=:\n" \
"s_waitcnt lgkmcnt(4)\n" \
"v_mfma_f32_16x16x32_bf16 v[0:3], a[200:203], v[24:27], v[0:3]\n" \
"ds_read_b128 v[16:19], v124 offset:28672\n" \
"v_mfma_f32_16x16x32_bf16 v[4:7], a[204:207], v[24:27], v[4:7]\n" \
"v_mfma_f32_16x16x32_bf16 v[8:11], a[200:203], v[28:31], v[8:11]\n" \
"ds_read_b128 v[20:23], v124 offset:61440\n" \
"v_mfma_f32_16x16x32_bf16 v[12:15], a[204:207], v[28:31], v[12:15]\n" \
"s_waitcnt lgkmcnt(4)\n" \
"v_mfma_f32_16x16x32_bf16 v[0:3], a[208:211], v[32:35], v[0:3]\n" \
"ds_read_b128 v[24:27], v124 offset:29696\n" \
"v_mfma_f32_16x16x32_bf16 v[4:7], a[212:215], v[32:35], v[4:7]\n" \
"v_mfma_f32_16x16x32_bf16 v[8:11], a[208:211], v[36:39], v[8:11]\n" \
"ds_read_b128 v[28:31], v124 offset:62464\n" \
"v_mfma_f32_16x16x32_bf16 v[12:15], a[212:215], v[36:39], v[12:15]\n" \
"s_waitcnt lgkmcnt(4)\n" \
"v_mfma_f32_16x16x32_bf16 v[0:3], a[216:219], v[40:43], v[0:3]\n" \
"ds_read_b128 v[32:35], v124 offset:30720\n" \
"v_mfma_f32_16x16x32_bf16 v[4:7], a[220:223], v[40:43], v[4:7]\n" \
"v_mfma_f32_16x16x32_bf16 v[8:11], a[216:219], v[44:47], v[8:11]\n" \
"ds_read_b128 v[36:39], v124 offset:63488\n" \
"v_mfma_f32_16x16x32_bf16 v[12:15], a[220:223], v[44:47], v[12:15]\n" \
"s_waitcnt lgkmcnt(4)\n" \
"v_mfma_f32_16x16x32_bf16 v[0:3], a[224:227], v[16:19], v[0:3]\n" \
"ds_read_b128 v[40:43], v124 offset:31744\n" \
"v_mfma_f32_16x16x32_bf16 v[4:7], a[228:231], v[16:19], v[4:7]\n" \
"v_mfma_f32_16x16x32_bf16 v[8:11], a[224:227], v[20:23], v[8:11]\n" \
"ds_read_b128 v[44:47], v124 offset:64512\n" \
"v_mfma_f32_16x16x32_bf16 v[12:15], a[228:231], v[20:23], v[12:15]\n" \
"s_waitcnt lgkmcnt(4)\n" \
"v_mfma_f32_16x16x32_bf16 v[0:3], a[232:235], v[24:27], v[0:3]\n" \
"v_mfma_f32_16x16x32_bf16 v[4:7], a[236:239], v[24:27], v[4:7]\n" \
"v_mfma_f32_16x16x32_bf16 v[8:11], a[232:235], v[28:31], v[8:11]\n" \
"v_mfma_f32_16x16x32_bf16 v[12:15], a[236:239], v[28:31], v[12:15]\n" \
"s_waitcnt lgkmcnt(2)\n" \
"v_mfma_f32_16x16x32_bf16 v[0:3], a[240:243], v[32:35], v[0:3]\n" \
"v_mfma_f32_16x16x32_bf16 v[4:7], a[244:247], v[32:35], v[4:7]\n" \
"v_mfma_f32_16x16x32_bf16 v[8:11], a[240:243], v[36:39], v[8:11]\n" \
"v_mfma_f32_16x16x32_bf16 v[12:15], a[244:247], v[36:39], v[12:15]\n" \
"s_waitcnt lgkmcnt(0)\n" \
"v_mfma_f32_16x16x32_bf16 v[0:3], a[248:251], v[40:43], v[0:3]\n" \
"v_mfma_f32_16x16x32_bf16 v[4:7], a[252:255], v[40:43], v[4:7]\n" \
"v_mfma_f32_16x16x32_bf16 v[8:11], a[248:251], v[44:47], v[8:11]\n" \
"v_mfma_f32_16x16x32_bf16 v[12:15], a[252:255], v[44:47], v[12:15]\n" \
"s_add_u32 s64, s60, 65536\n" \
"buffer_store_dwordx4 v[0:3], v121, s[44:47], s60 offen offset:0 sc1\n" \
"buffer_store_dwordx4 v[4:7], v121, s[44:47], s60 offen offset:256 sc1\n" \
"s_nop 0\n" \
"buffer_store_dwordx4 v[8:11], v121, s[44:47], s64 offen offset:0 sc1\n" \
"s_nop 2\n" \
"buffer_store_dwordx4 v[12:15], v121, s[44:47], s64 offen offset:256 sc1\n" \
"s_add_u32 s60, s60, s61\n" \
"s_add_u32 s48, s48, 1\n" \
"s_mov_b32 s69, 1\n" \
"s_cmp_lg_u32 s68, 0\n" \
"s_cbranch_scc1 Lsp_seg_%=\n" \
"s_waitcnt vmcnt(0)\n" \
"s_barrier\n" \
"v_mov_b32 v127, s48\n" \
"buffer_store_dword v127, off, s[44:47], s55 sc1\n" \
"s_mov_b32 s69, 0\n" \
"s_cmp_lt_u32 s48, 64\n" \
"s_cbranch_scc1 Lsp_wait_%=\n" \
"Lsp_end_%=:\n" \
"s_waitcnt vmcnt(0) lgkmcnt(0)\n"
#define ASM_SG \
"s_add_u32 m0, s56, 0\n" \
"s_nop 0\n" \
"buffer_load_dwordx4 v0, s[36:39], s53 offen lds\n" \
"buffer_load_dwordx4 v1, s[40:43], s53 offen offset:1024 lds\n" \
"s_add_u32 s53, s53, s54\n" \
"s_add_u32 m0, s56, 8192\n" \
"s_nop 0\n" \
"buffer_load_dwordx4 v0, s[36:39], s53 offen lds\n" \
"buffer_load_dwordx4 v1, s[40:43], s53 offen offset:1024 lds\n" \
"s_add_u32 s53, s53, s54\n" \
"s_add_u32 m0, s56, 16384\n" \
"s_nop 0\n" \
"buffer_load_dwordx4 v0, s[36:39], s53 offen lds\n" \
"buffer_load_dwordx4 v1, s[40:43], s53 offen offset:1024 lds\n" \
"s_add_u32 s53, s53, s54\n" \
"s_add_u32 m0, s56, 24576\n" \
"s_nop 0\n" \
"buffer_load_dwordx4 v0, s[36:39], s53 offen lds\n" \
"buffer_load_dwordx4 v1, s[40:43], s53 offen offset:1024 lds\n" \
"s_add_u32 s53, s53, s54\n" \
"s_add_u32 m0, s56, 32768\n" \
"s_nop 0\n" \
"buffer_load_dwordx4 v0, s[36:39], s53 offen lds\n" \
"buffer_load_dwordx4 v1, s[40:43], s53 offen offset:1024 lds\n" \
"s_add_u32 s53, s53, s54\n" \
"s_add_u32 m0, s56, 40960\n" \
"s_nop 0\n" \
"buffer_load_dwordx4 v0, s[36:39], s53 offen lds\n" \
"buffer_load_dwordx4 v1, s[40:43], s53 offen offset:1024 lds\n" \
"s_add_u32 s53, s53, s54\n" \
"s_add_u32 m0, s56, 49152\n" \
"s_nop 0\n" \
"buffer_load_dwordx4 v0, s[36:39], s53 offen lds\n" \
"buffer_load_dwordx4 v1, s[40:43], s53 offen offset:1024 lds\n" \
"s_add_u32 s53, s53, s54\n" \
"s_add_u32 m0, s56, 57344\n" \
"s_nop 0\n" \
"buffer_load_dwordx4 v0, s[36:39], s53 offen lds\n" \
"buffer_load_dwordx4 v1, s[40:43], s53 offen offset:1024 lds\n" \
"s_add_u32 s53, s53, s54\n" \
"s_add_u32 m0, s56, 65536\n" \
"s_nop 0\n" \
"buffer_load_dwordx4 v0, s[36:39], s53 offen lds\n" \
"buffer_load_dwordx4 v1, s[40:43], s53 offen offset:1024 lds\n" \
"s_add_u32 s53, s53, s54\n" \
"s_add_u32 m0, s56, 73728\n" \
"s_nop 0\n" \
"buffer_load_dwordx4 v0, s[36:39], s53 offen lds\n" \
"buffer_load_dwordx4 v1, s[40:43], s53 offen offset:1024 lds\n" \
"s_add_u32 s53, s53, s54\n" \
"s_add_u32 m0, s56, 81920\n" \
"s_nop 0\n" \
"buffer_load_dwordx4 v0, s[36:39], s53 offen lds\n" \
"buffer_load_dwordx4 v1, s[40:43], s53 offen offset:1024 lds\n" \
"s_add_u32 s53, s53, s54\n" \
"s_add_u32 m0, s56, 90112\n" \
"s_nop 0\n" \
"buffer_load_dwordx4 v0, s[36:39], s53 offen lds\n" \
"buffer_load_dwordx4 v1, s[40:43], s53 offen offset:1024 lds\n" \
"s_add_u32 s53, s53, s54\n" \
"s_add_u32 m0, s56, 98304\n" \
"s_nop 0\n" \
"buffer_load_dwordx4 v0, s[36:39], s53 offen lds\n" \
"buffer_load_dwordx4 v1, s[40:43], s53 offen offset:1024 lds\n" \
"s_add_u32 s53, s53, s54\n" \
"s_add_u32 m0, s56, 106496\n" \
"s_nop 0\n" \
"buffer_load_dwordx4 v0, s[36:39], s53 offen lds\n" \
"buffer_load_dwordx4 v1, s[40:43], s53 offen offset:1024 lds\n" \
"s_add_u32 s53, s53, s54\n" \
"s_add_u32 m0, s56, 114688\n" \
"s_nop 0\n" \
"buffer_load_dwordx4 v0, s[36:39], s53 offen lds\n" \
"buffer_load_dwordx4 v1, s[40:43], s53 offen offset:1024 lds\n" \
"s_add_u32 s53, s53, s54\n" \
"s_add_u32 m0, s56, 122880\n" \
"s_nop 0\n" \
"buffer_load_dwordx4 v0, s[36:39], s53 offen lds\n" \
"buffer_load_dwordx4 v1, s[40:43], s53 offen offset:1024 lds\n" \
"s_add_u32 s53, s53, s54\n" \
"s_waitcnt vmcnt(16)\n" \
"s_barrier\n" \
"ds_read_b64_tr_b16 a[0:1], v4 offset:0\n" \
"ds_read_b64_tr_b16 a[2:3], v4 offset:4096\n" \
"ds_read_b64_tr_b16 a[4:5], v5 offset:0\n" \
"ds_read_b64_tr_b16 a[6:7], v5 offset:4096\n" \
"ds_read_b64_tr_b16 a[8:9], v4 offset:8192\n" \
"ds_read_b64_tr_b16 a[10:11], v4 offset:12288\n" \
"ds_read_b64_tr_b16 a[12:13], v5 offset:8192\n" \
"ds_read_b64_tr_b16 a[14:15], v5 offset:12288\n" \
"ds_read_b64_tr_b16 a[16:17], v4 offset:16384\n" \
"ds_read_b64_tr_b16 a[18:19], v4 offset:20480\n" \
"ds_read_b64_tr_b16 a[20:21], v5 offset:16384\n" \
"ds_read_b64_tr_b16 a[22:23], v5 offset:20480\n" \
"ds_read_b64_tr_b16 a[24:25], v4 offset:24576\n" \
"ds_read_b64_tr_b16 a[26:27], v4 offset:28672\n" \
"ds_read_b64_tr_b16 a[28:29], v5 offset:24576\n" \
"ds_read_b64_tr_b16 a[30:31], v5 offset:28672\n" \
"ds_read_b64_tr_b16 a[32:33], v4 offset:32768\n" \
"ds_read_b64_tr_b16 a[34:35], v4 offset:36864\n" \
"ds_read_b64_tr_b16 a[36:37], v5 offset:32768\n" \
"ds_read_b64_tr_b16 a[38:39], v5 offset:36864\n" \
"ds_read_b64_tr_b16 a[40:41], v4 offset:40960\n" \
"ds_read_b64_tr_b16 a[42:43], v4 offset:45056\n" \
"ds_read_b64_tr_b16 a[44:45], v5 offset:40960\n" \
"ds_read_b64_tr_b16 a[46:47], v5 offset:45056\n" \
"ds_read_b64_tr_b16 a[48:49], v4 offset:49152\n" \
"ds_read_b64_tr_b16 a[50:51], v4 offset:53248\n" \
"ds_read_b64_tr_b16 a[52:53], v5 offset:49152\n" \
"ds_read_b64_tr_b16 a[54:55], v5 offset:53248\n" \
"ds_read_b64_tr_b16 a[56:57], v4 offset:57344\n" \
"ds_read_b64_tr_b16 a[58:59], v4 offset:61440\n" \
"ds_read_b64_tr_b16 a[60:61], v5 offset:57344\n" \
"ds_read_b64_tr_b16 a[62:63], v5 offset:61440\n" \
"s_waitcnt lgkmcnt(0)\n" \
"s_barrier\n" \
"s_add_u32 m0, s56, 0\n" \
"s_nop 0\n" \
"buffer_load_dwordx4 v0, s[36:39], s53 offen lds\n" \
"buffer_load_dwordx4 v1, s[40:43], s53 offen offset:1024 lds\n" \
"s_add_u32 s53, s53, s54\n" \
"s_add_u32 m0, s56, 8192\n" \
"s_nop 0\n" \
"buffer_load_dwordx4 v0, s[36:39], s53 offen lds\n" \
"buffer_load_dwordx4 v1, s[40:43], s53 offen offset:1024 lds\n" \
"s_add_u32 s53, s53, s54\n" \
"s_add_u32 m0, s56, 16384\n" \
"s_nop 0\n" \
"buffer_load_dwordx4 v0, s[36:39], s53 offen lds\n" \
"buffer_load_dwordx4 v1, s[40:43], s53 offen offset:1024 lds\n" \
"s_add_u32 s53, s53, s54\n" \
"s_add_u32 m0, s56, 24576\n" \
"s_nop 0\n" \
"buffer_load_dwordx4 v0, s[36:39], s53 offen lds\n" \
"buffer_load_dwordx4 v1, s[40:43], s53 offen offset:1024 lds\n" \
"s_add_u32 s53, s53, s54\n" \
"s_add_u32 m0, s56, 32768\n" \
"s_nop 0\n" \
"buffer_load_dwordx4 v0, s[36:39], s53 offen lds\n" \
"buffer_load_dwordx4 v1, s[40:43], s53 offen offset:1024 lds\n" \
"s_add_u32 s53, s53, s54\n" \
"s_add_u32 m0, s56, 40960\n" \
"s_nop 0\n" \
"buffer_load_dwordx4 v0, s[36:39], s53 offen lds\n" \
"buffer_load_dwordx4 v1, s[40:43], s53 offen offset:1024 lds\n" \
"s_add_u32 s53, s53, s54\n" \
"s_add_u32 m0, s56, 49152\n" \
"s_nop 0\n" \
"buffer_load_dwordx4 v0, s[36:39], s53 offen lds\n" \
"buffer_load_dwordx4 v1, s[40:43], s53 offen offset:1024 lds\n" \
"s_add_u32 s53, s53, s54\n" \
"s_add_u32 m0, s56, 57344\n" \
"s_nop 0\n" \
"buffer_load_dwordx4 v0, s[36:39], s53 offen lds\n" \
"buffer_load_dwordx4 v1, s[40:43], s53 offen offset:1024 lds\n" \
"s_add_u32 s53, s53, s54\n" \
"s_waitcnt vmcnt(16)\n" \
"s_barrier\n" \
"ds_read_b64_tr_b16 a[64:65], v6 offset:0\n" \
"ds_read_b64_tr_b16 a[66:67], v6 offset:4096\n" \
"ds_read_b64_tr_b16 a[68:69], v7 offset:0\n" \
"ds_read_b64_tr_b16 a[70:71], v7 offset:4096\n" \
"ds_read_b64_tr_b16 a[72:73], v6 offset:8192\n" \
"ds_read_b64_tr_b16 a[74:75], v6 offset:12288\n" \
"ds_read_b64_tr_b16 a[76:77], v7 offset:8192\n" \
"ds_read_b64_tr_b16 a[78:79], v7 offset:12288\n" \
"ds_read_b64_tr_b16 a[80:81], v6 offset:16384\n" \
"ds_read_b64_tr_b16 a[82:83], v6 offset:20480\n" \
"ds_read_b64_tr_b16 a[84:85], v7 offset:16384\n" \
"ds_read_b64_tr_b16 a[86:87], v7 offset:20480\n" \
"ds_read_b64_tr_b16 a[88:89], v6 offset:24576\n" \
"ds_read_b64_tr_b16 a[90:91], v6 offset:28672\n" \
"ds_read_b64_tr_b16 a[92:93], v7 offset:24576\n" \
"ds_read_b64_tr_b16 a[94:95], v7 offset:28672\n" \
"ds_read_b64_tr_b16 a[96:97], v6 offset:32768\n" \
"ds_read_b64_tr_b16 a[98:99], v6 offset:36864\n" \
"ds_read_b64_tr_b16 a[100:101], v7 offset:32768\n" \
"ds_read_b64_tr_b16 a[102:103], v7 offset:36864\n" \
"ds_read_b64_tr_b16 a[104:105], v6 offset:40960\n" \
"ds_read_b64_tr_b16 a[106:107], v6 offset:45056\n" \
"ds_read_b64_tr_b16 a[108:109], v7 offset:40960\n" \
"ds_read_b64_tr_b16 a[110:111], v7 offset:45056\n" \
"ds_read_b64_tr_b16 a[112:113], v6 offset:49152\n" \
"ds_read_b64_tr_b16 a[114:115], v6 offset:53248\n" \
"ds_read_b64_tr_b16 a[116:117], v7 offset:49152\n" \
"ds_read_b64_tr_b16 a[118:119], v7 offset:53248\n" \
"ds_read_b64_tr_b16 a[120:121], v6 offset:57344\n" \
"ds_read_b64_tr_b16 a[122:123], v6 offset:61440\n" \
"ds_read_b64_tr_b16 a[124:125], v7 offset:57344\n" \
"ds_read_b64_tr_b16 a[126:127], v7 offset:61440\n" \
"s_waitcnt lgkmcnt(0)\n" \
"s_barrier\n" \
"s_add_u32 m0, s56, 65536\n" \
"s_nop 0\n" \
"buffer_load_dwordx4 v0, s[36:39], s53 offen lds\n" \
"buffer_load_dwordx4 v1, s[40:43], s53 offen offset:1024 lds\n" \
"s_add_u32 s53, s53, s54\n" \
"s_add_u32 m0, s56, 73728\n" \
"s_nop 0\n" \
"buffer_load_dwordx4 v0, s[36:39], s53 offen lds\n" \
"buffer_load_dwordx4 v1, s[40:43], s53 offen offset:1024 lds\n" \
"s_add_u32 s53, s53, s54\n" \
"s_add_u32 m0, s56, 81920\n" \
"s_nop 0\n" \
"buffer_load_dwordx4 v0, s[36:39], s53 offen lds\n" \
"buffer_load_dwordx4 v1, s[40:43], s53 offen offset:1024 lds\n" \
"s_add_u32 s53, s53, s54\n" \
"s_add_u32 m0, s56, 90112\n" \
"s_nop 0\n" \
"buffer_load_dwordx4 v0, s[36:39], s53 offen lds\n" \
"buffer_load_dwordx4 v1, s[40:43], s53 offen offset:1024 lds\n" \
"s_add_u32 s53, s53, s54\n" \
"s_add_u32 m0, s56, 98304\n" \
"s_nop 0\n" \
"buffer_load_dwordx4 v0, s[36:39], s53 offen lds\n" \
"buffer_load_dwordx4 v1, s[40:43], s53 offen offset:1024 lds\n" \
"s_add_u32 s53, s53, s54\n" \
"s_add_u32 m0, s56, 106496\n" \
"s_nop 0\n" \
"buffer_load_dwordx4 v0, s[36:39], s53 offen lds\n" \
"buffer_load_dwordx4 v1, s[40:43], s53 offen offset:1024 lds\n" \
"s_add_u32 s53, s53, s54\n" \
"s_add_u32 m0, s56, 114688\n" \
"s_nop 0\n" \
"buffer_load_dwordx4 v0, s[36:39], s53 offen lds\n" \
"buffer_load_dwordx4 v1, s[40:43], s53 offen offset:1024 lds\n" \
"s_add_u32 s53, s53, s54\n" \
"s_add_u32 m0, s56, 122880\n" \
"s_nop 0\n" \
"buffer_load_dwordx4 v0, s[36:39], s53 offen lds\n" \
"buffer_load_dwordx4 v1, s[40:43], s53 offen offset:1024 lds\n" \
"s_add_u32 s53, s53, s54\n" \
"s_waitcnt vmcnt(16)\n" \
"s_barrier\n" \
"ds_read_b64_tr_b16 a[128:129], v4 offset:0\n" \
"ds_read_b64_tr_b16 a[130:131], v4 offset:4096\n" \
"ds_read_b64_tr_b16 a[132:133], v5 offset:0\n" \
"ds_read_b64_tr_b16 a[134:135], v5 offset:4096\n" \
"ds_read_b64_tr_b16 a[136:137], v4 offset:8192\n" \
"ds_read_b64_tr_b16 a[138:139], v4 offset:12288\n" \
"ds_read_b64_tr_b16 a[140:141], v5 offset:8192\n" \
"ds_read_b64_tr_b16 a[142:143], v5 offset:12288\n" \
"ds_read_b64_tr_b16 a[144:145], v4 offset:16384\n" \
"ds_read_b64_tr_b16 a[146:147], v4 offset:20480\n" \
"ds_read_b64_tr_b16 a[148:149], v5 offset:16384\n" \
"ds_read_b64_tr_b16 a[150:151], v5 offset:20480\n" \
"ds_read_b64_tr_b16 a[152:153], v4 offset:24576\n" \
"ds_read_b64_tr_b16 a[154:155], v4 offset:28672\n" \
"ds_read_b64_tr_b16 a[156:157], v5 offset:24576\n" \
"ds_read_b64_tr_b16 a[158:159], v5 offset:28672\n" \
"ds_read_b64_tr_b16 a[160:161], v4 offset:32768\n" \
"ds_read_b64_tr_b16 a[162:163], v4 offset:36864\n" \
"ds_read_b64_tr_b16 a[164:165], v5 offset:32768\n" \
"ds_read_b64_tr_b16 a[166:167], v5 offset:36864\n" \
"ds_read_b64_tr_b16 a[168:169], v4 offset:40960\n" \
"ds_read_b64_tr_b16 a[170:171], v4 offset:45056\n" \
"ds_read_b64_tr_b16 a[172:173], v5 offset:40960\n" \
"ds_read_b64_tr_b16 a[174:175], v5 offset:45056\n" \
"ds_read_b64_tr_b16 a[176:177], v4 offset:49152\n" \
"ds_read_b64_tr_b16 a[178:179], v4 offset:53248\n" \
"ds_read_b64_tr_b16 a[180:181], v5 offset:49152\n" \
"ds_read_b64_tr_b16 a[182:183], v5 offset:53248\n" \
"ds_read_b64_tr_b16 a[184:185], v4 offset:57344\n" \
"ds_read_b64_tr_b16 a[186:187], v4 offset:61440\n" \
"ds_read_b64_tr_b16 a[188:189], v5 offset:57344\n" \
"ds_read_b64_tr_b16 a[190:191], v5 offset:61440\n" \
"s_waitcnt lgkmcnt(0)\n" \
"s_waitcnt vmcnt(0)\n" \
"s_barrier\n" \
"ds_read_b64_tr_b16 a[192:193], v6 offset:0\n" \
"ds_read_b64_tr_b16 a[194:195], v6 offset:4096\n" \
"ds_read_b64_tr_b16 a[196:197], v7 offset:0\n" \
"ds_read_b64_tr_b16 a[198:199], v7 offset:4096\n" \
"ds_read_b64_tr_b16 a[200:201], v6 offset:8192\n" \
"ds_read_b64_tr_b16 a[202:203], v6 offset:12288\n" \
"ds_read_b64_tr_b16 a[204:205], v7 offset:8192\n" \
"ds_read_b64_tr_b16 a[206:207], v7 offset:12288\n" \
"ds_read_b64_tr_b16 a[208:209], v6 offset:16384\n" \
"ds_read_b64_tr_b16 a[210:211], v6 offset:20480\n" \
"ds_read_b64_tr_b16 a[212:213], v7 offset:16384\n" \
"ds_read_b64_tr_b16 a[214:215], v7 offset:20480\n" \
"ds_read_b64_tr_b16 a[216:217], v6 offset:24576\n" \
"ds_read_b64_tr_b16 a[218:219], v6 offset:28672\n" \
"ds_read_b64_tr_b16 a[220:221], v7 offset:24576\n" \
"ds_read_b64_tr_b16 a[222:223], v7 offset:28672\n" \
"ds_read_b64_tr_b16 a[224:225], v6 offset:32768\n" \
"ds_read_b64_tr_b16 a[226:227], v6 offset:36864\n" \
"ds_read_b64_tr_b16 a[228:229], v7 offset:32768\n" \
"ds_read_b64_tr_b16 a[230:231], v7 offset:36864\n" \
"ds_read_b64_tr_b16 a[232:233], v6 offset:40960\n" \
"ds_read_b64_tr_b16 a[234:235], v6 offset:45056\n" \
"ds_read_b64_tr_b16 a[236:237], v7 offset:40960\n" \
"ds_read_b64_tr_b16 a[238:239], v7 offset:45056\n" \
"ds_read_b64_tr_b16 a[240:241], v6 offset:49152\n" \
"ds_read_b64_tr_b16 a[242:243], v6 offset:53248\n" \
"ds_read_b64_tr_b16 a[244:245], v7 offset:49152\n" \
"ds_read_b64_tr_b16 a[246:247], v7 offset:53248\n" \
"ds_read_b64_tr_b16 a[248:249], v6 offset:57344\n" \
"ds_read_b64_tr_b16 a[250:251], v6 offset:61440\n" \
"ds_read_b64_tr_b16 a[252:253], v7 offset:57344\n" \
"ds_read_b64_tr_b16 a[254:255], v7 offset:61440\n" \
"s_waitcnt lgkmcnt(0)\n" \
"s_barrier\n" \
"s_mov_b32 s67, 0xbfb8aa3b\n" \
"s_mov_b32 s65, 0\n" \
"s_mov_b32 s69, 0\n" \
"Lsg_wait_%=:\n" \
"s_mov_b32 s58, 400000\n" \
"s_and_b32 s59, s49, s71\n" \
"s_mul_i32 s59, s59, s72\n" \
"s_add_u32 s52, s73, s59\n" \
"Lsg_poll_%=:\n" \
"buffer_load_dword v126, v122, s[44:47], s52 offen sc1\n" \
"s_waitcnt vmcnt(0)\n" \
"v_cmp_ge_u32 vcc, s49, v126\n" \
"s_cbranch_vccz Lsg_go_%=\n" \
"s_sleep 1\n" \
"s_sub_u32 s58, s58, 1\n" \
"s_cmp_lg_u32 s58, 0\n" \
"s_cbranch_scc1 Lsg_poll_%=\n" \
"s_mov_b32 s65, 1\n" \
"s_branch Lsg_end_%=\n" \
"Lsg_go_%=:\n" \
"s_and_b32 s59, s49, 1\n" \
"s_lshl_b32 s59, s59, 16\n" \
"s_add_u32 s59, s59, s57\n" \
"s_add_u32 m0, s59, 0\n" \
"s_add_u32 s64, s50, 0\n" \
"buffer_load_dwordx4 v120, s[44:47], s64 offen offset:0 sc1 lds\n" \
"buffer_load_dwordx4 v120, s[44:47], s64 offen offset:1024 sc1 lds\n" \
"buffer_load_dwordx4 v120, s[44:47], s64 offen offset:2048 sc1 lds\n" \
"buffer_load_dwordx4 v120, s[44:47], s64 offen offset:3072 sc1 lds\n" \
"s_add_u32 m0, s59, 8192\n" \
"s_add_u32 s64, s50, 8192\n" \
"buffer_load_dwordx4 v120, s[44:47], s64 offen offset:0 sc1 lds\n" \
"buffer_load_dwordx4 v120, s[44:47], s64 offen offset:1024 sc1 lds\n" \
"buffer_load_dwordx4 v120, s[44:47], s64 offen offset:2048 sc1 lds\n" \
"buffer_load_dwordx4 v120, s[44:47], s64 offen offset:3072 sc1 lds\n" \
"s_add_u32 m0, s59, 16384\n" \
"s_add_u32 s64, s50, 16384\n" \
"buffer_load_dwordx4 v120, s[44:47], s64 offen offset:0 sc1 lds\n" \
"buffer_load_dwordx4 v120, s[44:47], s64 offen offset:1024 sc1 lds\n" \
"buffer_load_dwordx4 v120, s[44:47], s64 offen offset:2048 sc1 lds\n" \
"buffer_load_dwordx4 v120, s[44:47], s64 offen offset:3072 sc1 lds\n" \
"s_add_u32 m0, s59, 24576\n" \
"s_add_u32 s64, s50, 24576\n" \
"buffer_load_dwordx4 v120, s[44:47], s64 offen offset:0 sc1 lds\n" \
"buffer_load_dwordx4 v120, s[44:47], s64 offen offset:1024 sc1 lds\n" \
"buffer_load_dwordx4 v120, s[44:47], s64 offen offset:2048 sc1 lds\n" \
"buffer_load_dwordx4 v120, s[44:47], s64 offen offset:3072 sc1 lds\n" \
"s_add_u32 s50, s50, s51\n" \
"s_add_u32 s49, s49, 1\n" \
"Lsg_seg_%=:\n" \
"s_and_b32 s59, s48, 1\n" \
"s_lshl_b32 s59, s59, 16\n" \
"v_add_u32 v124, s59, v120\n" \
"s_and_b32 s59, s49, s71\n" \
"s_mul_i32 s59, s59, s72\n" \
"s_add_u32 s52, s73, s59\n" \
"s_waitcnt vmcnt(12)\n" \
"s_barrier\n" \
"ds_read_b128 v[16:19], v124 offset:0\n" \
"ds_read_b128 v[20:23], v124 offset:32768\n" \
"ds_read_b128 v[24:27], v124 offset:1024\n" \
"ds_read_b128 v[28:31], v124 offset:33792\n" \
"ds_read_b128 v[32:35], v124 offset:2048\n" \
"ds_read_b128 v[36:39], v124 offset:34816\n" \
"s_waitcnt lgkmcnt(4)\n" \
"v_mfma_f32_16x16x32_bf16 v[0:3], a[0:3], v[16:19], 0\n" \
"ds_read_b128 v[40:43], v124 offset:3072\n" \
"v_mfma_f32_16x16x32_bf16 v[4:7], a[4:7], v[16:19], 0\n" \
"v_mfma_f32_16x16x32_bf16 v[8:11], a[0:3], v[20:23], 0\n" \
"ds_read_b128 v[44:47], v124 offset:35840\n" \
"v_mfma_f32_16x16x32_bf16 v[12:15], a[4:7], v[20:23], 0\n" \
"s_waitcnt lgkmcnt(4)\n" \
"v_mfma_f32_16x16x32_bf16 v[0:3], a[8:11], v[24:27], v[0:3]\n" \
"ds_read_b128 v[16:19], v124 offset:4096\n" \
"v_mfma_f32_16x16x32_bf16 v[4:7], a[12:15], v[24:27], v[4:7]\n" \
"v_mfma_f32_16x16x32_bf16 v[8:11], a[8:11], v[28:31], v[8:11]\n" \
"ds_read_b128 v[20:23], v124 offset:36864\n" \
"v_mfma_f32_16x16x32_bf16 v[12:15], a[12:15], v[28:31], v[12:15]\n" \
"s_waitcnt lgkmcnt(4)\n" \
"v_mfma_f32_16x16x32_bf16 v[0:3], a[16:19], v[32:35], v[0:3]\n" \
"ds_read_b128 v[24:27], v124 offset:5120\n" \
"v_mfma_f32_16x16x32_bf16 v[4:7], a[20:23], v[32:35], v[4:7]\n" \
"v_mfma_f32_16x16x32_bf16 v[8:11], a[16:19], v[36:39], v[8:11]\n" \
"ds_read_b128 v[28:31], v124 offset:37888\n" \
"v_mfma_f32_16x16x32_bf16 v[12:15], a[20:23], v[36:39], v[12:15]\n" \
"s_waitcnt lgkmcnt(4)\n" \
"v_mfma_f32_16x16x32_bf16 v[0:3], a[24:27], v[40:43], v[0:3]\n" \
"ds_read_b128 v[32:35], v124 offset:6144\n" \
"v_mfma_f32_16x16x32_bf16 v[4:7], a[28:31], v[40:43], v[4:7]\n" \
"v_mfma_f32_16x16x32_bf16 v[8:11], a[24:27], v[44:47], v[8:11]\n" \
"ds_read_b128 v[36:39], v124 offset:38912\n" \
"v_mfma_f32_16x16x32_bf16 v[12:15], a[28:31], v[44:47], v[12:15]\n" \
"s_waitcnt lgkmcnt(4)\n" \
"v_mfma_f32_16x16x32_bf16 v[0:3], a[32:35], v[16:19], v[0:3]\n" \
"ds_read_b128 v[40:43], v124 offset:7168\n" \
"v_mfma_f32_16x16x32_bf16 v[4:7], a[36:39], v[16:19], v[4:7]\n" \
"v_mfma_f32_16x16x32_bf16 v[8:11], a[32:35], v[20:23], v[8:11]\n" \
"ds_read_b128 v[44:47], v124 offset:39936\n" \
"v_mfma_f32_16x16x32_bf16 v[12:15], a[36:39], v[20:23], v[12:15]\n" \
"s_waitcnt lgkmcnt(4)\n" \
"v_mfma_f32_16x16x32_bf16 v[0:3], a[40:43], v[24:27], v[0:3]\n" \
"s_waitcnt vmcnt(8)\n" \
"s_barrier\n" \
"buffer_load_dword v126, v122, s[44:47], s52 offen sc1\n" \
"ds_read_b128 v[16:19], v124 offset:8192\n" \
"v_mfma_f32_16x16x32_bf16 v[4:7], a[44:47], v[24:27], v[4:7]\n" \
"v_mfma_f32_16x16x32_bf16 v[8:11], a[40:43], v[28:31], v[8:11]\n" \
"ds_read_b128 v[20:23], v124 offset:40960\n" \
"v_mfma_f32_16x16x32_bf16 v[12:15], a[44:47], v[28:31], v[12:15]\n" \
"s_waitcnt lgkmcnt(4)\n" \
"v_mfma_f32_16x16x32_bf16 v[0:3], a[48:51], v[32:35], v[0:3]\n" \
"ds_read_b128 v[24:27], v124 offset:9216\n" \
"v_mfma_f32_16x16x32_bf16 v[4:7], a[52:55], v[32:35], v[4:7]\n" \
"v_mfma_f32_16x16x32_bf16 v[8:11], a[48:51], v[36:39], v[8:11]\n" \
"ds_read_b128 v[28:31], v124 offset:41984\n" \
"v_mfma_f32_16x16x32_bf16 v[12:15], a[52:55], v[36:39], v[12:15]\n" \
"s_waitcnt lgkmcnt(4)\n" \
"v_mfma_f32_16x16x32_bf16 v[0:3], a[56:59], v[40:43], v[0:3]\n" \
"ds_read_b128 v[32:35], v124 offset:10240\n" \
"v_mfma_f32_16x16x32_bf16 v[4:7], a[60:63], v[40:43], v[4:7]\n" \
"v_mfma_f32_16x16x32_bf16 v[8:11], a[56:59], v[44:47], v[8:11]\n" \
"ds_read_b128 v[36:39], v124 offset:43008\n" \
"v_mfma_f32_16x16x32_bf16 v[12:15], a[60:63], v[44:47], v[12:15]\n" \
"s_waitcnt lgkmcnt(4)\n" \
"v_mfma_f32_16x16x32_bf16 v[0:3], a[64:67], v[16:19], v[0:3]\n" \
"ds_read_b128 v[40:43], v124 offset:11264\n" \
"v_mfma_f32_16x16x32_bf16 v[4:7], a[68:71], v[16:19], v[4:7]\n" \
"v_mfma_f32_16x16x32_bf16 v[8:11], a[64:67], v[20:23], v[8:11]\n" \
"ds_read_b128 v[44:47], v124 offset:44032\n" \
"v_mfma_f32_16x16x32_bf16 v[12:15], a[68:71], v[20:23], v[12:15]\n" \
"s_waitcnt lgkmcnt(4)\n" \
"v_mfma_f32_16x16x32_bf16 v[0:3], a[72:75], v[24:27], v[0:3]\n" \
"ds_read_b128 v[16:19], v124 offset:12288\n" \
"v_mfma_f32_16x16x32_bf16 v[4:7], a[76:79], v[24:27], v[4:7]\n" \
"v_mfma_f32_16x16x32_bf16 v[8:11], a[72:75], v[28:31], v[8:11]\n" \
"ds_read_b128 v[20:23], v124 offset:45056\n" \
"v_mfma_f32_16x16x32_bf16 v[12:15], a[76:79], v[28:31], v[12:15]\n" \
"s_waitcnt lgkmcnt(4)\n" \
"v_mfma_f32_16x16x32_bf16 v[0:3], a[80:83], v[32:35], v[0:3]\n" \
"ds_read_b128 v[24:27], v124 offset:13312\n" \
"v_mfma_f32_16x16x32_bf16 v[4:7], a[84:87], v[32:35], v[4:7]\n" \
"v_mfma_f32_16x16x32_bf16 v[8:11], a[80:83], v[36:39], v[8:11]\n" \
"ds_read_b128 v[28:31], v124 offset:46080\n" \
"v_mfma_f32_16x16x32_bf16 v[12:15], a[84:87], v[36:39], v[12:15]\n" \
"s_waitcnt lgkmcnt(4)\n" \
"v_mfma_f32_16x16x32_bf16 v[0:3], a[88:91], v[40:43], v[0:3]\n" \
"ds_read_b128 v[32:35], v124 offset:14336\n" \
"v_mfma_f32_16x16x32_bf16 v[4:7], a[92:95], v[40:43], v[4:7]\n" \
"v_mfma_f32_16x16x32_bf16 v[8:11], a[88:91], v[44:47], v[8:11]\n" \
"ds_read_b128 v[36:39], v124 offset:47104\n" \
"v_mfma_f32_16x16x32_bf16 v[12:15], a[92:95], v[44:47], v[12:15]\n" \
"s_waitcnt lgkmcnt(4)\n" \
"v_mfma_f32_16x16x32_bf16 v[0:3], a[96:99], v[16:19], v[0:3]\n" \
"ds_read_b128 v[40:43], v124 offset:15360\n" \
"v_mfma_f32_16x16x32_bf16 v[4:7], a[100:103], v[16:19], v[4:7]\n" \
"v_mfma_f32_16x16x32_bf16 v[8:11], a[96:99], v[20:23], v[8:11]\n" \
"ds_read_b128 v[44:47], v124 offset:48128\n" \
"v_mfma_f32_16x16x32_bf16 v[12:15], a[100:103], v[20:23], v[12:15]\n" \
"s_waitcnt lgkmcnt(4)\n" \
"v_mfma_f32_16x16x32_bf16 v[0:3], a[104:107], v[24:27], v[0:3]\n" \
"s_waitcnt vmcnt(5)\n" \
"s_barrier\n" \
"ds_read_b128 v[16:19], v124 offset:16384\n" \
"v_mfma_f32_16x16x32_bf16 v[4:7], a[108:111], v[24:27], v[4:7]\n" \
"v_mfma_f32_16x16x32_bf16 v[8:11], a[104:107], v[28:31], v[8:11]\n" \
"ds_read_b128 v[20:23], v124 offset:49152\n" \
"v_mfma_f32_16x16x32_bf16 v[12:15], a[108:111], v[28:31], v[12:15]\n" \
"s_waitcnt lgkmcnt(4)\n" \
"v_mfma_f32_16x16x32_bf16 v[0:3], a[112:115], v[32:35], v[0:3]\n" \
"ds_read_b128 v[24:27], v124 offset:17408\n" \
"v_mfma_f32_16x16x32_bf16 v[4:7], a[116:119], v[32:35], v[4:7]\n" \
"v_mfma_f32_16x16x32_bf16 v[8:11], a[112:115], v[36:39], v[8:11]\n" \
"ds_read_b128 v[28:31], v124 offset:50176\n" \
"v_mfma_f32_16x16x32_bf16 v[12:15], a[116:119], v[36:39], v[12:15]\n" \
"s_waitcnt lgkmcnt(4)\n" \
"v_mfma_f32_16x16x32_bf16 v[0:3], a[120:123], v[40:43], v[0:3]\n" \
"ds_read_b128 v[32:35], v124 offset:18432\n" \
"v_mfma_f32_16x16x32_bf16 v[4:7], a[124:127], v[40:43], v[4:7]\n" \
"v_mfma_f32_16x16x32_bf16 v[8:11], a[120:123], v[44:47], v[8:11]\n" \
"ds_read_b128 v[36:39], v124 offset:51200\n" \
"v_mfma_f32_16x16x32_bf16 v[12:15], a[124:127], v[44:47], v[12:15]\n" \
"s_waitcnt lgkmcnt(4)\n" \
"v_mfma_f32_16x16x32_bf16 v[0:3], a[128:131], v[16:19], v[0:3]\n" \
"ds_read_b128 v[40:43], v124 offset:19456\n" \
"v_mfma_f32_16x16x32_bf16 v[4:7], a[132:135], v[16:19], v[4:7]\n" \
"v_mfma_f32_16x16x32_bf16 v[8:11], a[128:131], v[20:23], v[8:11]\n" \
"ds_read_b128 v[44:47], v124 offset:52224\n" \
"v_mfma_f32_16x16x32_bf16 v[12:15], a[132:135], v[20:23], v[12:15]\n" \
"s_waitcnt lgkmcnt(4)\n" \
"v_mfma_f32_16x16x32_bf16 v[0:3], a[136:139], v[24:27], v[0:3]\n" \
"ds_read_b128 v[16:19], v124 offset:20480\n" \
"v_mfma_f32_16x16x32_bf16 v[4:7], a[140:143], v[24:27], v[4:7]\n" \
"v_mfma_f32_16x16x32_bf16 v[8:11], a[136:139], v[28:31], v[8:11]\n" \
"ds_read_b128 v[20:23], v124 offset:53248\n" \
"v_mfma_f32_16x16x32_bf16 v[12:15], a[140:143], v[28:31], v[12:15]\n" \
"s_waitcnt lgkmcnt(4)\n" \
"v_mfma_f32_16x16x32_bf16 v[0:3], a[144:147], v[32:35], v[0:3]\n" \
"ds_read_b128 v[24:27], v124 offset:21504\n" \
"v_mfma_f32_16x16x32_bf16 v[4:7], a[148:151], v[32:35], v[4:7]\n" \
"v_mfma_f32_16x16x32_bf16 v[8:11], a[144:147], v[36:39], v[8:11]\n" \
"ds_read_b128 v[28:31], v124 offset:54272\n" \
"v_mfma_f32_16x16x32_bf16 v[12:15], a[148:151], v[36:39], v[12:15]\n" \
"s_waitcnt lgkmcnt(4)\n" \
"v_mfma_f32_16x16x32_bf16 v[0:3], a[152:155], v[40:43], v[0:3]\n" \
"ds_read_b128 v[32:35], v124 offset:22528\n" \
"v_mfma_f32_16x16x32_bf16 v[4:7], a[156:159], v[40:43], v[4:7]\n" \
"v_mfma_f32_16x16x32_bf16 v[8:11], a[152:155], v[44:47], v[8:11]\n" \
"ds_read_b128 v[36:39], v124 offset:55296\n" \
"v_mfma_f32_16x16x32_bf16 v[12:15], a[156:159], v[44:47], v[12:15]\n" \
"s_waitcnt lgkmcnt(4)\n" \
"v_mfma_f32_16x16x32_bf16 v[0:3], a[160:163], v[16:19], v[0:3]\n" \
"ds_read_b128 v[40:43], v124 offset:23552\n" \
"v_mfma_f32_16x16x32_bf16 v[4:7], a[164:167], v[16:19], v[4:7]\n" \
"v_mfma_f32_16x16x32_bf16 v[8:11], a[160:163], v[20:23], v[8:11]\n" \
"ds_read_b128 v[44:47], v124 offset:56320\n" \
"v_mfma_f32_16x16x32_bf16 v[12:15], a[164:167], v[20:23], v[12:15]\n" \
"s_waitcnt lgkmcnt(4)\n" \
"v_mfma_f32_16x16x32_bf16 v[0:3], a[168:171], v[24:27], v[0:3]\n" \
"s_waitcnt vmcnt(0)\n" \
"v_cmp_ge_u32 vcc, s49, v126\n" \
"s_cmp_eq_u64 vcc, 0\n" \
"s_cselect_b32 s59, 1, 0\n" \
"s_cmp_lt_u32 s49, 64\n" \
"s_cselect_b32 s59, s59, 0\n" \
"v_mov_b32 v126, s59\n" \
"ds_write_b32 v123, v126\n" \
"s_waitcnt lgkmcnt(0)\n" \
"s_barrier\n" \
"v_mov_b32 v126, 133120\n" \
"ds_read_b32 v126, v126\n" \
"s_cmp_eq_u32 s69, 0\n" \
"s_cbranch_scc1 Lsg_nopub_%=\n" \
"v_mov_b32 v127, s48\n" \
"buffer_store_dword v127, off, s[44:47], s55 sc1\n" \
"s_mov_b32 s69, 0\n" \
"Lsg_nopub_%=:\n" \
"ds_read_b128 v[16:19], v124 offset:24576\n" \
"v_mfma_f32_16x16x32_bf16 v[4:7], a[172:175], v[24:27], v[4:7]\n" \
"v_mfma_f32_16x16x32_bf16 v[8:11], a[168:171], v[28:31], v[8:11]\n" \
"ds_read_b128 v[20:23], v124 offset:57344\n" \
"v_mfma_f32_16x16x32_bf16 v[12:15], a[172:175], v[28:31], v[12:15]\n" \
"v_mfma_f32_16x16x32_bf16 v[0:3], a[176:179], v[32:35], v[0:3]\n" \
"ds_read_b128 v[24:27], v124 offset:25600\n" \
"v_mfma_f32_16x16x32_bf16 v[4:7], a[180:183], v[32:35], v[4:7]\n" \
"v_mfma_f32_16x16x32_bf16 v[8:11], a[176:179], v[36:39], v[8:11]\n" \
"ds_read_b128 v[28:31], v124 offset:58368\n" \
"v_mfma_f32_16x16x32_bf16 v[12:15], a[180:183], v[36:39], v[12:15]\n" \
"v_mfma_f32_16x16x32_bf16 v[0:3], a[184:187], v[40:43], v[0:3]\n" \
"ds_read_b128 v[32:35], v124 offset:26624\n" \
"v_mfma_f32_16x16x32_bf16 v[4:7], a[188:191], v[40:43], v[4:7]\n" \
"v_mfma_f32_16x16x32_bf16 v[8:11], a[184:187], v[44:47], v[8:11]\n" \
"ds_read_b128 v[36:39], v124 offset:59392\n" \
"v_mfma_f32_16x16x32_bf16 v[12:15], a[188:191], v[44:47], v[12:15]\n" \
"s_waitcnt lgkmcnt(4)\n" \
"v_mfma_f32_16x16x32_bf16 v[0:3], a[192:195], v[16:19], v[0:3]\n" \
"ds_read_b128 v[40:43], v124 offset:27648\n" \
"v_mfma_f32_16x16x32_bf16 v[4:7], a[196:199], v[16:19], v[4:7]\n" \
"v_mfma_f32_16x16x32_bf16 v[8:11], a[192:195], v[20:23], v[8:11]\n" \
"ds_read_b128 v[44:47], v124 offset:60416\n" \
"v_mfma_f32_16x16x32_bf16 v[12:15], a[196:199], v[20:23], v[12:15]\n" \
"v_readfirstlane_b32 s68, v126\n" \
"s_cmp_eq_u32 s68, 0\n" \
"s_cbranch_scc1 Lsg_nopre_%=\n" \
"s_and_b32 s59, s49, 1\n" \
"s_lshl_b32 s59, s59, 16\n" \
"s_add_u32 s59, s59, s57\n" \
"s_add_u32 m0, s59, 0\n" \
"s_add_u32 s64, s50, 0\n" \
"buffer_load_dwordx4 v120, s[44:47], s64 offen offset:0 sc1 lds\n" \
"buffer_load_dwordx4 v120, s[44:47], s64 offen offset:1024 sc1 lds\n" \
"buffer_load_dwordx4 v120, s[44:47], s64 offen offset:2048 sc1 lds\n" \
"buffer_load_dwordx4 v120, s[44:47], s64 offen offset:3072 sc1 lds\n" \
"s_add_u32 m0, s59, 8192\n" \
"s_add_u32 s64, s50, 8192\n" \
"buffer_load_dwordx4 v120, s[44:47], s64 offen offset:0 sc1 lds\n" \
"buffer_load_dwordx4 v120, s[44:47], s64 offen offset:1024 sc1 lds\n" \
"buffer_load_dwordx4 v120, s[44:47], s64 offen offset:2048 sc1 lds\n" \
"buffer_load_dwordx4 v120, s[44:47], s64 offen offset:3072 sc1 lds\n" \
"s_add_u32 m0, s59, 16384\n" \
"s_add_u32 s64, s50, 16384\n" \
"buffer_load_dwordx4 v120, s[44:47], s64 offen offset:0 sc1 lds\n" \
"buffer_load_dwordx4 v120, s[44:47], s64 offen offset:1024 sc1 lds\n" \
"buffer_load_dwordx4 v120, s[44:47], s64 offen offset:2048 sc1 lds\n" \
"buffer_load_dwordx4 v120, s[44:47], s64 offen offset:3072 sc1 lds\n" \
"s_add_u32 m0, s59, 24576\n" \
"s_add_u32 s64, s50, 24576\n" \
"buffer_load_dwordx4 v120, s[44:47], s64 offen offset:0 sc1 lds\n" \
"buffer_load_dwordx4 v120, s[44:47], s64 offen offset:1024 sc1 lds\n" \
"buffer_load_dwordx4 v120, s[44:47], s64 offen offset:2048 sc1 lds\n" \
"buffer_load_dwordx4 v120, s[44:47], s64 offen offset:3072 sc1 lds\n" \
"s_add_u32 s50, s50, s51\n" \
"s_add_u32 s49, s49, 1\n" \
"Lsg_nopre_%=:\n" \
"s_waitcnt lgkmcnt(4)\n" \
"v_mfma_f32_16x16x32_bf16 v[0:3], a[200:203], v[24:27], v[0:3]\n" \
"ds_read_b128 v[16:19], v124 offset:28672\n" \
"v_mfma_f32_16x16x32_bf16 v[4:7], a[204:207], v[24:27], v[4:7]\n" \
"v_mfma_f32_16x16x32_bf16 v[8:11], a[200:203], v[28:31], v[8:11]\n" \
"ds_read_b128 v[20:23], v124 offset:61440\n" \
"v_mfma_f32_16x16x32_bf16 v[12:15], a[204:207], v[28:31], v[12:15]\n" \
"s_waitcnt lgkmcnt(4)\n" \
"v_mfma_f32_16x16x32_bf16 v[0:3], a[208:211], v[32:35], v[0:3]\n" \
"ds_read_b128 v[24:27], v124 offset:29696\n" \
"v_mfma_f32_16x16x32_bf16 v[4:7], a[212:215], v[32:35], v[4:7]\n" \
"v_mfma_f32_16x16x32_bf16 v[8:11], a[208:211], v[36:39], v[8:11]\n" \
"ds_read_b128 v[28:31], v124 offset:62464\n" \
"v_mfma_f32_16x16x32_bf16 v[12:15], a[212:215], v[36:39], v[12:15]\n" \
"s_waitcnt lgkmcnt(4)\n" \
"v_mfma_f32_16x16x32_bf16 v[0:3], a[216:219], v[40:43], v[0:3]\n" \
"ds_read_b128 v[32:35], v124 offset:30720\n" \
"v_mfma_f32_16x16x32_bf16 v[4:7], a[220:223], v[40:43], v[4:7]\n" \
"v_mfma_f32_16x16x32_bf16 v[8:11], a[216:219], v[44:47], v[8:11]\n" \
"ds_read_b128 v[36:39], v124 offset:63488\n" \
"v_mfma_f32_16x16x32_bf16 v[12:15], a[220:223], v[44:47], v[12:15]\n" \
"s_waitcnt lgkmcnt(4)\n" \
"v_mfma_f32_16x16x32_bf16 v[0:3], a[224:227], v[16:19], v[0:3]\n" \
"ds_read_b128 v[40:43], v124 offset:31744\n" \
"v_mfma_f32_16x16x32_bf16 v[4:7], a[228:231], v[16:19], v[4:7]\n" \
"v_mfma_f32_16x16x32_bf16 v[8:11], a[224:227], v[20:23], v[8:11]\n" \
"ds_read_b128 v[44:47], v124 offset:64512\n" \
"v_mfma_f32_16x16x32_bf16 v[12:15], a[228:231], v[20:23], v[12:15]\n" \
"s_waitcnt lgkmcnt(4)\n" \
"v_mfma_f32_16x16x32_bf16 v[0:3], a[232:235], v[24:27], v[0:3]\n" \
"v_mfma_f32_16x16x32_bf16 v[4:7], a[236:239], v[24:27], v[4:7]\n" \
"v_mfma_f32_16x16x32_bf16 v[8:11], a[232:235], v[28:31], v[8:11]\n" \
"v_mfma_f32_16x16x32_bf16 v[12:15], a[236:239], v[28:31], v[12:15]\n" \
"s_waitcnt lgkmcnt(2)\n" \
"v_mfma_f32_16x16x32_bf16 v[0:3], a[240:243], v[32:35], v[0:3]\n" \
"v_mfma_f32_16x16x32_bf16 v[4:7], a[244:247], v[32:35], v[4:7]\n" \
"v_mfma_f32_16x16x32_bf16 v[8:11], a[240:243], v[36:39], v[8:11]\n" \
"v_mfma_f32_16x16x32_bf16 v[12:15], a[244:247], v[36:39], v[12:15]\n" \
"s_waitcnt lgkmcnt(0)\n" \
"v_mfma_f32_16x16x32_bf16 v[0:3], a[248:251], v[40:43], v[0:3]\n" \
"v_mfma_f32_16x16x32_bf16 v[4:7], a[252:255], v[40:43], v[4:7]\n" \
"v_mfma_f32_16x16x32_bf16 v[8:11], a[248:251], v[44:47], v[8:11]\n" \
"v_mfma_f32_16x16x32_bf16 v[12:15], a[252:255], v[44:47], v[12:15]\n" \
"s_add_u32 s64, s60, 114688\n" \
"v_mul_f32 v48, s67, v0\n" \
"v_mul_f32 v49, s67, v1\n" \
"v_mul_f32 v50, s67, v2\n" \
"v_mul_f32 v51, s67, v3\n" \
"v_exp_f32 v48, v48\n" \
"v_exp_f32 v49, v49\n" \
"v_exp_f32 v50, v50\n" \
"v_exp_f32 v51, v51\n" \
"v_pk_mul_f32 v[0:1], v[0:1], v[4:5]\n" \
"v_pk_mul_f32 v[2:3], v[2:3], v[6:7]\n" \
"v_pk_add_f32 v[48:49], v[48:49], 1.0 op_sel_hi:[1,0]\n" \
"v_pk_add_f32 v[50:51], v[50:51], 1.0 op_sel_hi:[1,0]\n" \
"v_rcp_f32 v48, v48\n" \
"v_rcp_f32 v49, v49\n" \
"v_rcp_f32 v50, v50\n" \
"v_rcp_f32 v51, v51\n" \
"v_pk_mul_f32 v[0:1], v[0:1], v[48:49]\n" \
"v_pk_mul_f32 v[2:3], v[2:3], v[50:51]\n" \
"v_cvt_pk_bf16_f32 v52, v0, v1\n" \
"v_cvt_pk_bf16_f32 v53, v2, v3\n" \
"buffer_store_dwordx2 v[52:53], v121, s[44:47], s60 offen sc1\n" \
"v_mul_f32 v56, s67, v8\n" \
"v_mul_f32 v57, s67, v9\n" \
"v_mul_f32 v58, s67, v10\n" \
"v_mul_f32 v59, s67, v11\n" \
"v_exp_f32 v56, v56\n" \
"v_exp_f32 v57, v57\n" \
"v_exp_f32 v58, v58\n" \
"v_exp_f32 v59, v59\n" \
"v_pk_mul_f32 v[8:9], v[8:9], v[12:13]\n" \
"v_pk_mul_f32 v[10:11], v[10:11], v[14:15]\n" \
"v_pk_add_f32 v[56:57], v[56:57], 1.0 op_sel_hi:[1,0]\n" \
"v_pk_add_f32 v[58:59], v[58:59], 1.0 op_sel_hi:[1,0]\n" \
"v_rcp_f32 v56, v56\n" \
"v_rcp_f32 v57, v57\n" \
"v_rcp_f32 v58, v58\n" \
"v_rcp_f32 v59, v59\n" \
"v_pk_mul_f32 v[8:9], v[8:9], v[56:57]\n" \
"v_pk_mul_f32 v[10:11], v[10:11], v[58:59]\n" \
"v_cvt_pk_bf16_f32 v60, v8, v9\n" \
"v_cvt_pk_bf16_f32 v61, v10, v11\n" \
"buffer_store_dwordx2 v[60:61], v121, s[44:47], s64 offen sc1\n" \
"s_add_u32 s60, s60, s61\n" \
"s_add_u32 s48, s48, 1\n" \
"s_mov_b32 s69, 1\n" \
"s_cmp_lg_u32 s68, 0\n" \
"s_cbranch_scc1 Lsg_seg_%=\n" \
"s_waitcnt vmcnt(0)\n" \
"s_barrier\n" \
"v_mov_b32 v127, s48\n" \
"buffer_store_dword v127, off, s[44:47], s55 sc1\n" \
"s_mov_b32 s69, 0\n" \
"s_cmp_lt_u32 s48, 64\n" \
"s_cbranch_scc1 Lsg_wait_%=\n" \
"Lsg_end_%=:\n" \
"s_waitcnt vmcnt(0) lgkmcnt(0)\n"
#define ASM_SD \
"s_add_u32 m0, s56, 0\n" \
"s_nop 0\n" \
"buffer_load_dwordx4 v0, s[36:39], s53 offen lds\n" \
"buffer_load_dwordx4 v1, s[40:43], s53 offen offset:1024 lds\n" \
"s_add_u32 s53, s53, s54\n" \
"s_add_u32 m0, s56, 8192\n" \
"s_nop 0\n" \
"buffer_load_dwordx4 v0, s[36:39], s53 offen lds\n" \
"buffer_load_dwordx4 v1, s[40:43], s53 offen offset:1024 lds\n" \
"s_add_u32 s53, s53, s54\n" \
"s_add_u32 m0, s56, 16384\n" \
"s_nop 0\n" \
"buffer_load_dwordx4 v0, s[36:39], s53 offen lds\n" \
"buffer_load_dwordx4 v1, s[40:43], s53 offen offset:1024 lds\n" \
"s_add_u32 s53, s53, s54\n" \
"s_add_u32 m0, s56, 24576\n" \
"s_nop 0\n" \
"buffer_load_dwordx4 v0, s[36:39], s53 offen lds\n" \
"buffer_load_dwordx4 v1, s[40:43], s53 offen offset:1024 lds\n" \
"s_add_u32 s53, s53, s54\n" \
"s_add_u32 m0, s56, 32768\n" \
"s_nop 0\n" \
"buffer_load_dwordx4 v0, s[36:39], s53 offen lds\n" \
"buffer_load_dwordx4 v1, s[40:43], s53 offen offset:1024 lds\n" \
"s_add_u32 s53, s53, s54\n" \
"s_add_u32 m0, s56, 40960\n" \
"s_nop 0\n" \
"buffer_load_dwordx4 v0, s[36:39], s53 offen lds\n" \
"buffer_load_dwordx4 v1, s[40:43], s53 offen offset:1024 lds\n" \
"s_add_u32 s53, s53, s54\n" \
"s_add_u32 m0, s56, 49152\n" \
"s_nop 0\n" \
"buffer_load_dwordx4 v0, s[36:39], s53 offen lds\n" \
"buffer_load_dwordx4 v1, s[40:43], s53 offen offset:1024 lds\n" \
"s_add_u32 s53, s53, s54\n" \
"s_add_u32 m0, s56, 57344\n" \
"s_nop 0\n" \
"buffer_load_dwordx4 v0, s[36:39], s53 offen lds\n" \
"buffer_load_dwordx4 v1, s[40:43], s53 offen offset:1024 lds\n" \
"s_add_u32 s53, s53, s54\n" \
"s_add_u32 m0, s56, 65536\n" \
"s_nop 0\n" \
"buffer_load_dwordx4 v0, s[36:39], s53 offen lds\n" \
"buffer_load_dwordx4 v1, s[40:43], s53 offen offset:1024 lds\n" \
"s_add_u32 s53, s53, s54\n" \
"s_add_u32 m0, s56, 73728\n" \
"s_nop 0\n" \
"buffer_load_dwordx4 v0, s[36:39], s53 offen lds\n" \
"buffer_load_dwordx4 v1, s[40:43], s53 offen offset:1024 lds\n" \
"s_add_u32 s53, s53, s54\n" \
"s_add_u32 m0, s56, 81920\n" \
"s_nop 0\n" \
"buffer_load_dwordx4 v0, s[36:39], s53 offen lds\n" \
"buffer_load_dwordx4 v1, s[40:43], s53 offen offset:1024 lds\n" \
"s_add_u32 s53, s53, s54\n" \
"s_add_u32 m0, s56, 90112\n" \
"s_nop 0\n" \
"buffer_load_dwordx4 v0, s[36:39], s53 offen lds\n" \
"buffer_load_dwordx4 v1, s[40:43], s53 offen offset:1024 lds\n" \
"s_add_u32 s53, s53, s54\n" \
"s_add_u32 m0, s56, 98304\n" \
"s_nop 0\n" \
"buffer_load_dwordx4 v0, s[36:39], s53 offen lds\n" \
"buffer_load_dwordx4 v1, s[40:43], s53 offen offset:1024 lds\n" \
"s_add_u32 s53, s53, s54\n" \
"s_add_u32 m0, s56, 106496\n" \
"s_nop 0\n" \
"buffer_load_dwordx4 v0, s[36:39], s53 offen lds\n" \
"buffer_load_dwordx4 v1, s[40:43], s53 offen offset:1024 lds\n" \
"s_add_u32 s53, s53, s54\n" \
"s_add_u32 m0, s56, 114688\n" \
"s_nop 0\n" \
"buffer_load_dwordx4 v0, s[36:39], s53 offen lds\n" \
"buffer_load_dwordx4 v1, s[40:43], s53 offen offset:1024 lds\n" \
"s_add_u32 s53, s53, s54\n" \
"s_add_u32 m0, s56, 122880\n" \
"s_nop 0\n" \
"buffer_load_dwordx4 v0, s[36:39], s53 offen lds\n" \
"buffer_load_dwordx4 v1, s[40:43], s53 offen offset:1024 lds\n" \
"s_add_u32 s53, s53, s54\n" \
"s_waitcnt vmcnt(16)\n" \
"s_barrier\n" \
"ds_read_b64_tr_b16 a[0:1], v4 offset:0\n" \
"ds_read_b64_tr_b16 a[2:3], v4 offset:4096\n" \
"ds_read_b64_tr_b16 a[4:5], v5 offset:0\n" \
"ds_read_b64_tr_b16 a[6:7], v5 offset:4096\n" \
"ds_read_b64_tr_b16 a[8:9], v4 offset:8192\n" \
"ds_read_b64_tr_b16 a[10:11], v4 offset:12288\n" \
"ds_read_b64_tr_b16 a[12:13], v5 offset:8192\n" \
"ds_read_b64_tr_b16 a[14:15], v5 offset:12288\n" \
"ds_read_b64_tr_b16 a[16:17], v4 offset:16384\n" \
"ds_read_b64_tr_b16 a[18:19], v4 offset:20480\n" \
"ds_read_b64_tr_b16 a[20:21], v5 offset:16384\n" \
"ds_read_b64_tr_b16 a[22:23], v5 offset:20480\n" \
"ds_read_b64_tr_b16 a[24:25], v4 offset:24576\n" \
"ds_read_b64_tr_b16 a[26:27], v4 offset:28672\n" \
"ds_read_b64_tr_b16 a[28:29], v5 offset:24576\n" \
"ds_read_b64_tr_b16 a[30:31], v5 offset:28672\n" \
"ds_read_b64_tr_b16 a[32:33], v4 offset:32768\n" \
"ds_read_b64_tr_b16 a[34:35], v4 offset:36864\n" \
"ds_read_b64_tr_b16 a[36:37], v5 offset:32768\n" \
"ds_read_b64_tr_b16 a[38:39], v5 offset:36864\n" \
"ds_read_b64_tr_b16 a[40:41], v4 offset:40960\n" \
"ds_read_b64_tr_b16 a[42:43], v4 offset:45056\n" \
"ds_read_b64_tr_b16 a[44:45], v5 offset:40960\n" \
"ds_read_b64_tr_b16 a[46:47], v5 offset:45056\n" \
"ds_read_b64_tr_b16 a[48:49], v4 offset:49152\n" \
"ds_read_b64_tr_b16 a[50:51], v4 offset:53248\n" \
"ds_read_b64_tr_b16 a[52:53], v5 offset:49152\n" \
"ds_read_b64_tr_b16 a[54:55], v5 offset:53248\n" \
"ds_read_b64_tr_b16 a[56:57], v4 offset:57344\n" \
"ds_read_b64_tr_b16 a[58:59], v4 offset:61440\n" \
"ds_read_b64_tr_b16 a[60:61], v5 offset:57344\n" \
"ds_read_b64_tr_b16 a[62:63], v5 offset:61440\n" \
"s_waitcnt lgkmcnt(0)\n" \
"s_barrier\n" \
"s_add_u32 m0, s56, 0\n" \
"s_nop 0\n" \
"buffer_load_dwordx4 v0, s[36:39], s53 offen lds\n" \
"buffer_load_dwordx4 v1, s[40:43], s53 offen offset:1024 lds\n" \
"s_add_u32 s53, s53, s54\n" \
"s_add_u32 m0, s56, 8192\n" \
"s_nop 0\n" \
"buffer_load_dwordx4 v0, s[36:39], s53 offen lds\n" \
"buffer_load_dwordx4 v1, s[40:43], s53 offen offset:1024 lds\n" \
"s_add_u32 s53, s53, s54\n" \
"s_add_u32 m0, s56, 16384\n" \
"s_nop 0\n" \
"buffer_load_dwordx4 v0, s[36:39], s53 offen lds\n" \
"buffer_load_dwordx4 v1, s[40:43], s53 offen offset:1024 lds\n" \
"s_add_u32 s53, s53, s54\n" \
"s_add_u32 m0, s56, 24576\n" \
"s_nop 0\n" \
"buffer_load_dwordx4 v0, s[36:39], s53 offen lds\n" \
"buffer_load_dwordx4 v1, s[40:43], s53 offen offset:1024 lds\n" \
"s_add_u32 s53, s53, s54\n" \
"s_add_u32 m0, s56, 32768\n" \
"s_nop 0\n" \
"buffer_load_dwordx4 v0, s[36:39], s53 offen lds\n" \
"buffer_load_dwordx4 v1, s[40:43], s53 offen offset:1024 lds\n" \
"s_add_u32 s53, s53, s54\n" \
"s_add_u32 m0, s56, 40960\n" \
"s_nop 0\n" \
"buffer_load_dwordx4 v0, s[36:39], s53 offen lds\n" \
"buffer_load_dwordx4 v1, s[40:43], s53 offen offset:1024 lds\n" \
"s_add_u32 s53, s53, s54\n" \
"s_add_u32 m0, s56, 49152\n" \
"s_nop 0\n" \
"buffer_load_dwordx4 v0, s[36:39], s53 offen lds\n" \
"buffer_load_dwordx4 v1, s[40:43], s53 offen offset:1024 lds\n" \
"s_add_u32 s53, s53, s54\n" \
"s_add_u32 m0, s56, 57344\n" \
"s_nop 0\n" \
"buffer_load_dwordx4 v0, s[36:39], s53 offen lds\n" \
"buffer_load_dwordx4 v1, s[40:43], s53 offen offset:1024 lds\n" \
"s_add_u32 s53, s53, s54\n" \
"s_waitcnt vmcnt(16)\n" \
"s_barrier\n" \
"ds_read_b64_tr_b16 a[64:65], v6 offset:0\n" \
"ds_read_b64_tr_b16 a[66:67], v6 offset:4096\n" \
"ds_read_b64_tr_b16 a[68:69], v7 offset:0\n" \
"ds_read_b64_tr_b16 a[70:71], v7 offset:4096\n" \
"ds_read_b64_tr_b16 a[72:73], v6 offset:8192\n" \
"ds_read_b64_tr_b16 a[74:75], v6 offset:12288\n" \
"ds_read_b64_tr_b16 a[76:77], v7 offset:8192\n" \
"ds_read_b64_tr_b16 a[78:79], v7 offset:12288\n" \
"ds_read_b64_tr_b16 a[80:81], v6 offset:16384\n" \
"ds_read_b64_tr_b16 a[82:83], v6 offset:20480\n" \
"ds_read_b64_tr_b16 a[84:85], v7 offset:16384\n" \
"ds_read_b64_tr_b16 a[86:87], v7 offset:20480\n" \
"ds_read_b64_tr_b16 a[88:89], v6 offset:24576\n" \
"ds_read_b64_tr_b16 a[90:91], v6 offset:28672\n" \
"ds_read_b64_tr_b16 a[92:93], v7 offset:24576\n" \
"ds_read_b64_tr_b16 a[94:95], v7 offset:28672\n" \
"ds_read_b64_tr_b16 a[96:97], v6 offset:32768\n" \
"ds_read_b64_tr_b16 a[98:99], v6 offset:36864\n" \
"ds_read_b64_tr_b16 a[100:101], v7 offset:32768\n" \
"ds_read_b64_tr_b16 a[102:103], v7 offset:36864\n" \
"ds_read_b64_tr_b16 a[104:105], v6 offset:40960\n" \
"ds_read_b64_tr_b16 a[106:107], v6 offset:45056\n" \
"ds_read_b64_tr_b16 a[108:109], v7 offset:40960\n" \
"ds_read_b64_tr_b16 a[110:111], v7 offset:45056\n" \
"ds_read_b64_tr_b16 a[112:113], v6 offset:49152\n" \
"ds_read_b64_tr_b16 a[114:115], v6 offset:53248\n" \
"ds_read_b64_tr_b16 a[116:117], v7 offset:49152\n" \
"ds_read_b64_tr_b16 a[118:119], v7 offset:53248\n" \
"ds_read_b64_tr_b16 a[120:121], v6 offset:57344\n" \
"ds_read_b64_tr_b16 a[122:123], v6 offset:61440\n" \
"ds_read_b64_tr_b16 a[124:125], v7 offset:57344\n" \
"ds_read_b64_tr_b16 a[126:127], v7 offset:61440\n" \
"s_waitcnt lgkmcnt(0)\n" \
"s_barrier\n" \
"s_add_u32 m0, s56, 65536\n" \
"s_nop 0\n" \
"buffer_load_dwordx4 v0, s[36:39], s53 offen lds\n" \
"buffer_load_dwordx4 v1, s[40:43], s53 offen offset:1024 lds\n" \
"s_add_u32 s53, s53, s54\n" \
"s_add_u32 m0, s56, 73728\n" \
"s_nop 0\n" \
"buffer_load_dwordx4 v0, s[36:39], s53 offen lds\n" \
"buffer_load_dwordx4 v1, s[40:43], s53 offen offset:1024 lds\n" \
"s_add_u32 s53, s53, s54\n" \
"s_add_u32 m0, s56, 81920\n" \
"s_nop 0\n" \
"buffer_load_dwordx4 v0, s[36:39], s53 offen lds\n" \
"buffer_load_dwordx4 v1, s[40:43], s53 offen offset:1024 lds\n" \
"s_add_u32 s53, s53, s54\n" \
"s_add_u32 m0, s56, 90112\n" \
"s_nop 0\n" \
"buffer_load_dwordx4 v0, s[36:39], s53 offen lds\n" \
"buffer_load_dwordx4 v1, s[40:43], s53 offen offset:1024 lds\n" \
"s_add_u32 s53, s53, s54\n" \
"s_waitcnt vmcnt(8)\n" \
"s_barrier\n" \
"ds_read_b64_tr_b16 a[128:129], v4 offset:0\n" \
"ds_read_b64_tr_b16 a[130:131], v4 offset:4096\n" \
"ds_read_b64_tr_b16 a[132:133], v5 offset:0\n" \
"ds_read_b64_tr_b16 a[134:135], v5 offset:4096\n" \
"ds_read_b64_tr_b16 a[136:137], v4 offset:8192\n" \
"ds_read_b64_tr_b16 a[138:139], v4 offset:12288\n" \
"ds_read_b64_tr_b16 a[140:141], v5 offset:8192\n" \
"ds_read_b64_tr_b16 a[142:143], v5 offset:12288\n" \
"ds_read_b64_tr_b16 a[144:145], v4 offset:16384\n" \
"ds_read_b64_tr_b16 a[146:147], v4 offset:20480\n" \
"ds_read_b64_tr_b16 a[148:149], v5 offset:16384\n" \
"ds_read_b64_tr_b16 a[150:151], v5 offset:20480\n" \
"ds_read_b64_tr_b16 a[152:153], v4 offset:24576\n" \
"ds_read_b64_tr_b16 a[154:155], v4 offset:28672\n" \
"ds_read_b64_tr_b16 a[156:157], v5 offset:24576\n" \
"ds_read_b64_tr_b16 a[158:159], v5 offset:28672\n" \
"ds_read_b64_tr_b16 a[160:161], v4 offset:32768\n" \
"ds_read_b64_tr_b16 a[162:163], v4 offset:36864\n" \
"ds_read_b64_tr_b16 a[164:165], v5 offset:32768\n" \
"ds_read_b64_tr_b16 a[166:167], v5 offset:36864\n" \
"ds_read_b64_tr_b16 a[168:169], v4 offset:40960\n" \
"ds_read_b64_tr_b16 a[170:171], v4 offset:45056\n" \
"ds_read_b64_tr_b16 a[172:173], v5 offset:40960\n" \
"ds_read_b64_tr_b16 a[174:175], v5 offset:45056\n" \
"ds_read_b64_tr_b16 a[176:177], v4 offset:49152\n" \
"ds_read_b64_tr_b16 a[178:179], v4 offset:53248\n" \
"ds_read_b64_tr_b16 a[180:181], v5 offset:49152\n" \
"ds_read_b64_tr_b16 a[182:183], v5 offset:53248\n" \
"ds_read_b64_tr_b16 a[184:185], v4 offset:57344\n" \
"ds_read_b64_tr_b16 a[186:187], v4 offset:61440\n" \
"ds_read_b64_tr_b16 a[188:189], v5 offset:57344\n" \
"ds_read_b64_tr_b16 a[190:191], v5 offset:61440\n" \
"s_waitcnt lgkmcnt(0)\n" \
"s_waitcnt vmcnt(0)\n" \
"s_barrier\n" \
"ds_read_b64_tr_b16 a[192:193], v6 offset:0\n" \
"ds_read_b64_tr_b16 a[194:195], v6 offset:4096\n" \
"ds_read_b64_tr_b16 a[196:197], v7 offset:0\n" \
"ds_read_b64_tr_b16 a[198:199], v7 offset:4096\n" \
"ds_read_b64_tr_b16 a[200:201], v6 offset:8192\n" \
"ds_read_b64_tr_b16 a[202:203], v6 offset:12288\n" \
"ds_read_b64_tr_b16 a[204:205], v7 offset:8192\n" \
"ds_read_b64_tr_b16 a[206:207], v7 offset:12288\n" \
"ds_read_b64_tr_b16 a[208:209], v6 offset:16384\n" \
"ds_read_b64_tr_b16 a[210:211], v6 offset:20480\n" \
"ds_read_b64_tr_b16 a[212:213], v7 offset:16384\n" \
"ds_read_b64_tr_b16 a[214:215], v7 offset:20480\n" \
"ds_read_b64_tr_b16 a[216:217], v6 offset:24576\n" \
"ds_read_b64_tr_b16 a[218:219], v6 offset:28672\n" \
"ds_read_b64_tr_b16 a[220:221], v7 offset:24576\n" \
"ds_read_b64_tr_b16 a[222:223], v7 offset:28672\n" \
"s_waitcnt lgkmcnt(0)\n" \
"s_barrier\n" \
"s_mov_b32 s65, 0\n" \
"s_mov_b32 s69, 0\n" \
"Lsd_wait_%=:\n" \
"s_mov_b32 s58, 400000\n" \
"s_and_b32 s59, s49, s71\n" \
"s_mul_i32 s59, s59, s72\n" \
"s_add_u32 s52, s73, s59\n" \
"Lsd_poll_%=:\n" \
"buffer_load_dword v126, v122, s[44:47], s52 offen sc1\n" \
"s_waitcnt vmcnt(0)\n" \
"v_cmp_ge_u32 vcc, s49, v126\n" \
"s_cbranch_vccz Lsd_go_%=\n" \
"s_sleep 1\n" \
"s_sub_u32 s58, s58, 1\n" \
"s_cmp_lg_u32 s58, 0\n" \
"s_cbranch_scc1 Lsd_poll_%=\n" \
"s_mov_b32 s65, 1\n" \
"s_branch Lsd_end_%=\n" \
"Lsd_go_%=:\n" \
"s_and_b32 s59, s49, 1\n" \
"s_lshl_b32 s59, s59, 16\n" \
"s_add_u32 s59, s59, s57\n" \
"s_add_u32 m0, s59, 0\n" \
"s_add_u32 s64, s50, 0\n" \
"buffer_load_dwordx4 v120, s[44:47], s64 offen offset:0 sc1 lds\n" \
"buffer_load_dwordx4 v120, s[44:47], s64 offen offset:1024 sc1 lds\n" \
"buffer_load_dwordx4 v120, s[44:47], s64 offen offset:2048 sc1 lds\n" \
"s_add_u32 m0, m0, s70\n" \
"s_add_u32 s64, s64, s70\n" \
"buffer_load_dwordx4 v120, s[44:47], s64 offen offset:2048 sc1 lds\n" \
"s_add_u32 m0, s59, 7168\n" \
"s_add_u32 s64, s50, 7168\n" \
"buffer_load_dwordx4 v120, s[44:47], s64 offen offset:0 sc1 lds\n" \
"buffer_load_dwordx4 v120, s[44:47], s64 offen offset:1024 sc1 lds\n" \
"buffer_load_dwordx4 v120, s[44:47], s64 offen offset:2048 sc1 lds\n" \
"s_add_u32 m0, m0, s70\n" \
"s_add_u32 s64, s64, s70\n" \
"buffer_load_dwordx4 v120, s[44:47], s64 offen offset:2048 sc1 lds\n" \
"s_add_u32 m0, s59, 14336\n" \
"s_add_u32 s64, s50, 14336\n" \
"buffer_load_dwordx4 v120, s[44:47], s64 offen offset:0 sc1 lds\n" \
"buffer_load_dwordx4 v120, s[44:47], s64 offen offset:1024 sc1 lds\n" \
"buffer_load_dwordx4 v120, s[44:47], s64 offen offset:2048 sc1 lds\n" \
"s_add_u32 m0, m0, s70\n" \
"s_add_u32 s64, s64, s70\n" \
"buffer_load_dwordx4 v120, s[44:47], s64 offen offset:2048 sc1 lds\n" \
"s_add_u32 m0, s59, 21504\n" \
"s_add_u32 s64, s50, 21504\n" \
"buffer_load_dwordx4 v120, s[44:47], s64 offen offset:0 sc1 lds\n" \
"buffer_load_dwordx4 v120, s[44:47], s64 offen offset:1024 sc1 lds\n" \
"buffer_load_dwordx4 v120, s[44:47], s64 offen offset:2048 sc1 lds\n" \
"s_add_u32 m0, m0, s70\n" \
"s_add_u32 s64, s64, s70\n" \
"buffer_load_dwordx4 v120, s[44:47], s64 offen offset:2048 sc1 lds\n" \
"s_add_u32 s50, s50, s51\n" \
"s_add_u32 s49, s49, 1\n" \
"Lsd_seg_%=:\n" \
"s_and_b32 s59, s48, 1\n" \
"s_lshl_b32 s59, s59, 16\n" \
"v_add_u32 v124, s59, v120\n" \
"s_and_b32 s59, s49, s71\n" \
"s_mul_i32 s59, s59, s72\n" \
"s_add_u32 s52, s73, s59\n" \
"s_waitcnt vmcnt(12)\n" \
"s_barrier\n" \
"ds_read_b128 v[16:19], v124 offset:0\n" \
"ds_read_b128 v[20:23], v124 offset:32768\n" \
"ds_read_b128 v[24:27], v124 offset:1024\n" \
"ds_read_b128 v[28:31], v124 offset:33792\n" \
"ds_read_b128 v[32:35], v124 offset:2048\n" \
"ds_read_b128 v[36:39], v124 offset:34816\n" \
"s_waitcnt lgkmcnt(4)\n" \
"v_mfma_f32_16x16x32_bf16 v[0:3], a[0:3], v[16:19], 0\n" \
"ds_read_b128 v[40:43], v124 offset:3072\n" \
"v_mfma_f32_16x16x32_bf16 v[4:7], a[4:7], v[16:19], 0\n" \
"v_mfma_f32_16x16x32_bf16 v[8:11], a[0:3], v[20:23], 0\n" \
"ds_read_b128 v[44:47], v124 offset:35840\n" \
"v_mfma_f32_16x16x32_bf16 v[12:15], a[4:7], v[20:23], 0\n" \
"s_waitcnt lgkmcnt(4)\n" \
"v_mfma_f32_16x16x32_bf16 v[0:3], a[8:11], v[24:27], v[0:3]\n" \
"ds_read_b128 v[16:19], v124 offset:4096\n" \
"v_mfma_f32_16x16x32_bf16 v[4:7], a[12:15], v[24:27], v[4:7]\n" \
"v_mfma_f32_16x16x32_bf16 v[8:11], a[8:11], v[28:31], v[8:11]\n" \
"ds_read_b128 v[20:23], v124 offset:36864\n" \
"v_mfma_f32_16x16x32_bf16 v[12:15], a[12:15], v[28:31], v[12:15]\n" \
"s_waitcnt lgkmcnt(4)\n" \
"v_mfma_f32_16x16x32_bf16 v[0:3], a[16:19], v[32:35], v[0:3]\n" \
"ds_read_b128 v[24:27], v124 offset:5120\n" \
"v_mfma_f32_16x16x32_bf16 v[4:7], a[20:23], v[32:35], v[4:7]\n" \
"v_mfma_f32_16x16x32_bf16 v[8:11], a[16:19], v[36:39], v[8:11]\n" \
"ds_read_b128 v[28:31], v124 offset:37888\n" \
"v_mfma_f32_16x16x32_bf16 v[12:15], a[20:23], v[36:39], v[12:15]\n" \
"s_waitcnt lgkmcnt(4)\n" \
"v_mfma_f32_16x16x32_bf16 v[0:3], a[24:27], v[40:43], v[0:3]\n" \
"ds_read_b128 v[32:35], v124 offset:6144\n" \
"v_mfma_f32_16x16x32_bf16 v[4:7], a[28:31], v[40:43], v[4:7]\n" \
"v_mfma_f32_16x16x32_bf16 v[8:11], a[24:27], v[44:47], v[8:11]\n" \
"ds_read_b128 v[36:39], v124 offset:38912\n" \
"v_mfma_f32_16x16x32_bf16 v[12:15], a[28:31], v[44:47], v[12:15]\n" \
"s_waitcnt lgkmcnt(4)\n" \
"v_mfma_f32_16x16x32_bf16 v[0:3], a[32:35], v[16:19], v[0:3]\n" \
"s_waitcnt vmcnt(8)\n" \
"s_barrier\n" \
"buffer_load_dword v126, v122, s[44:47], s52 offen sc1\n" \
"ds_read_b128 v[40:43], v124 offset:7168\n" \
"v_mfma_f32_16x16x32_bf16 v[4:7], a[36:39], v[16:19], v[4:7]\n" \
"v_mfma_f32_16x16x32_bf16 v[8:11], a[32:35], v[20:23], v[8:11]\n" \
"ds_read_b128 v[44:47], v124 offset:39936\n" \
"v_mfma_f32_16x16x32_bf16 v[12:15], a[36:39], v[20:23], v[12:15]\n" \
"s_waitcnt lgkmcnt(4)\n" \
"v_mfma_f32_16x16x32_bf16 v[0:3], a[40:43], v[24:27], v[0:3]\n" \
"ds_read_b128 v[16:19], v124 offset:8192\n" \
"v_mfma_f32_16x16x32_bf16 v[4:7], a[44:47], v[24:27], v[4:7]\n" \
"v_mfma_f32_16x16x32_bf16 v[8:11], a[40:43], v[28:31], v[8:11]\n" \
"ds_read_b128 v[20:23], v124 offset:40960\n" \
"v_mfma_f32_16x16x32_bf16 v[12:15], a[44:47], v[28:31], v[12:15]\n" \
"s_waitcnt lgkmcnt(4)\n" \
"v_mfma_f32_16x16x32_bf16 v[0:3], a[48:51], v[32:35], v[0:3]\n" \
"ds_read_b128 v[24:27], v124 offset:9216\n" \
"v_mfma_f32_16x16x32_bf16 v[4:7], a[52:55], v[32:35], v[4:7]\n" \
"v_mfma_f32_16x16x32_bf16 v[8:11], a[48:51], v[36:39], v[8:11]\n" \
"ds_read_b128 v[28:31], v124 offset:41984\n" \
"v_mfma_f32_16x16x32_bf16 v[12:15], a[52:55], v[36:39], v[12:15]\n" \
"s_waitcnt lgkmcnt(4)\n" \
"v_mfma_f32_16x16x32_bf16 v[0:3], a[56:59], v[40:43], v[0:3]\n" \
"ds_read_b128 v[32:35], v124 offset:10240\n" \
"v_mfma_f32_16x16x32_bf16 v[4:7], a[60:63], v[40:43], v[4:7]\n" \
"v_mfma_f32_16x16x32_bf16 v[8:11], a[56:59], v[44:47], v[8:11]\n" \
"ds_read_b128 v[36:39], v124 offset:43008\n" \
"v_mfma_f32_16x16x32_bf16 v[12:15], a[60:63], v[44:47], v[12:15]\n" \
"s_waitcnt lgkmcnt(4)\n" \
"v_mfma_f32_16x16x32_bf16 v[0:3], a[64:67], v[16:19], v[0:3]\n" \
"ds_read_b128 v[40:43], v124 offset:11264\n" \
"v_mfma_f32_16x16x32_bf16 v[4:7], a[68:71], v[16:19], v[4:7]\n" \
"v_mfma_f32_16x16x32_bf16 v[8:11], a[64:67], v[20:23], v[8:11]\n" \
"ds_read_b128 v[44:47], v124 offset:44032\n" \
"v_mfma_f32_16x16x32_bf16 v[12:15], a[68:71], v[20:23], v[12:15]\n" \
"s_waitcnt lgkmcnt(4)\n" \
"v_mfma_f32_16x16x32_bf16 v[0:3], a[72:75], v[24:27], v[0:3]\n" \
"ds_read_b128 v[16:19], v124 offset:12288\n" \
"v_mfma_f32_16x16x32_bf16 v[4:7], a[76:79], v[24:27], v[4:7]\n" \
"v_mfma_f32_16x16x32_bf16 v[8:11], a[72:75], v[28:31], v[8:11]\n" \
"ds_read_b128 v[20:23], v124 offset:45056\n" \
"v_mfma_f32_16x16x32_bf16 v[12:15], a[76:79], v[28:31], v[12:15]\n" \
"s_waitcnt lgkmcnt(4)\n" \
"v_mfma_f32_16x16x32_bf16 v[0:3], a[80:83], v[32:35], v[0:3]\n" \
"ds_read_b128 v[24:27], v124 offset:13312\n" \
"v_mfma_f32_16x16x32_bf16 v[4:7], a[84:87], v[32:35], v[4:7]\n" \
"v_mfma_f32_16x16x32_bf16 v[8:11], a[80:83], v[36:39], v[8:11]\n" \
"ds_read_b128 v[28:31], v124 offset:46080\n" \
"v_mfma_f32_16x16x32_bf16 v[12:15], a[84:87], v[36:39], v[12:15]\n" \
"s_waitcnt lgkmcnt(4)\n" \
"v_mfma_f32_16x16x32_bf16 v[0:3], a[88:91], v[40:43], v[0:3]\n" \
"s_waitcnt vmcnt(5)\n" \
"s_barrier\n" \
"ds_read_b128 v[32:35], v124 offset:14336\n" \
"v_mfma_f32_16x16x32_bf16 v[4:7], a[92:95], v[40:43], v[4:7]\n" \
"v_mfma_f32_16x16x32_bf16 v[8:11], a[88:91], v[44:47], v[8:11]\n" \
"ds_read_b128 v[36:39], v124 offset:47104\n" \
"v_mfma_f32_16x16x32_bf16 v[12:15], a[92:95], v[44:47], v[12:15]\n" \
"s_waitcnt lgkmcnt(4)\n" \
"v_mfma_f32_16x16x32_bf16 v[0:3], a[96:99], v[16:19], v[0:3]\n" \
"ds_read_b128 v[40:43], v124 offset:15360\n" \
"v_mfma_f32_16x16x32_bf16 v[4:7], a[100:103], v[16:19], v[4:7]\n" \
"v_mfma_f32_16x16x32_bf16 v[8:11], a[96:99], v[20:23], v[8:11]\n" \
"ds_read_b128 v[44:47], v124 offset:48128\n" \
"v_mfma_f32_16x16x32_bf16 v[12:15], a[100:103], v[20:23], v[12:15]\n" \
"s_waitcnt lgkmcnt(4)\n" \
"v_mfma_f32_16x16x32_bf16 v[0:3], a[104:107], v[24:27], v[0:3]\n" \
"ds_read_b128 v[16:19], v124 offset:16384\n" \
"v_mfma_f32_16x16x32_bf16 v[4:7], a[108:111], v[24:27], v[4:7]\n" \
"v_mfma_f32_16x16x32_bf16 v[8:11], a[104:107], v[28:31], v[8:11]\n" \
"ds_read_b128 v[20:23], v124 offset:49152\n" \
"v_mfma_f32_16x16x32_bf16 v[12:15], a[108:111], v[28:31], v[12:15]\n" \
"s_waitcnt lgkmcnt(4)\n" \
"v_mfma_f32_16x16x32_bf16 v[0:3], a[112:115], v[32:35], v[0:3]\n" \
"ds_read_b128 v[24:27], v124 offset:17408\n" \
"v_mfma_f32_16x16x32_bf16 v[4:7], a[116:119], v[32:35], v[4:7]\n" \
"v_mfma_f32_16x16x32_bf16 v[8:11], a[112:115], v[36:39], v[8:11]\n" \
"ds_read_b128 v[28:31], v124 offset:50176\n" \
"v_mfma_f32_16x16x32_bf16 v[12:15], a[116:119], v[36:39], v[12:15]\n" \
"s_waitcnt lgkmcnt(4)\n" \
"v_mfma_f32_16x16x32_bf16 v[0:3], a[120:123], v[40:43], v[0:3]\n" \
"ds_read_b128 v[32:35], v124 offset:18432\n" \
"v_mfma_f32_16x16x32_bf16 v[4:7], a[124:127], v[40:43], v[4:7]\n" \
"v_mfma_f32_16x16x32_bf16 v[8:11], a[120:123], v[44:47], v[8:11]\n" \
"ds_read_b128 v[36:39], v124 offset:51200\n" \
"v_mfma_f32_16x16x32_bf16 v[12:15], a[124:127], v[44:47], v[12:15]\n" \
"s_waitcnt lgkmcnt(4)\n" \
"v_mfma_f32_16x16x32_bf16 v[0:3], a[128:131], v[16:19], v[0:3]\n" \
"ds_read_b128 v[40:43], v124 offset:19456\n" \
"v_mfma_f32_16x16x32_bf16 v[4:7], a[132:135], v[16:19], v[4:7]\n" \
"v_mfma_f32_16x16x32_bf16 v[8:11], a[128:131], v[20:23], v[8:11]\n" \
"ds_read_b128 v[44:47], v124 offset:52224\n" \
"v_mfma_f32_16x16x32_bf16 v[12:15], a[132:135], v[20:23], v[12:15]\n" \
"s_waitcnt lgkmcnt(4)\n" \
"v_mfma_f32_16x16x32_bf16 v[0:3], a[136:139], v[24:27], v[0:3]\n" \
"ds_read_b128 v[16:19], v124 offset:20480\n" \
"v_mfma_f32_16x16x32_bf16 v[4:7], a[140:143], v[24:27], v[4:7]\n" \
"v_mfma_f32_16x16x32_bf16 v[8:11], a[136:139], v[28:31], v[8:11]\n" \
"ds_read_b128 v[20:23], v124 offset:53248\n" \
"v_mfma_f32_16x16x32_bf16 v[12:15], a[140:143], v[28:31], v[12:15]\n" \
"s_waitcnt lgkmcnt(4)\n" \
"v_mfma_f32_16x16x32_bf16 v[0:3], a[144:147], v[32:35], v[0:3]\n" \
"s_waitcnt vmcnt(0)\n" \
"v_cmp_ge_u32 vcc, s49, v126\n" \
"s_cmp_eq_u64 vcc, 0\n" \
"s_cselect_b32 s59, 1, 0\n" \
"s_cmp_lt_u32 s49, 64\n" \
"s_cselect_b32 s59, s59, 0\n" \
"v_mov_b32 v126, s59\n" \
"ds_write_b32 v123, v126\n" \
"s_waitcnt lgkmcnt(0)\n" \
"s_barrier\n" \
"v_mov_b32 v126, 133120\n" \
"ds_read_b32 v126, v126\n" \
"s_cmp_eq_u32 s69, 0\n" \
"s_cbranch_scc1 Lsd_nopub_%=\n" \
"v_mov_b32 v127, s48\n" \
"buffer_store_dword v127, off, s[44:47], s55 sc1\n" \
"s_mov_b32 s69, 0\n" \
"Lsd_nopub_%=:\n" \
"ds_read_b128 v[24:27], v124 offset:21504\n" \
"v_mfma_f32_16x16x32_bf16 v[4:7], a[148:151], v[32:35], v[4:7]\n" \
"v_mfma_f32_16x16x32_bf16 v[8:11], a[144:147], v[36:39], v[8:11]\n" \
"ds_read_b128 v[28:31], v124 offset:54272\n" \
"v_mfma_f32_16x16x32_bf16 v[12:15], a[148:151], v[36:39], v[12:15]\n" \
"v_mfma_f32_16x16x32_bf16 v[0:3], a[152:155], v[40:43], v[0:3]\n" \
"ds_read_b128 v[32:35], v124 offset:22528\n" \
"v_mfma_f32_16x16x32_bf16 v[4:7], a[156:159], v[40:43], v[4:7]\n" \
"v_mfma_f32_16x16x32_bf16 v[8:11], a[152:155], v[44:47], v[8:11]\n" \
"ds_read_b128 v[36:39], v124 offset:55296\n" \
"v_mfma_f32_16x16x32_bf16 v[12:15], a[156:159], v[44:47], v[12:15]\n" \
"v_mfma_f32_16x16x32_bf16 v[0:3], a[160:163], v[16:19], v[0:3]\n" \
"ds_read_b128 v[40:43], v124 offset:23552\n" \
"v_mfma_f32_16x16x32_bf16 v[4:7], a[164:167], v[16:19], v[4:7]\n" \
"v_mfma_f32_16x16x32_bf16 v[8:11], a[160:163], v[20:23], v[8:11]\n" \
"ds_read_b128 v[44:47], v124 offset:56320\n" \
"v_mfma_f32_16x16x32_bf16 v[12:15], a[164:167], v[20:23], v[12:15]\n" \
"s_waitcnt lgkmcnt(4)\n" \
"v_mfma_f32_16x16x32_bf16 v[0:3], a[168:171], v[24:27], v[0:3]\n" \
"ds_read_b128 v[16:19], v124 offset:24576\n" \
"v_mfma_f32_16x16x32_bf16 v[4:7], a[172:175], v[24:27], v[4:7]\n" \
"v_mfma_f32_16x16x32_bf16 v[8:11], a[168:171], v[28:31], v[8:11]\n" \
"ds_read_b128 v[20:23], v124 offset:57344\n" \
"v_mfma_f32_16x16x32_bf16 v[12:15], a[172:175], v[28:31], v[12:15]\n" \
"v_readfirstlane_b32 s68, v126\n" \
"s_cmp_eq_u32 s68, 0\n" \
"s_cbranch_scc1 Lsd_nopre_%=\n" \
"s_and_b32 s59, s49, 1\n" \
"s_lshl_b32 s59, s59, 16\n" \
"s_add_u32 s59, s59, s57\n" \
"s_add_u32 m0, s59, 0\n" \
"s_add_u32 s64, s50, 0\n" \
"buffer_load_dwordx4 v120, s[44:47], s64 offen offset:0 sc1 lds\n" \
"buffer_load_dwordx4 v120, s[44:47], s64 offen offset:1024 sc1 lds\n" \
"buffer_load_dwordx4 v120, s[44:47], s64 offen offset:2048 sc1 lds\n" \
"s_add_u32 m0, m0, s70\n" \
"s_add_u32 s64, s64, s70\n" \
"buffer_load_dwordx4 v120, s[44:47], s64 offen offset:2048 sc1 lds\n" \
"s_add_u32 m0, s59, 7168\n" \
"s_add_u32 s64, s50, 7168\n" \
"buffer_load_dwordx4 v120, s[44:47], s64 offen offset:0 sc1 lds\n" \
"buffer_load_dwordx4 v120, s[44:47], s64 offen offset:1024 sc1 lds\n" \
"buffer_load_dwordx4 v120, s[44:47], s64 offen offset:2048 sc1 lds\n" \
"s_add_u32 m0, m0, s70\n" \
"s_add_u32 s64, s64, s70\n" \
"buffer_load_dwordx4 v120, s[44:47], s64 offen offset:2048 sc1 lds\n" \
"s_add_u32 m0, s59, 14336\n" \
"s_add_u32 s64, s50, 14336\n" \
"buffer_load_dwordx4 v120, s[44:47], s64 offen offset:0 sc1 lds\n" \
"buffer_load_dwordx4 v120, s[44:47], s64 offen offset:1024 sc1 lds\n" \
"buffer_load_dwordx4 v120, s[44:47], s64 offen offset:2048 sc1 lds\n" \
"s_add_u32 m0, m0, s70\n" \
"s_add_u32 s64, s64, s70\n" \
"buffer_load_dwordx4 v120, s[44:47], s64 offen offset:2048 sc1 lds\n" \
"s_add_u32 m0, s59, 21504\n" \
"s_add_u32 s64, s50, 21504\n" \
"buffer_load_dwordx4 v120, s[44:47], s64 offen offset:0 sc1 lds\n" \
"buffer_load_dwordx4 v120, s[44:47], s64 offen offset:1024 sc1 lds\n" \
"buffer_load_dwordx4 v120, s[44:47], s64 offen offset:2048 sc1 lds\n" \
"s_add_u32 m0, m0, s70\n" \
"s_add_u32 s64, s64, s70\n" \
"buffer_load_dwordx4 v120, s[44:47], s64 offen offset:2048 sc1 lds\n" \
"s_add_u32 s50, s50, s51\n" \
"s_add_u32 s49, s49, 1\n" \
"Lsd_nopre_%=:\n" \
"s_waitcnt lgkmcnt(4)\n" \
"v_mfma_f32_16x16x32_bf16 v[0:3], a[176:179], v[32:35], v[0:3]\n" \
"ds_read_b128 v[24:27], v124 offset:25600\n" \
"v_mfma_f32_16x16x32_bf16 v[4:7], a[180:183], v[32:35], v[4:7]\n" \
"v_mfma_f32_16x16x32_bf16 v[8:11], a[176:179], v[36:39], v[8:11]\n" \
"ds_read_b128 v[28:31], v124 offset:58368\n" \
"v_mfma_f32_16x16x32_bf16 v[12:15], a[180:183], v[36:39], v[12:15]\n" \
"s_waitcnt lgkmcnt(4)\n" \
"v_mfma_f32_16x16x32_bf16 v[0:3], a[184:187], v[40:43], v[0:3]\n" \
"ds_read_b128 v[32:35], v124 offset:26624\n" \
"v_mfma_f32_16x16x32_bf16 v[4:7], a[188:191], v[40:43], v[4:7]\n" \
"v_mfma_f32_16x16x32_bf16 v[8:11], a[184:187], v[44:47], v[8:11]\n" \
"ds_read_b128 v[36:39], v124 offset:59392\n" \
"v_mfma_f32_16x16x32_bf16 v[12:15], a[188:191], v[44:47], v[12:15]\n" \
"s_waitcnt lgkmcnt(4)\n" \
"v_mfma_f32_16x16x32_bf16 v[0:3], a[192:195], v[16:19], v[0:3]\n" \
"ds_read_b128 v[40:43], v124 offset:27648\n" \
"v_mfma_f32_16x16x32_bf16 v[4:7], a[196:199], v[16:19], v[4:7]\n" \
"v_mfma_f32_16x16x32_bf16 v[8:11], a[192:195], v[20:23], v[8:11]\n" \
"ds_read_b128 v[44:47], v124 offset:60416\n" \
"v_mfma_f32_16x16x32_bf16 v[12:15], a[196:199], v[20:23], v[12:15]\n" \
"s_waitcnt lgkmcnt(4)\n" \
"v_mfma_f32_16x16x32_bf16 v[0:3], a[200:203], v[24:27], v[0:3]\n" \
"v_mfma_f32_16x16x32_bf16 v[4:7], a[204:207], v[24:27], v[4:7]\n" \
"v_mfma_f32_16x16x32_bf16 v[8:11], a[200:203], v[28:31], v[8:11]\n" \
"v_mfma_f32_16x16x32_bf16 v[12:15], a[204:207], v[28:31], v[12:15]\n" \
"s_waitcnt lgkmcnt(2)\n" \
"v_mfma_f32_16x16x32_bf16 v[0:3], a[208:211], v[32:35], v[0:3]\n" \
"v_mfma_f32_16x16x32_bf16 v[4:7], a[212:215], v[32:35], v[4:7]\n" \
"v_mfma_f32_16x16x32_bf16 v[8:11], a[208:211], v[36:39], v[8:11]\n" \
"v_mfma_f32_16x16x32_bf16 v[12:15], a[212:215], v[36:39], v[12:15]\n" \
"s_waitcnt lgkmcnt(0)\n" \
"v_mfma_f32_16x16x32_bf16 v[0:3], a[216:219], v[40:43], v[0:3]\n" \
"v_mfma_f32_16x16x32_bf16 v[4:7], a[220:223], v[40:43], v[4:7]\n" \
"v_mfma_f32_16x16x32_bf16 v[8:11], a[216:219], v[44:47], v[8:11]\n" \
"v_mfma_f32_16x16x32_bf16 v[12:15], a[220:223], v[44:47], v[12:15]\n" \
"s_add_u32 s64, s60, 65536\n" \
"buffer_store_dwordx4 v[0:3], v121, s[44:47], s60 offen offset:0 sc1\n" \
"buffer_store_dwordx4 v[4:7], v121, s[44:47], s60 offen offset:256 sc1\n" \
"s_nop 0\n" \
"buffer_store_dwordx4 v[8:11], v121, s[44:47], s64 offen offset:0 sc1\n" \
"s_nop 2\n" \
"buffer_store_dwordx4 v[12:15], v121, s[44:47], s64 offen offset:256 sc1\n" \
"s_add_u32 s60, s60, s61\n" \
"s_add_u32 s48, s48, 1\n" \
"s_mov_b32 s69, 1\n" \
"s_cmp_lg_u32 s68, 0\n" \
"s_cbranch_scc1 Lsd_seg_%=\n" \
"s_waitcnt vmcnt(0)\n" \
"s_barrier\n" \
"v_mov_b32 v127, s48\n" \
"buffer_store_dword v127, off, s[44:47], s55 sc1\n" \
"s_mov_b32 s69, 0\n" \
"s_cmp_lt_u32 s48, 64\n" \
"s_cbranch_scc1 Lsd_wait_%=\n" \
"Lsd_end_%=:\n" \
"s_waitcnt vmcnt(0) lgkmcnt(0)\n"
#define ASM_QK \
"s_mov_b32 s65, 0\n" \
"v_mov_b32 v106, v0\n" \
"s_xor_b32 s64, s66, 0\n" \
"s_lshl_b32 s64, s64, 2\n" \
"s_add_u32 s64, s64, 0\n" \
"s_lshl_b32 s64, s64, 19\n" \
"s_add_u32 m0, s55, 0\n" \
"s_nop 0\n" \
"buffer_load_dwordx4 v116, s[36:39], s64 offen offset:0 lds\n" \
"buffer_load_dwordx4 v117, s[36:39], s64 offen offset:1024 lds\n" \
"buffer_load_dwordx4 v118, s[36:39], s64 offen offset:2048 lds\n" \
"buffer_load_dwordx4 v119, s[36:39], s64 offen offset:3072 lds\n" \
"s_add_u32 m0, s55, 4096\n" \
"s_nop 0\n" \
"buffer_load_dwordx4 v116, s[40:43], s64 offen offset:0 lds\n" \
"buffer_load_dwordx4 v117, s[40:43], s64 offen offset:1024 lds\n" \
"buffer_load_dwordx4 v118, s[40:43], s64 offen offset:2048 lds\n" \
"buffer_load_dwordx4 v119, s[40:43], s64 offen offset:3072 lds\n" \
"s_xor_b32 s64, s66, 0\n" \
"s_lshl_b32 s64, s64, 2\n" \
"s_add_u32 s64, s64, 1\n" \
"s_lshl_b32 s64, s64, 19\n" \
"s_add_u32 m0, s55, 8192\n" \
"s_nop 0\n" \
"buffer_load_dwordx4 v116, s[36:39], s64 offen offset:0 lds\n" \
"buffer_load_dwordx4 v117, s[36:39], s64 offen offset:1024 lds\n" \
"buffer_load_dwordx4 v118, s[36:39], s64 offen offset:2048 lds\n" \
"buffer_load_dwordx4 v119, s[36:39], s64 offen offset:3072 lds\n" \
"s_add_u32 m0, s55, 12288\n" \
"s_nop 0\n" \
"buffer_load_dwordx4 v116, s[40:43], s64 offen offset:0 lds\n" \
"buffer_load_dwordx4 v117, s[40:43], s64 offen offset:1024 lds\n" \
"buffer_load_dwordx4 v118, s[40:43], s64 offen offset:2048 lds\n" \
"buffer_load_dwordx4 v119, s[40:43], s64 offen offset:3072 lds\n" \
"s_xor_b32 s64, s66, 0\n" \
"s_lshl_b32 s64, s64, 2\n" \
"s_add_u32 s64, s64, 2\n" \
"s_lshl_b32 s64, s64, 19\n" \
"s_add_u32 m0, s55, 16384\n" \
"s_nop 0\n" \
"buffer_load_dwordx4 v116, s[36:39], s64 offen offset:0 lds\n" \
"buffer_load_dwordx4 v117, s[36:39], s64 offen offset:1024 lds\n" \
"buffer_load_dwordx4 v118, s[36:39], s64 offen offset:2048 lds\n" \
"buffer_load_dwordx4 v119, s[36:39], s64 offen offset:3072 lds\n" \
"s_add_u32 m0, s55, 20480\n" \
"s_nop 0\n" \
"buffer_load_dwordx4 v116, s[40:43], s64 offen offset:0 lds\n" \
"buffer_load_dwordx4 v117, s[40:43], s64 offen offset:1024 lds\n" \
"buffer_load_dwordx4 v118, s[40:43], s64 offen offset:2048 lds\n" \
"buffer_load_dwordx4 v119, s[40:43], s64 offen offset:3072 lds\n" \
"s_xor_b32 s64, s66, 1\n" \
"s_lshl_b32 s64, s64, 2\n" \
"s_add_u32 s64, s64, 0\n" \
"s_lshl_b32 s64, s64, 19\n" \
"s_add_u32 m0, s55, 24576\n" \
"s_nop 0\n" \
"buffer_load_dwordx4 v116, s[36:39], s64 offen offset:0 lds\n" \
"buffer_load_dwordx4 v117, s[36:39], s64 offen offset:1024 lds\n" \
"buffer_load_dwordx4 v118, s[36:39], s64 offen offset:2048 lds\n" \
"buffer_load_dwordx4 v119, s[36:39], s64 offen offset:3072 lds\n" \
"s_add_u32 m0, s55, 28672\n" \
"s_nop 0\n" \
"buffer_load_dwordx4 v116, s[40:43], s64 offen offset:0 lds\n" \
"buffer_load_dwordx4 v117, s[40:43], s64 offen offset:1024 lds\n" \
"buffer_load_dwordx4 v118, s[40:43], s64 offen offset:2048 lds\n" \
"buffer_load_dwordx4 v119, s[40:43], s64 offen offset:3072 lds\n" \
"s_waitcnt vmcnt(24)\n" \
"ds_read_b64_tr_b16 a[0:1], v120 offset:0\n" \
"ds_read_b64_tr_b16 a[2:3], v120 offset:512\n" \
"ds_read_b64_tr_b16 a[4:5], v120 offset:2048\n" \
"ds_read_b64_tr_b16 a[6:7], v120 offset:2560\n" \
"ds_read_b64_tr_b16 a[8:9], v121 offset:0\n" \
"ds_read_b64_tr_b16 a[10:11], v121 offset:512\n" \
"ds_read_b64_tr_b16 a[12:13], v121 offset:2048\n" \
"ds_read_b64_tr_b16 a[14:15], v121 offset:2560\n" \
"ds_read_b64_tr_b16 a[16:17], v120 offset:4096\n" \
"ds_read_b64_tr_b16 a[18:19], v120 offset:4608\n" \
"ds_read_b64_tr_b16 a[20:21], v120 offset:6144\n" \
"ds_read_b64_tr_b16 a[22:23], v120 offset:6656\n" \
"ds_read_b64_tr_b16 a[24:25], v121 offset:4096\n" \
"ds_read_b64_tr_b16 a[26:27], v121 offset:4608\n" \
"ds_read_b64_tr_b16 a[28:29], v121 offset:6144\n" \
"ds_read_b64_tr_b16 a[30:31], v121 offset:6656\n" \
"s_waitcnt lgkmcnt(0)\n" \
"s_xor_b32 s64, s66, 1\n" \
"s_lshl_b32 s64, s64, 2\n" \
"s_add_u32 s64, s64, 1\n" \
"s_lshl_b32 s64, s64, 19\n" \
"s_add_u32 m0, s55, 0\n" \
"s_nop 0\n" \
"buffer_load_dwordx4 v116, s[36:39], s64 offen offset:0 lds\n" \
"buffer_load_dwordx4 v117, s[36:39], s64 offen offset:1024 lds\n" \
"buffer_load_dwordx4 v118, s[36:39], s64 offen offset:2048 lds\n" \
"buffer_load_dwordx4 v119, s[36:39], s64 offen offset:3072 lds\n" \
"s_add_u32 m0, s55, 4096\n" \
"s_nop 0\n" \
"buffer_load_dwordx4 v116, s[40:43], s64 offen offset:0 lds\n" \
"buffer_load_dwordx4 v117, s[40:43], s64 offen offset:1024 lds\n" \
"buffer_load_dwordx4 v118, s[40:43], s64 offen offset:2048 lds\n" \
"buffer_load_dwordx4 v119, s[40:43], s64 offen offset:3072 lds\n" \
"s_waitcnt vmcnt(24)\n" \
"ds_read_b64_tr_b16 a[32:33], v120 offset:8192\n" \
"ds_read_b64_tr_b16 a[34:35], v120 offset:8704\n" \
"ds_read_b64_tr_b16 a[36:37], v120 offset:10240\n" \
"ds_read_b64_tr_b16 a[38:39], v120 offset:10752\n" \
"ds_read_b64_tr_b16 a[40:41], v121 offset:8192\n" \
"ds_read_b64_tr_b16 a[42:43], v121 offset:8704\n" \
"ds_read_b64_tr_b16 a[44:45], v121 offset:10240\n" \
"ds_read_b64_tr_b16 a[46:47], v121 offset:10752\n" \
"ds_read_b64_tr_b16 a[48:49], v120 offset:12288\n" \
"ds_read_b64_tr_b16 a[50:51], v120 offset:12800\n" \
"ds_read_b64_tr_b16 a[52:53], v120 offset:14336\n" \
"ds_read_b64_tr_b16 a[54:55], v120 offset:14848\n" \
"ds_read_b64_tr_b16 a[56:57], v121 offset:12288\n" \
"ds_read_b64_tr_b16 a[58:59], v121 offset:12800\n" \
"ds_read_b64_tr_b16 a[60:61], v121 offset:14336\n" \
"ds_read_b64_tr_b16 a[62:63], v121 offset:14848\n" \
"s_waitcnt lgkmcnt(0)\n" \
"s_xor_b32 s64, s66, 2\n" \
"s_lshl_b32 s64, s64, 2\n" \
"s_add_u32 s64, s64, 0\n" \
"s_lshl_b32 s64, s64, 19\n" \
"s_add_u32 m0, s55, 8192\n" \
"s_nop 0\n" \
"buffer_load_dwordx4 v116, s[36:39], s64 offen offset:0 lds\n" \
"buffer_load_dwordx4 v117, s[36:39], s64 offen offset:1024 lds\n" \
"buffer_load_dwordx4 v118, s[36:39], s64 offen offset:2048 lds\n" \
"buffer_load_dwordx4 v119, s[36:39], s64 offen offset:3072 lds\n" \
"s_add_u32 m0, s55, 12288\n" \
"s_nop 0\n" \
"buffer_load_dwordx4 v116, s[40:43], s64 offen offset:0 lds\n" \
"buffer_load_dwordx4 v117, s[40:43], s64 offen offset:1024 lds\n" \
"buffer_load_dwordx4 v118, s[40:43], s64 offen offset:2048 lds\n" \
"buffer_load_dwordx4 v119, s[40:43], s64 offen offset:3072 lds\n" \
"s_waitcnt vmcnt(24)\n" \
"ds_read_b64_tr_b16 a[64:65], v120 offset:16384\n" \
"ds_read_b64_tr_b16 a[66:67], v120 offset:16896\n" \
"ds_read_b64_tr_b16 a[68:69], v120 offset:18432\n" \
"ds_read_b64_tr_b16 a[70:71], v120 offset:18944\n" \
"ds_read_b64_tr_b16 a[72:73], v121 offset:16384\n" \
"ds_read_b64_tr_b16 a[74:75], v121 offset:16896\n" \
"ds_read_b64_tr_b16 a[76:77], v121 offset:18432\n" \
"ds_read_b64_tr_b16 a[78:79], v121 offset:18944\n" \
"ds_read_b64_tr_b16 a[80:81], v120 offset:20480\n" \
"ds_read_b64_tr_b16 a[82:83], v120 offset:20992\n" \
"ds_read_b64_tr_b16 a[84:85], v120 offset:22528\n" \
"ds_read_b64_tr_b16 a[86:87], v120 offset:23040\n" \
"ds_read_b64_tr_b16 a[88:89], v121 offset:20480\n" \
"ds_read_b64_tr_b16 a[90:91], v121 offset:20992\n" \
"ds_read_b64_tr_b16 a[92:93], v121 offset:22528\n" \
"ds_read_b64_tr_b16 a[94:95], v121 offset:23040\n" \
"s_waitcnt lgkmcnt(0)\n" \
"s_xor_b32 s64, s66, 3\n" \
"s_lshl_b32 s64, s64, 2\n" \
"s_add_u32 s64, s64, 0\n" \
"s_lshl_b32 s64, s64, 19\n" \
"s_add_u32 m0, s55, 16384\n" \
"s_nop 0\n" \
"buffer_load_dwordx4 v116, s[36:39], s64 offen offset:0 lds\n" \
"buffer_load_dwordx4 v117, s[36:39], s64 offen offset:1024 lds\n" \
"buffer_load_dwordx4 v118, s[36:39], s64 offen offset:2048 lds\n" \
"buffer_load_dwordx4 v119, s[36:39], s64 offen offset:3072 lds\n" \
"s_add_u32 m0, s55, 20480\n" \
"s_nop 0\n" \
"buffer_load_dwordx4 v116, s[40:43], s64 offen offset:0 lds\n" \
"buffer_load_dwordx4 v117, s[40:43], s64 offen offset:1024 lds\n" \
"buffer_load_dwordx4 v118, s[40:43], s64 offen offset:2048 lds\n" \
"buffer_load_dwordx4 v119, s[40:43], s64 offen offset:3072 lds\n" \
"s_waitcnt vmcnt(24)\n" \
"ds_read_b64_tr_b16 a[96:97], v120 offset:24576\n" \
"ds_read_b64_tr_b16 a[98:99], v120 offset:25088\n" \
"ds_read_b64_tr_b16 a[100:101], v120 offset:26624\n" \
"ds_read_b64_tr_b16 a[102:103], v120 offset:27136\n" \
"ds_read_b64_tr_b16 a[104:105], v121 offset:24576\n" \
"ds_read_b64_tr_b16 a[106:107], v121 offset:25088\n" \
"ds_read_b64_tr_b16 a[108:109], v121 offset:26624\n" \
"ds_read_b64_tr_b16 a[110:111], v121 offset:27136\n" \
"ds_read_b64_tr_b16 a[112:113], v120 offset:28672\n" \
"ds_read_b64_tr_b16 a[114:115], v120 offset:29184\n" \
"ds_read_b64_tr_b16 a[116:117], v120 offset:30720\n" \
"ds_read_b64_tr_b16 a[118:119], v120 offset:31232\n" \
"ds_read_b64_tr_b16 a[120:121], v121 offset:28672\n" \
"ds_read_b64_tr_b16 a[122:123], v121 offset:29184\n" \
"ds_read_b64_tr_b16 a[124:125], v121 offset:30720\n" \
"ds_read_b64_tr_b16 a[126:127], v121 offset:31232\n" \
"s_waitcnt lgkmcnt(0)\n" \
"s_xor_b32 s64, s66, 3\n" \
"s_lshl_b32 s64, s64, 2\n" \
"s_add_u32 s64, s64, 1\n" \
"s_lshl_b32 s64, s64, 19\n" \
"s_add_u32 m0, s55, 24576\n" \
"s_nop 0\n" \
"buffer_load_dwordx4 v116, s[36:39], s64 offen offset:0 lds\n" \
"buffer_load_dwordx4 v117, s[36:39], s64 offen offset:1024 lds\n" \
"buffer_load_dwordx4 v118, s[36:39], s64 offen offset:2048 lds\n" \
"buffer_load_dwordx4 v119, s[36:39], s64 offen offset:3072 lds\n" \
"s_add_u32 m0, s55, 28672\n" \
"s_nop 0\n" \
"buffer_load_dwordx4 v116, s[40:43], s64 offen offset:0 lds\n" \
"buffer_load_dwordx4 v117, s[40:43], s64 offen offset:1024 lds\n" \
"buffer_load_dwordx4 v118, s[40:43], s64 offen offset:2048 lds\n" \
"buffer_load_dwordx4 v119, s[40:43], s64 offen offset:3072 lds\n" \
"s_waitcnt vmcnt(24)\n" \
"ds_read_b64_tr_b16 a[128:129], v120 offset:0\n" \
"ds_read_b64_tr_b16 a[130:131], v120 offset:512\n" \
"ds_read_b64_tr_b16 a[132:133], v120 offset:2048\n" \
"ds_read_b64_tr_b16 a[134:135], v120 offset:2560\n" \
"ds_read_b64_tr_b16 a[136:137], v121 offset:0\n" \
"ds_read_b64_tr_b16 a[138:139], v121 offset:512\n" \
"ds_read_b64_tr_b16 a[140:141], v121 offset:2048\n" \
"ds_read_b64_tr_b16 a[142:143], v121 offset:2560\n" \
"ds_read_b64_tr_b16 a[144:145], v120 offset:4096\n" \
"ds_read_b64_tr_b16 a[146:147], v120 offset:4608\n" \
"ds_read_b64_tr_b16 a[148:149], v120 offset:6144\n" \
"ds_read_b64_tr_b16 a[150:151], v120 offset:6656\n" \
"ds_read_b64_tr_b16 a[152:153], v121 offset:4096\n" \
"ds_read_b64_tr_b16 a[154:155], v121 offset:4608\n" \
"ds_read_b64_tr_b16 a[156:157], v121 offset:6144\n" \
"ds_read_b64_tr_b16 a[158:159], v121 offset:6656\n" \
"s_waitcnt lgkmcnt(0)\n" \
"s_xor_b32 s64, s66, 1\n" \
"s_lshl_b32 s64, s64, 2\n" \
"s_add_u32 s64, s64, 2\n" \
"s_lshl_b32 s64, s64, 19\n" \
"s_add_u32 m0, s55, 0\n" \
"s_nop 0\n" \
"buffer_load_dwordx4 v116, s[36:39], s64 offen offset:0 lds\n" \
"buffer_load_dwordx4 v117, s[36:39], s64 offen offset:1024 lds\n" \
"buffer_load_dwordx4 v118, s[36:39], s64 offen offset:2048 lds\n" \
"buffer_load_dwordx4 v119, s[36:39], s64 offen offset:3072 lds\n" \
"s_add_u32 m0, s55, 4096\n" \
"s_nop 0\n" \
"buffer_load_dwordx4 v116, s[40:43], s64 offen offset:0 lds\n" \
"buffer_load_dwordx4 v117, s[40:43], s64 offen offset:1024 lds\n" \
"buffer_load_dwordx4 v118, s[40:43], s64 offen offset:2048 lds\n" \
"buffer_load_dwordx4 v119, s[40:43], s64 offen offset:3072 lds\n" \
"s_waitcnt vmcnt(24)\n" \
"ds_read_b64_tr_b16 a[160:161], v120 offset:8192\n" \
"ds_read_b64_tr_b16 a[162:163], v120 offset:8704\n" \
"ds_read_b64_tr_b16 a[164:165], v120 offset:10240\n" \
"ds_read_b64_tr_b16 a[166:167], v120 offset:10752\n" \
"ds_read_b64_tr_b16 a[168:169], v121 offset:8192\n" \
"ds_read_b64_tr_b16 a[170:171], v121 offset:8704\n" \
"ds_read_b64_tr_b16 a[172:173], v121 offset:10240\n" \
"ds_read_b64_tr_b16 a[174:175], v121 offset:10752\n" \
"ds_read_b64_tr_b16 a[176:177], v120 offset:12288\n" \
"ds_read_b64_tr_b16 a[178:179], v120 offset:12800\n" \
"ds_read_b64_tr_b16 a[180:181], v120 offset:14336\n" \
"ds_read_b64_tr_b16 a[182:183], v120 offset:14848\n" \
"ds_read_b64_tr_b16 a[184:185], v121 offset:12288\n" \
"ds_read_b64_tr_b16 a[186:187], v121 offset:12800\n" \
"ds_read_b64_tr_b16 a[188:189], v121 offset:14336\n" \
"ds_read_b64_tr_b16 a[190:191], v121 offset:14848\n" \
"s_waitcnt lgkmcnt(0)\n" \
"s_xor_b32 s64, s66, 2\n" \
"s_lshl_b32 s64, s64, 2\n" \
"s_add_u32 s64, s64, 2\n" \
"s_lshl_b32 s64, s64, 19\n" \
"s_add_u32 m0, s55, 8192\n" \
"s_nop 0\n" \
"buffer_load_dwordx4 v116, s[36:39], s64 offen offset:0 lds\n" \
"buffer_load_dwordx4 v117, s[36:39], s64 offen offset:1024 lds\n" \
"buffer_load_dwordx4 v118, s[36:39], s64 offen offset:2048 lds\n" \
"buffer_load_dwordx4 v119, s[36:39], s64 offen offset:3072 lds\n" \
"s_add_u32 m0, s55, 12288\n" \
"s_nop 0\n" \
"buffer_load_dwordx4 v116, s[40:43], s64 offen offset:0 lds\n" \
"buffer_load_dwordx4 v117, s[40:43], s64 offen offset:1024 lds\n" \
"buffer_load_dwordx4 v118, s[40:43], s64 offen offset:2048 lds\n" \
"buffer_load_dwordx4 v119, s[40:43], s64 offen offset:3072 lds\n" \
"s_waitcnt vmcnt(24)\n" \
"ds_read_b64_tr_b16 a[192:193], v120 offset:16384\n" \
"ds_read_b64_tr_b16 a[194:195], v120 offset:16896\n" \
"ds_read_b64_tr_b16 a[196:197], v120 offset:18432\n" \
"ds_read_b64_tr_b16 a[198:199], v120 offset:18944\n" \
"ds_read_b64_tr_b16 a[200:201], v121 offset:16384\n" \
"ds_read_b64_tr_b16 a[202:203], v121 offset:16896\n" \
"ds_read_b64_tr_b16 a[204:205], v121 offset:18432\n" \
"ds_read_b64_tr_b16 a[206:207], v121 offset:18944\n" \
"ds_read_b64_tr_b16 a[208:209], v120 offset:20480\n" \
"ds_read_b64_tr_b16 a[210:211], v120 offset:20992\n" \
"ds_read_b64_tr_b16 a[212:213], v120 offset:22528\n" \
"ds_read_b64_tr_b16 a[214:215], v120 offset:23040\n" \
"ds_read_b64_tr_b16 a[216:217], v121 offset:20480\n" \
"ds_read_b64_tr_b16 a[218:219], v121 offset:20992\n" \
"ds_read_b64_tr_b16 a[220:221], v121 offset:22528\n" \
"ds_read_b64_tr_b16 a[222:223], v121 offset:23040\n" \
"s_waitcnt lgkmcnt(0)\n" \
"s_xor_b32 s64, s66, 2\n" \
"s_lshl_b32 s64, s64, 2\n" \
"s_add_u32 s64, s64, 3\n" \
"s_lshl_b32 s64, s64, 19\n" \
"s_add_u32 m0, s55, 16384\n" \
"s_nop 0\n" \
"buffer_load_dwordx4 v116, s[36:39], s64 offen offset:0 lds\n" \
"buffer_load_dwordx4 v117, s[36:39], s64 offen offset:1024 lds\n" \
"buffer_load_dwordx4 v118, s[36:39], s64 offen offset:2048 lds\n" \
"buffer_load_dwordx4 v119, s[36:39], s64 offen offset:3072 lds\n" \
"s_add_u32 m0, s55, 20480\n" \
"s_nop 0\n" \
"buffer_load_dwordx4 v116, s[40:43], s64 offen offset:0 lds\n" \
"buffer_load_dwordx4 v117, s[40:43], s64 offen offset:1024 lds\n" \
"buffer_load_dwordx4 v118, s[40:43], s64 offen offset:2048 lds\n" \
"buffer_load_dwordx4 v119, s[40:43], s64 offen offset:3072 lds\n" \
"s_waitcnt vmcnt(24)\n" \
"ds_read_b64_tr_b16 a[224:225], v120 offset:24576\n" \
"ds_read_b64_tr_b16 a[226:227], v120 offset:25088\n" \
"ds_read_b64_tr_b16 a[228:229], v120 offset:26624\n" \
"ds_read_b64_tr_b16 a[230:231], v120 offset:27136\n" \
"ds_read_b64_tr_b16 a[232:233], v121 offset:24576\n" \
"ds_read_b64_tr_b16 a[234:235], v121 offset:25088\n" \
"ds_read_b64_tr_b16 a[236:237], v121 offset:26624\n" \
"ds_read_b64_tr_b16 a[238:239], v121 offset:27136\n" \
"ds_read_b64_tr_b16 a[240:241], v120 offset:28672\n" \
"ds_read_b64_tr_b16 a[242:243], v120 offset:29184\n" \
"ds_read_b64_tr_b16 a[244:245], v120 offset:30720\n" \
"ds_read_b64_tr_b16 a[246:247], v120 offset:31232\n" \
"ds_read_b64_tr_b16 a[248:249], v121 offset:28672\n" \
"ds_read_b64_tr_b16 a[250:251], v121 offset:29184\n" \
"ds_read_b64_tr_b16 a[252:253], v121 offset:30720\n" \
"ds_read_b64_tr_b16 a[254:255], v121 offset:31232\n" \
"s_waitcnt lgkmcnt(0)\n" \
"s_xor_b32 s64, s66, 3\n" \
"s_lshl_b32 s64, s64, 2\n" \
"s_add_u32 s64, s64, 2\n" \
"s_lshl_b32 s64, s64, 19\n" \
"s_add_u32 m0, s55, 24576\n" \
"s_nop 0\n" \
"buffer_load_dwordx4 v116, s[36:39], s64 offen offset:0 lds\n" \
"buffer_load_dwordx4 v117, s[36:39], s64 offen offset:1024 lds\n" \
"buffer_load_dwordx4 v118, s[36:39], s64 offen offset:2048 lds\n" \
"buffer_load_dwordx4 v119, s[36:39], s64 offen offset:3072 lds\n" \
"s_add_u32 m0, s55, 28672\n" \
"s_nop 0\n" \
"buffer_load_dwordx4 v116, s[40:43], s64 offen offset:0 lds\n" \
"buffer_load_dwordx4 v117, s[40:43], s64 offen offset:1024 lds\n" \
"buffer_load_dwordx4 v118, s[40:43], s64 offen offset:2048 lds\n" \
"buffer_load_dwordx4 v119, s[40:43], s64 offen offset:3072 lds\n" \
"s_waitcnt vmcnt(24)\n" \
"ds_read_b64_tr_b16 v[128:129], v120 offset:0\n" \
"ds_read_b64_tr_b16 v[130:131], v120 offset:512\n" \
"ds_read_b64_tr_b16 v[132:133], v120 offset:2048\n" \
"ds_read_b64_tr_b16 v[134:135], v120 offset:2560\n" \
"ds_read_b64_tr_b16 v[136:137], v121 offset:0\n" \
"ds_read_b64_tr_b16 v[138:139], v121 offset:512\n" \
"ds_read_b64_tr_b16 v[140:141], v121 offset:2048\n" \
"ds_read_b64_tr_b16 v[142:143], v121 offset:2560\n" \
"ds_read_b64_tr_b16 v[144:145], v120 offset:4096\n" \
"ds_read_b64_tr_b16 v[146:147], v120 offset:4608\n" \
"ds_read_b64_tr_b16 v[148:149], v120 offset:6144\n" \
"ds_read_b64_tr_b16 v[150:151], v120 offset:6656\n" \
"ds_read_b64_tr_b16 v[152:153], v121 offset:4096\n" \
"ds_read_b64_tr_b16 v[154:155], v121 offset:4608\n" \
"ds_read_b64_tr_b16 v[156:157], v121 offset:6144\n" \
"ds_read_b64_tr_b16 v[158:159], v121 offset:6656\n" \
"s_waitcnt lgkmcnt(0)\n" \
"s_xor_b32 s64, s66, 0\n" \
"s_lshl_b32 s64, s64, 2\n" \
"s_add_u32 s64, s64, 3\n" \
"s_lshl_b32 s64, s64, 19\n" \
"s_add_u32 m0, s55, 0\n" \
"s_nop 0\n" \
"buffer_load_dwordx4 v116, s[36:39], s64 offen offset:0 lds\n" \
"buffer_load_dwordx4 v117, s[36:39], s64 offen offset:1024 lds\n" \
"buffer_load_dwordx4 v118, s[36:39], s64 offen offset:2048 lds\n" \
"buffer_load_dwordx4 v119, s[36:39], s64 offen offset:3072 lds\n" \
"s_add_u32 m0, s55, 4096\n" \
"s_nop 0\n" \
"buffer_load_dwordx4 v116, s[40:43], s64 offen offset:0 lds\n" \
"buffer_load_dwordx4 v117, s[40:43], s64 offen offset:1024 lds\n" \
"buffer_load_dwordx4 v118, s[40:43], s64 offen offset:2048 lds\n" \
"buffer_load_dwordx4 v119, s[40:43], s64 offen offset:3072 lds\n" \
"s_waitcnt vmcnt(24)\n" \
"ds_read_b64_tr_b16 v[160:161], v120 offset:8192\n" \
"ds_read_b64_tr_b16 v[162:163], v120 offset:8704\n" \
"ds_read_b64_tr_b16 v[164:165], v120 offset:10240\n" \
"ds_read_b64_tr_b16 v[166:167], v120 offset:10752\n" \
"ds_read_b64_tr_b16 v[168:169], v121 offset:8192\n" \
"ds_read_b64_tr_b16 v[170:171], v121 offset:8704\n" \
"ds_read_b64_tr_b16 v[172:173], v121 offset:10240\n" \
"ds_read_b64_tr_b16 v[174:175], v121 offset:10752\n" \
"ds_read_b64_tr_b16 v[176:177], v120 offset:12288\n" \
"ds_read_b64_tr_b16 v[178:179], v120 offset:12800\n" \
"ds_read_b64_tr_b16 v[180:181], v120 offset:14336\n" \
"ds_read_b64_tr_b16 v[182:183], v120 offset:14848\n" \
"ds_read_b64_tr_b16 v[184:185], v121 offset:12288\n" \
"ds_read_b64_tr_b16 v[186:187], v121 offset:12800\n" \
"ds_read_b64_tr_b16 v[188:189], v121 offset:14336\n" \
"ds_read_b64_tr_b16 v[190:191], v121 offset:14848\n" \
"s_waitcnt lgkmcnt(0)\n" \
"s_xor_b32 s64, s66, 2\n" \
"s_lshl_b32 s64, s64, 2\n" \
"s_add_u32 s64, s64, 1\n" \
"s_lshl_b32 s64, s64, 19\n" \
"s_add_u32 m0, s55, 8192\n" \
"s_nop 0\n" \
"buffer_load_dwordx4 v116, s[36:39], s64 offen offset:0 lds\n" \
"buffer_load_dwordx4 v117, s[36:39], s64 offen offset:1024 lds\n" \
"buffer_load_dwordx4 v118, s[36:39], s64 offen offset:2048 lds\n" \
"buffer_load_dwordx4 v119, s[36:39], s64 offen offset:3072 lds\n" \
"s_add_u32 m0, s55, 12288\n" \
"s_nop 0\n" \
"buffer_load_dwordx4 v116, s[40:43], s64 offen offset:0 lds\n" \
"buffer_load_dwordx4 v117, s[40:43], s64 offen offset:1024 lds\n" \
"buffer_load_dwordx4 v118, s[40:43], s64 offen offset:2048 lds\n" \
"buffer_load_dwordx4 v119, s[40:43], s64 offen offset:3072 lds\n" \
"s_waitcnt vmcnt(24)\n" \
"ds_read_b64_tr_b16 v[192:193], v120 offset:16384\n" \
"ds_read_b64_tr_b16 v[194:195], v120 offset:16896\n" \
"ds_read_b64_tr_b16 v[196:197], v120 offset:18432\n" \
"ds_read_b64_tr_b16 v[198:199], v120 offset:18944\n" \
"ds_read_b64_tr_b16 v[200:201], v121 offset:16384\n" \
"ds_read_b64_tr_b16 v[202:203], v121 offset:16896\n" \
"ds_read_b64_tr_b16 v[204:205], v121 offset:18432\n" \
"ds_read_b64_tr_b16 v[206:207], v121 offset:18944\n" \
"ds_read_b64_tr_b16 v[208:209], v120 offset:20480\n" \
"ds_read_b64_tr_b16 v[210:211], v120 offset:20992\n" \
"ds_read_b64_tr_b16 v[212:213], v120 offset:22528\n" \
"ds_read_b64_tr_b16 v[214:215], v120 offset:23040\n" \
"ds_read_b64_tr_b16 v[216:217], v121 offset:20480\n" \
"ds_read_b64_tr_b16 v[218:219], v121 offset:20992\n" \
"ds_read_b64_tr_b16 v[220:221], v121 offset:22528\n" \
"ds_read_b64_tr_b16 v[222:223], v121 offset:23040\n" \
"s_waitcnt lgkmcnt(0)\n" \
"s_xor_b32 s64, s66, 1\n" \
"s_lshl_b32 s64, s64, 2\n" \
"s_add_u32 s64, s64, 3\n" \
"s_lshl_b32 s64, s64, 19\n" \
"s_add_u32 m0, s55, 16384\n" \
"s_nop 0\n" \
"buffer_load_dwordx4 v116, s[36:39], s64 offen offset:0 lds\n" \
"buffer_load_dwordx4 v117, s[36:39], s64 offen offset:1024 lds\n" \
"buffer_load_dwordx4 v118, s[36:39], s64 offen offset:2048 lds\n" \
"buffer_load_dwordx4 v119, s[36:39], s64 offen offset:3072 lds\n" \
"s_add_u32 m0, s55, 20480\n" \
"s_nop 0\n" \
"buffer_load_dwordx4 v116, s[40:43], s64 offen offset:0 lds\n" \
"buffer_load_dwordx4 v117, s[40:43], s64 offen offset:1024 lds\n" \
"buffer_load_dwordx4 v118, s[40:43], s64 offen offset:2048 lds\n" \
"buffer_load_dwordx4 v119, s[40:43], s64 offen offset:3072 lds\n" \
"s_waitcnt vmcnt(24)\n" \
"ds_read_b64_tr_b16 v[224:225], v120 offset:24576\n" \
"ds_read_b64_tr_b16 v[226:227], v120 offset:25088\n" \
"ds_read_b64_tr_b16 v[228:229], v120 offset:26624\n" \
"ds_read_b64_tr_b16 v[230:231], v120 offset:27136\n" \
"ds_read_b64_tr_b16 v[232:233], v121 offset:24576\n" \
"ds_read_b64_tr_b16 v[234:235], v121 offset:25088\n" \
"ds_read_b64_tr_b16 v[236:237], v121 offset:26624\n" \
"ds_read_b64_tr_b16 v[238:239], v121 offset:27136\n" \
"ds_read_b64_tr_b16 v[240:241], v120 offset:28672\n" \
"ds_read_b64_tr_b16 v[242:243], v120 offset:29184\n" \
"ds_read_b64_tr_b16 v[244:245], v120 offset:30720\n" \
"ds_read_b64_tr_b16 v[246:247], v120 offset:31232\n" \
"ds_read_b64_tr_b16 v[248:249], v121 offset:28672\n" \
"ds_read_b64_tr_b16 v[250:251], v121 offset:29184\n" \
"ds_read_b64_tr_b16 v[252:253], v121 offset:30720\n" \
"ds_read_b64_tr_b16 v[254:255], v121 offset:31232\n" \
"s_waitcnt lgkmcnt(0)\n" \
"s_xor_b32 s64, s66, 3\n" \
"s_lshl_b32 s64, s64, 2\n" \
"s_add_u32 s64, s64, 3\n" \
"s_lshl_b32 s64, s64, 19\n" \
"s_add_u32 m0, s55, 24576\n" \
"s_nop 0\n" \
"buffer_load_dwordx4 v116, s[36:39], s64 offen offset:0 lds\n" \
"buffer_load_dwordx4 v117, s[36:39], s64 offen offset:1024 lds\n" \
"buffer_load_dwordx4 v118, s[36:39], s64 offen offset:2048 lds\n" \
"buffer_load_dwordx4 v119, s[36:39], s64 offen offset:3072 lds\n" \
"s_add_u32 m0, s55, 28672\n" \
"s_nop 0\n" \
"buffer_load_dwordx4 v116, s[40:43], s64 offen offset:0 lds\n" \
"buffer_load_dwordx4 v117, s[40:43], s64 offen offset:1024 lds\n" \
"buffer_load_dwordx4 v118, s[40:43], s64 offen offset:2048 lds\n" \
"buffer_load_dwordx4 v119, s[40:43], s64 offen offset:3072 lds\n" \
"v_add_u32 v105, s55, v124\n" \
"s_waitcnt vmcnt(24)\n" \
"ds_read_b64_tr_b16 v[0:1], v120 offset:0\n" \
"ds_read_b64_tr_b16 v[2:3], v120 offset:512\n" \
"ds_read_b64_tr_b16 v[4:5], v120 offset:2048\n" \
"ds_read_b64_tr_b16 v[6:7], v120 offset:2560\n" \
"ds_read_b64_tr_b16 v[8:9], v121 offset:0\n" \
"ds_read_b64_tr_b16 v[10:11], v121 offset:512\n" \
"ds_read_b64_tr_b16 v[12:13], v121 offset:2048\n" \
"ds_read_b64_tr_b16 v[14:15], v121 offset:2560\n" \
"ds_read_b64_tr_b16 v[16:17], v120 offset:4096\n" \
"ds_read_b64_tr_b16 v[18:19], v120 offset:4608\n" \
"ds_read_b64_tr_b16 v[20:21], v120 offset:6144\n" \
"ds_read_b64_tr_b16 v[22:23], v120 offset:6656\n" \
"ds_read_b64_tr_b16 v[24:25], v121 offset:4096\n" \
"ds_read_b64_tr_b16 v[26:27], v121 offset:4608\n" \
"ds_read_b64_tr_b16 v[28:29], v121 offset:6144\n" \
"ds_read_b64_tr_b16 v[30:31], v121 offset:6656\n" \
"s_waitcnt lgkmcnt(0)\n" \
"ds_write_b128 v105, v[0:3] offset:0\n" \
"ds_write_b128 v105, v[4:7] offset:1024\n" \
"ds_write_b128 v105, v[8:11] offset:2048\n" \
"ds_write_b128 v105, v[12:15] offset:3072\n" \
"ds_write_b128 v105, v[16:19] offset:4096\n" \
"ds_write_b128 v105, v[20:23] offset:5120\n" \
"ds_write_b128 v105, v[24:27] offset:6144\n" \
"ds_write_b128 v105, v[28:31] offset:7168\n" \
"s_waitcnt lgkmcnt(0)\n" \
"s_waitcnt vmcnt(16)\n" \
"ds_read_b64_tr_b16 v[0:1], v120 offset:8192\n" \
"ds_read_b64_tr_b16 v[2:3], v120 offset:8704\n" \
"ds_read_b64_tr_b16 v[4:5], v120 offset:10240\n" \
"ds_read_b64_tr_b16 v[6:7], v120 offset:10752\n" \
"ds_read_b64_tr_b16 v[8:9], v121 offset:8192\n" \
"ds_read_b64_tr_b16 v[10:11], v121 offset:8704\n" \
"ds_read_b64_tr_b16 v[12:13], v121 offset:10240\n" \
"ds_read_b64_tr_b16 v[14:15], v121 offset:10752\n" \
"ds_read_b64_tr_b16 v[16:17], v120 offset:12288\n" \
"ds_read_b64_tr_b16 v[18:19], v120 offset:12800\n" \
"ds_read_b64_tr_b16 v[20:21], v120 offset:14336\n" \
"ds_read_b64_tr_b16 v[22:23], v120 offset:14848\n" \
"ds_read_b64_tr_b16 v[24:25], v121 offset:12288\n" \
"ds_read_b64_tr_b16 v[26:27], v121 offset:12800\n" \
"ds_read_b64_tr_b16 v[28:29], v121 offset:14336\n" \
"ds_read_b64_tr_b16 v[30:31], v121 offset:14848\n" \
"s_waitcnt lgkmcnt(0)\n" \
"ds_write_b128 v105, v[0:3] offset:8192\n" \
"ds_write_b128 v105, v[4:7] offset:9216\n" \
"ds_write_b128 v105, v[8:11] offset:10240\n" \
"ds_write_b128 v105, v[12:15] offset:11264\n" \
"ds_write_b128 v105, v[16:19] offset:12288\n" \
"ds_write_b128 v105, v[20:23] offset:13312\n" \
"ds_write_b128 v105, v[24:27] offset:14336\n" \
"ds_write_b128 v105, v[28:31] offset:15360\n" \
"s_waitcnt lgkmcnt(0)\n" \
"s_waitcnt vmcnt(8)\n" \
"ds_read_b64_tr_b16 v[0:1], v120 offset:16384\n" \
"ds_read_b64_tr_b16 v[2:3], v120 offset:16896\n" \
"ds_read_b64_tr_b16 v[4:5], v120 offset:18432\n" \
"ds_read_b64_tr_b16 v[6:7], v120 offset:18944\n" \
"ds_read_b64_tr_b16 v[8:9], v121 offset:16384\n" \
"ds_read_b64_tr_b16 v[10:11], v121 offset:16896\n" \
"ds_read_b64_tr_b16 v[12:13], v121 offset:18432\n" \
"ds_read_b64_tr_b16 v[14:15], v121 offset:18944\n" \
"ds_read_b64_tr_b16 v[16:17], v120 offset:20480\n" \
"ds_read_b64_tr_b16 v[18:19], v120 offset:20992\n" \
"ds_read_b64_tr_b16 v[20:21], v120 offset:22528\n" \
"ds_read_b64_tr_b16 v[22:23], v120 offset:23040\n" \
"ds_read_b64_tr_b16 v[24:25], v121 offset:20480\n" \
"ds_read_b64_tr_b16 v[26:27], v121 offset:20992\n" \
"ds_read_b64_tr_b16 v[28:29], v121 offset:22528\n" \
"ds_read_b64_tr_b16 v[30:31], v121 offset:23040\n" \
"s_waitcnt lgkmcnt(0)\n" \
"ds_write_b128 v105, v[0:3] offset:16384\n" \
"ds_write_b128 v105, v[4:7] offset:17408\n" \
"ds_write_b128 v105, v[8:11] offset:18432\n" \
"ds_write_b128 v105, v[12:15] offset:19456\n" \
"ds_write_b128 v105, v[16:19] offset:20480\n" \
"ds_write_b128 v105, v[20:23] offset:21504\n" \
"ds_write_b128 v105, v[24:27] offset:22528\n" \
"ds_write_b128 v105, v[28:31] offset:23552\n" \
"s_waitcnt lgkmcnt(0)\n" \
"s_waitcnt vmcnt(0)\n" \
"ds_read_b64_tr_b16 v[0:1], v120 offset:24576\n" \
"ds_read_b64_tr_b16 v[2:3], v120 offset:25088\n" \
"ds_read_b64_tr_b16 v[4:5], v120 offset:26624\n" \
"ds_read_b64_tr_b16 v[6:7], v120 offset:27136\n" \
"ds_read_b64_tr_b16 v[8:9], v121 offset:24576\n" \
"ds_read_b64_tr_b16 v[10:11], v121 offset:25088\n" \
"ds_read_b64_tr_b16 v[12:13], v121 offset:26624\n" \
"ds_read_b64_tr_b16 v[14:15], v121 offset:27136\n" \
"ds_read_b64_tr_b16 v[16:17], v120 offset:28672\n" \
"ds_read_b64_tr_b16 v[18:19], v120 offset:29184\n" \
"ds_read_b64_tr_b16 v[20:21], v120 offset:30720\n" \
"ds_read_b64_tr_b16 v[22:23], v120 offset:31232\n" \
"ds_read_b64_tr_b16 v[24:25], v121 offset:28672\n" \
"ds_read_b64_tr_b16 v[26:27], v121 offset:29184\n" \
"ds_read_b64_tr_b16 v[28:29], v121 offset:30720\n" \
"ds_read_b64_tr_b16 v[30:31], v121 offset:31232\n" \
"s_waitcnt lgkmcnt(0)\n" \
"ds_write_b128 v105, v[0:3] offset:24576\n" \
"ds_write_b128 v105, v[4:7] offset:25600\n" \
"ds_write_b128 v105, v[8:11] offset:26624\n" \
"ds_write_b128 v105, v[12:15] offset:27648\n" \
"ds_write_b128 v105, v[16:19] offset:28672\n" \
"ds_write_b128 v105, v[20:23] offset:29696\n" \
"ds_write_b128 v105, v[24:27] offset:30720\n" \
"ds_write_b128 v105, v[28:31] offset:31744\n" \
"s_waitcnt lgkmcnt(0)\n" \
"v_mov_b32 v120, v105\n" \
"v_mov_b32 v116, v106\n" \
"v_mov_b32 v117, v106\n" \
"v_mov_b32 v118, v106\n" \
"v_mov_b32 v119, v106\n" \
"s_mov_b32 s58, 400000\n" \
"s_add_u32 s95, s48, 0\n" \
"s_min_u32 s95, s95, 63\n" \
"s_and_b32 s95, s95, 60\n" \
"s_add_u32 s95, s95, s94\n" \
"Lqk_kv0_%=:\n" \
"buffer_load_dword v16, off, s[44:47], s95 sc1\n" \
"s_waitcnt vmcnt(0)\n" \
"v_readfirstlane_b32 s59, v16\n" \
"s_cmp_lg_u32 s59, 0\n" \
"s_cbranch_scc1 Lqk_kv0_ok_%=\n" \
"s_sleep 4\n" \
"s_sub_u32 s58, s58, 1\n" \
"s_cmp_lg_u32 s58, 0\n" \
"s_cbranch_scc1 Lqk_kv0_%=\n" \
"s_branch Lqk_abort_%=\n" \
"Lqk_kv0_ok_%=:\n" \
"Lqk_loop_%=:\n" \
"s_mov_b32 m0, s56\n" \
"s_nop 0\n" \
"buffer_load_dwordx4 v124, s[44:47], s53 offen sc1 lds\n" \
"s_add_u32 m0, s56, 1024\n" \
"s_add_u32 s64, s53, 131072\n" \
"buffer_load_dwordx4 v124, s[44:47], s64 offen sc1 lds\n" \
"buffer_load_dwordx4 v[96:99], v124, s[44:47], s54 offen sc1\n" \
"buffer_load_dwordx4 v[100:103], v124, s[44:47], s54 offen offset:1024 sc1\n" \
"v_mov_b32 v22, s48\n" \
"s_waitcnt vmcnt(4)\n" \
"buffer_store_dword v22, off, s[44:47], s57 sc1\n" \
"ds_read_b128 v[32:35], v120 offset:0\n" \
"ds_read_b128 v[36:39], v120 offset:1024\n" \
"ds_read_b128 v[40:43], v120 offset:2048\n" \
"ds_read_b128 v[44:47], v120 offset:3072\n" \
"ds_read_b128 v[48:51], v120 offset:4096\n" \
"ds_read_b128 v[52:55], v120 offset:5120\n" \
"ds_read_b128 v[56:59], v120 offset:6144\n" \
"ds_read_b128 v[60:63], v120 offset:7168\n" \
"ds_read_b128 v[64:67], v120 offset:8192\n" \
"ds_read_b128 v[68:71], v120 offset:9216\n" \
"ds_read_b128 v[72:75], v120 offset:10240\n" \
"ds_read_b128 v[76:79], v120 offset:11264\n" \
"v_mov_b32 v80, 0\n" \
"v_mov_b32 v81, 0\n" \
"v_mov_b32 v82, 0\n" \
"v_mov_b32 v83, 0\n" \
"v_mov_b32 v84, 0\n" \
"v_mov_b32 v85, 0\n" \
"v_mov_b32 v86, 0\n" \
"v_mov_b32 v87, 0\n" \
"v_mov_b32 v88, 0\n" \
"v_mov_b32 v89, 0\n" \
"v_mov_b32 v90, 0\n" \
"v_mov_b32 v91, 0\n" \
"v_mov_b32 v92, 0\n" \
"v_mov_b32 v93, 0\n" \
"v_mov_b32 v94, 0\n" \
"v_mov_b32 v95, 0\n" \
"s_lshr_b32 s64, s48, 4\n" \
"s_lshr_b32 s64, s86, s64\n" \
"s_and_b32 s59, s64, 1\n" \
"s_lshl_b32 s59, s59, 13\n" \
"s_add_u32 s87, s50, s59\n" \
"s_and_b32 s59, s64, 16\n" \
"s_lshl_b32 s59, s59, 9\n" \
"s_add_u32 s96, s51, s59\n" \
"s_mov_b32 s58, 400000\n" \
"s_add_u32 s98, s48, 1\n" \
"s_and_b32 s98, s98, 63\n" \
"v_xor_b32 v104, 4096, v123\n" \
"v_xor_b32 v105, 8192, v123\n" \
"v_xor_b32 v106, 12288, v123\n" \
"s_and_b32 s59, s48, 3\n" \
"s_cmp_lg_u32 s59, 3\n" \
"s_cbranch_scc1 Lqk_nokv_%=\n" \
"s_add_u32 s95, s48, 1\n" \
"s_min_u32 s95, s95, 63\n" \
"s_and_b32 s95, s95, 60\n" \
"s_add_u32 s95, s95, s94\n" \
"Lqk_kv_%=:\n" \
"buffer_load_dword v16, off, s[44:47], s95 sc1\n" \
"s_waitcnt vmcnt(0)\n" \
"v_readfirstlane_b32 s59, v16\n" \
"s_cmp_lg_u32 s59, 0\n" \
"s_cbranch_scc1 Lqk_kv_ok_%=\n" \
"s_sleep 4\n" \
"s_sub_u32 s58, s58, 1\n" \
"s_cmp_lg_u32 s58, 0\n" \
"s_cbranch_scc1 Lqk_kv_%=\n" \
"s_branch Lqk_abort_%=\n" \
"Lqk_kv_ok_%=:\n" \
"Lqk_nokv_%=:\n" \
"s_cmp_eq_u32 s48, 0\n" \
"s_cbranch_scc1 Lqk_first_%=\n" \
"s_mov_b32 s93, s48\n" \
"s_waitcnt lgkmcnt(0)\n" \
"s_lshl_b32 s59, s72, 6\n" \
"s_sub_u32 s59, s59, s89\n" \
"s_sub_u32 s59, s59, s88\n" \
"s_min_i32 s59, s59, 768\n" \
"s_ashr_i32 s59, s59, 2\n" \
"s_add_u32 s88, s88, s59\n" \
"v_lshlrev_b32 v21, 3, v124\n" \
"v_and_b32 v21, 0x1f80, v21\n" \
"buffer_load_dword v20, v21, s[44:47], s101 offen sc1\n" \
"v_bfe_u32 v17, v124, 4, 5\n" \
"v_lshlrev_b32 v17, 7, v17\n" \
"Lqk_hint_%=:\n" \
"buffer_load_dword v16, v17, s[44:47], s92 offen sc1\n" \
"s_waitcnt vmcnt(0)\n" \
"v_subrev_u32 v18, s93, v16\n" \
"v_and_b32 v18, 63, v18\n" \
"v_cmp_lt_u32 vcc, 31, v18\n" \
"s_cbranch_vccz Lqk_hint_ok_%=\n" \
"s_sub_u32 s58, s58, 1\n" \
"s_cmp_lg_u32 s58, 0\n" \
"s_cbranch_scc1 Lqk_hint_%=\n" \
"s_branch Lqk_abort_%=\n" \
"Lqk_hint_ok_%=:\n" \
"v_lshlrev_b32 v18, 20, v18\n" \
"v_sub_u32 v16, v16, v18\n" \
"v_max_u32_dpp v20, v20, v20 row_ror:8 row_mask:0xf bank_mask:0xf\n" \
"s_nop 1\n" \
"v_max_u32_dpp v20, v20, v20 row_ror:4 row_mask:0xf bank_mask:0xf\n" \
"s_nop 1\n" \
"v_max_u32_dpp v20, v20, v20 row_ror:2 row_mask:0xf bank_mask:0xf\n" \
"s_nop 1\n" \
"v_max_u32_dpp v20, v20, v20 row_ror:1 row_mask:0xf bank_mask:0xf\n" \
"v_mov_b32 v21, v20\n" \
"s_nop 1\n" \
"v_permlane16_swap_b32 v20, v21\n" \
"v_max_u32 v20, v20, v21\n" \
"v_mov_b32 v21, v20\n" \
"s_nop 1\n" \
"v_permlane32_swap_b32 v20, v21\n" \
"v_max_u32 v20, v20, v21\n" \
"s_nop 0\n" \
"v_readfirstlane_b32 s95, v20\n" \
"s_add_u32 s95, s95, s68\n" \
"s_nop 0\n" \
"v_add_u32 v17, s95, v16\n" \
"Lqk_dly_%=:\n" \
"s_memrealtime s[74:75]\n" \
"s_waitcnt lgkmcnt(0)\n" \
"s_lshl_b32 s74, s74, 6\n" \
"v_subrev_u32 v18, s74, v17\n" \
"v_cmp_lt_i32 vcc, 0, v18\n" \
"s_cbranch_vccnz Lqk_dly_%=\n" \
"Lqk_first_%=:\n" \
"s_waitcnt vmcnt(0) lgkmcnt(0)\n" \
"buffer_load_dwordx4 v[0:3], v122, s[44:47], s49 offen offset:0 sc1\n" \
"s_nop 11\n" \
"buffer_load_dwordx4 v[4:7], v122, s[44:47], s49 offen offset:1024 sc1\n" \
"s_nop 11\n" \
"buffer_load_dwordx4 v[8:11], v122, s[44:47], s49 offen offset:2048 sc1\n" \
"s_nop 11\n" \
"buffer_load_dwordx4 v[12:15], v122, s[44:47], s49 offen offset:3072 sc1\n" \
"s_waitcnt vmcnt(3)\n" \
"Lqk_pollok_%=:\n" \
"v_smfmac_f32_16x16x64_bf16 v[80:83], v[0:3], a[0:7], v127\n" \
"ds_write_b128 v123, v[0:3]\n" \
"v_smfmac_f32_16x16x64_bf16 v[84:87], v[0:3], a[8:15], v127\n" \
"v_max3_u32 v28, v0, v1, v2\n" \
"v_smfmac_f32_16x16x64_bf16 v[88:91], v[0:3], a[16:23], v127\n" \
"v_smfmac_f32_16x16x64_bf16 v[92:95], v[0:3], a[24:31], v127\n" \
"s_waitcnt vmcnt(2)\n" \
"ds_write_b128 v123, v[4:7] offset:1024\n" \
"v_smfmac_f32_16x16x64_bf16 v[80:83], v[4:7], a[32:39], v127\n" \
"v_max3_u32 v28, v28, v3, v4\n" \
"v_smfmac_f32_16x16x64_bf16 v[84:87], v[4:7], a[40:47], v127\n" \
"v_max3_u32 v28, v28, v5, v6\n" \
"v_max_u32 v28, v28, v7\n" \
"v_smfmac_f32_16x16x64_bf16 v[88:91], v[4:7], a[48:55], v127\n" \
"v_cmp_eq_u32 vcc, -1, v28\n" \
"v_smfmac_f32_16x16x64_bf16 v[92:95], v[4:7], a[56:63], v127\n" \
"s_waitcnt vmcnt(1)\n" \
"ds_write_b128 v123, v[8:11] offset:2048\n" \
"v_smfmac_f32_16x16x64_bf16 v[80:83], v[8:11], a[64:71], v127\n" \
"s_cbranch_vccnz Lqk_redo_%=\n" \
"s_waitcnt lgkmcnt(1)\n" \
"s_barrier\n" \
"ds_read_b128 v[16:19], v104\n" \
"v_smfmac_f32_16x16x64_bf16 v[84:87], v[8:11], a[72:79], v127\n" \
"ds_read_b128 v[20:23], v104 offset:1024\n" \
"v_smfmac_f32_16x16x64_bf16 v[88:91], v[8:11], a[80:87], v127\n" \
"v_max3_u32 v28, v8, v9, v10\n" \
"v_smfmac_f32_16x16x64_bf16 v[92:95], v[8:11], a[88:95], v127\n" \
"s_waitcnt vmcnt(0)\n" \
"ds_write_b128 v123, v[12:15] offset:3072\n" \
"v_smfmac_f32_16x16x64_bf16 v[80:83], v[12:15], v[32:39], v127\n" \
"v_max3_u32 v28, v28, v11, v12\n" \
"v_smfmac_f32_16x16x64_bf16 v[84:87], v[12:15], v[40:47], v127\n" \
"v_max3_u32 v28, v28, v13, v14\n" \
"v_max_u32 v28, v28, v15\n" \
"v_smfmac_f32_16x16x64_bf16 v[88:91], v[12:15], v[48:55], v127\n" \
"v_cmp_eq_u32 vcc, -1, v28\n" \
"v_smfmac_f32_16x16x64_bf16 v[92:95], v[12:15], v[56:63], v127\n" \
"s_waitcnt lgkmcnt(2)\n" \
"s_memrealtime s[74:75]\n" \
"v_smfmac_f32_16x16x64_bf16 v[80:83], v[16:19], a[96:103], v127\n" \
"s_cbranch_vccnz Lqk_redo2_%=\n" \
"Lqk_back2_%=:\n" \
"s_waitcnt lgkmcnt(0)\n" \
"s_barrier\n" \
"ds_read_b128 v[24:27], v105\n" \
"v_smfmac_f32_16x16x64_bf16 v[84:87], v[16:19], a[104:111], v127\n" \
"ds_read_b128 v[28:31], v105 offset:1024\n" \
"s_lshl_b32 s89, s74, 6\n" \
"s_add_u32 s74, s89, s88\n" \
"v_smfmac_f32_16x16x64_bf16 v[88:91], v[16:19], a[112:119], v127\n" \
"s_andn2_b32 s74, s74, 63\n" \
"s_or_b32 s74, s74, s98\n" \
"v_mov_b32 v121, s74\n" \
"v_smfmac_f32_16x16x64_bf16 v[92:95], v[16:19], a[120:127], v127\n" \
"buffer_store_dword v121, off, s[44:47], s97 sc1\n" \
"v_smfmac_f32_16x16x64_bf16 v[80:83], v[20:23], a[128:135], v127\n" \
"ds_read_b128 v[32:35], v120 offset:12288\n" \
"v_smfmac_f32_16x16x64_bf16 v[84:87], v[20:23], a[136:143], v127\n" \
"ds_read_b128 v[36:39], v120 offset:13312\n" \
"v_smfmac_f32_16x16x64_bf16 v[88:91], v[20:23], a[144:151], v127\n" \
"ds_read_b128 v[40:43], v120 offset:14336\n" \
"v_smfmac_f32_16x16x64_bf16 v[92:95], v[20:23], a[152:159], v127\n" \
"ds_read_b128 v[44:47], v120 offset:15360\n" \
"s_waitcnt lgkmcnt(5)\n" \
"v_smfmac_f32_16x16x64_bf16 v[80:83], v[24:27], a[160:167], v127\n" \
"ds_read_b128 v[0:3], v106 offset:0\n" \
"v_smfmac_f32_16x16x64_bf16 v[84:87], v[24:27], a[168:175], v127\n" \
"ds_read_b128 v[4:7], v106 offset:1024\n" \
"v_smfmac_f32_16x16x64_bf16 v[88:91], v[24:27], a[176:183], v127\n" \
"ds_read_b128 v[8:11], v104 offset:2048\n" \
"v_smfmac_f32_16x16x64_bf16 v[92:95], v[24:27], a[184:191], v127\n" \
"ds_read_b128 v[12:15], v104 offset:3072\n" \
"s_waitcnt lgkmcnt(8)\n" \
"v_smfmac_f32_16x16x64_bf16 v[80:83], v[28:31], v[64:71], v127\n" \
"ds_read_b128 v[48:51], v120 offset:16384\n" \
"v_smfmac_f32_16x16x64_bf16 v[84:87], v[28:31], v[72:79], v127\n" \
"ds_read_b128 v[52:55], v120 offset:17408\n" \
"s_waitcnt lgkmcnt(8)\n" \
"v_smfmac_f32_16x16x64_bf16 v[88:91], v[28:31], v[32:39], v127\n" \
"ds_read_b128 v[56:59], v120 offset:18432\n" \
"s_waitcnt lgkmcnt(7)\n" \
"v_smfmac_f32_16x16x64_bf16 v[92:95], v[28:31], v[40:47], v127\n" \
"ds_read_b128 v[60:63], v120 offset:19456\n" \
"s_waitcnt lgkmcnt(7)\n" \
"v_smfmac_f32_16x16x64_bf16 v[80:83], v[0:3], a[192:199], v127\n" \
"ds_read_b128 v[64:67], v120 offset:20480\n" \
"v_smfmac_f32_16x16x64_bf16 v[84:87], v[0:3], a[200:207], v127\n" \
"ds_read_b128 v[68:71], v120 offset:21504\n" \
"v_smfmac_f32_16x16x64_bf16 v[88:91], v[0:3], a[208:215], v127\n" \
"ds_read_b128 v[72:75], v120 offset:22528\n" \
"v_smfmac_f32_16x16x64_bf16 v[92:95], v[0:3], a[216:223], v127\n" \
"ds_read_b128 v[76:79], v120 offset:23552\n" \
"s_waitcnt lgkmcnt(10)\n" \
"v_smfmac_f32_16x16x64_bf16 v[80:83], v[4:7], a[224:231], v127\n" \
"ds_read_b128 v[16:19], v105 offset:2048\n" \
"v_smfmac_f32_16x16x64_bf16 v[84:87], v[4:7], a[232:239], v127\n" \
"ds_read_b128 v[20:23], v105 offset:3072\n" \
"v_smfmac_f32_16x16x64_bf16 v[88:91], v[4:7], a[240:247], v127\n" \
"ds_read_b128 v[24:27], v106 offset:2048\n" \
"v_smfmac_f32_16x16x64_bf16 v[92:95], v[4:7], a[248:255], v127\n" \
"ds_read_b128 v[28:31], v106 offset:3072\n" \
"s_waitcnt lgkmcnt(13)\n" \
"v_smfmac_f32_16x16x64_bf16 v[80:83], v[8:11], v[128:135], v127\n" \
"ds_read_b128 v[32:35], v120 offset:24576\n" \
"v_smfmac_f32_16x16x64_bf16 v[84:87], v[8:11], v[136:143], v127\n" \
"ds_read_b128 v[36:39], v120 offset:25600\n" \
"v_smfmac_f32_16x16x64_bf16 v[88:91], v[8:11], v[144:151], v127\n" \
"ds_read_b128 v[40:43], v120 offset:26624\n" \
"v_smfmac_f32_16x16x64_bf16 v[92:95], v[8:11], v[152:159], v127\n" \
"ds_read_b128 v[44:47], v120 offset:27648\n" \
"s_waitcnt lgkmcnt(15)\n" \
"s_waitcnt lgkmcnt(14)\n" \
"v_smfmac_f32_16x16x64_bf16 v[80:83], v[12:15], v[48:55], v127\n" \
"s_waitcnt lgkmcnt(12)\n" \
"v_smfmac_f32_16x16x64_bf16 v[84:87], v[12:15], v[56:63], v127\n" \
"ds_read_b128 v[48:51], v120 offset:28672\n" \
"s_waitcnt lgkmcnt(11)\n" \
"v_smfmac_f32_16x16x64_bf16 v[88:91], v[12:15], v[64:71], v127\n" \
"ds_read_b128 v[52:55], v120 offset:29696\n" \
"s_waitcnt lgkmcnt(10)\n" \
"v_smfmac_f32_16x16x64_bf16 v[92:95], v[12:15], v[72:79], v127\n" \
"ds_read_b128 v[56:59], v120 offset:30720\n" \
"s_waitcnt lgkmcnt(10)\n" \
"v_smfmac_f32_16x16x64_bf16 v[80:83], v[16:19], v[160:167], v127\n" \
"ds_read_b128 v[60:63], v120 offset:31744\n" \
"v_smfmac_f32_16x16x64_bf16 v[84:87], v[16:19], v[168:175], v127\n" \
"s_waitcnt lgkmcnt(10)\n" \
"v_smfmac_f32_16x16x64_bf16 v[80:83], v[20:23], v[192:199], v127\n" \
"v_smfmac_f32_16x16x64_bf16 v[84:87], v[20:23], v[200:207], v127\n" \
"s_waitcnt lgkmcnt(9)\n" \
"v_smfmac_f32_16x16x64_bf16 v[80:83], v[24:27], v[224:231], v127\n" \
"v_smfmac_f32_16x16x64_bf16 v[84:87], v[24:27], v[232:239], v127\n" \
"s_waitcnt lgkmcnt(8)\n" \
"s_waitcnt lgkmcnt(6)\n" \
"v_smfmac_f32_16x16x64_bf16 v[80:83], v[28:31], v[32:39], v127\n" \
"s_waitcnt lgkmcnt(4)\n" \
"v_smfmac_f32_16x16x64_bf16 v[84:87], v[28:31], v[40:47], v127\n" \
"v_smfmac_f32_16x16x64_bf16 v[88:91], v[16:19], v[176:183], v127\n" \
"v_smfmac_f32_16x16x64_bf16 v[92:95], v[16:19], v[184:191], v127\n" \
"v_smfmac_f32_16x16x64_bf16 v[88:91], v[20:23], v[208:215], v127\n" \
"v_permlane32_swap_b32 v80, v84\n" \
"v_smfmac_f32_16x16x64_bf16 v[92:95], v[20:23], v[216:223], v127\n" \
"v_permlane32_swap_b32 v81, v85\n" \
"v_smfmac_f32_16x16x64_bf16 v[88:91], v[24:27], v[240:247], v127\n" \
"v_permlane32_swap_b32 v82, v86\n" \
"v_smfmac_f32_16x16x64_bf16 v[92:95], v[24:27], v[248:255], v127\n" \
"v_permlane32_swap_b32 v83, v87\n" \
"s_waitcnt lgkmcnt(2)\n" \
"v_smfmac_f32_16x16x64_bf16 v[88:91], v[28:31], v[48:55], v127\n" \
"v_pk_add_f32 v[80:81], v[80:81], v[84:85]\n" \
"s_waitcnt lgkmcnt(0)\n" \
"v_smfmac_f32_16x16x64_bf16 v[92:95], v[28:31], v[56:63], v127\n" \
"v_pk_add_f32 v[82:83], v[82:83], v[86:87]\n" \
"v_cvt_pk_bf16_f32 v84, v80, v81\n" \
"v_cvt_pk_bf16_f32 v85, v82, v83\n" \
"v_add_u32 v12, s56, v124\n" \
"ds_read_b128 v[16:19], v12\n" \
"ds_read_b128 v[20:23], v12 offset:1024\n" \
"v_mfma_f32_16x16x16_bf16 v[0:3], v[84:85], v[108:109], 0\n" \
"v_xor_b32 v13, 2048, v12\n" \
"v_mfma_f32_16x16x16_bf16 v[4:7], v[84:85], v[110:111], 0\n" \
"v_permlane32_swap_b32 v88, v92\n" \
"v_permlane32_swap_b32 v89, v93\n" \
"v_permlane32_swap_b32 v90, v94\n" \
"v_permlane32_swap_b32 v91, v95\n" \
"v_pk_add_f32 v[88:89], v[88:89], v[92:93]\n" \
"v_pk_add_f32 v[90:91], v[90:91], v[94:95]\n" \
"v_cvt_pk_bf16_f32 v92, v88, v89\n" \
"v_cvt_pk_bf16_f32 v93, v90, v91\n" \
"ds_read_b128 v[24:27], v13\n" \
"ds_read_b128 v[28:31], v13 offset:1024\n" \
"v_mfma_f32_16x16x16_bf16 v[0:3], v[92:93], v[112:113], v[0:3]\n" \
"v_xor_b32 v13, 4096, v12\n" \
"v_xor_b32 v14, 6144, v12\n" \
"v_mfma_f32_16x16x16_bf16 v[4:7], v[92:93], v[114:115], v[4:7]\n" \
"ds_read_b128 v[32:35], v13\n" \
"ds_read_b128 v[36:39], v13 offset:1024\n" \
"ds_read_b128 v[40:43], v14\n" \
"ds_read_b128 v[44:47], v14 offset:1024\n" \
"v_add_u32 v12, s67, v124\n" \
"v_xor_b32 v13, 1024, v12\n" \
"v_cvt_pk_bf16_f32 v8, v0, v1\n" \
"v_cvt_pk_bf16_f32 v9, v2, v3\n" \
"v_xor_b32 v14, 2048, v12\n" \
"v_xor_b32 v15, 3072, v12\n" \
"v_cvt_pk_bf16_f32 v10, v4, v5\n" \
"v_cvt_pk_bf16_f32 v11, v6, v7\n" \
"ds_write_b128 v12, v[8:11]\n" \
"s_waitcnt lgkmcnt(7)\n" \
"v_mfma_f32_16x16x32_bf16 v[48:51], v[16:19], v[8:11], 0\n" \
"v_mfma_f32_16x16x32_bf16 v[52:55], v[20:23], v[8:11], 0\n" \
"s_waitcnt lgkmcnt(0)\n" \
"s_barrier\n" \
"ds_read_b128 v[60:63], v13\n" \
"ds_read_b128 v[64:67], v14\n" \
"ds_read_b128 v[68:71], v15\n" \
"v_mfma_f32_16x16x32_bf16 v[56:59], v[8:11], v[8:11], v[116:119]\n" \
"buffer_load_dwordx2 v[104:105], v126, s[44:47], s96 offen sc1\n" \
"buffer_load_dwordx2 v[106:107], v126, s[44:47], s96 offen offset:256 sc1\n" \
"v_mov_b32 v72, 0x3f803f80\n" \
"v_mov_b32 v73, 0x3f803f80\n" \
"v_mov_b32 v74, 0x3f803f80\n" \
"v_mov_b32 v75, 0x3f803f80\n" \
"s_waitcnt lgkmcnt(2)\n" \
"v_mfma_f32_16x16x32_bf16 v[48:51], v[24:27], v[60:63], v[48:51]\n" \
"v_mfma_f32_16x16x32_bf16 v[52:55], v[28:31], v[60:63], v[52:55]\n" \
"v_mfma_f32_16x16x32_bf16 v[56:59], v[60:63], v[60:63], v[56:59]\n" \
"s_waitcnt lgkmcnt(1)\n" \
"v_mfma_f32_16x16x32_bf16 v[48:51], v[32:35], v[64:67], v[48:51]\n" \
"v_mfma_f32_16x16x32_bf16 v[52:55], v[36:39], v[64:67], v[52:55]\n" \
"v_mfma_f32_16x16x32_bf16 v[56:59], v[64:67], v[64:67], v[56:59]\n" \
"s_waitcnt lgkmcnt(0)\n" \
"v_mfma_f32_16x16x32_bf16 v[48:51], v[40:43], v[68:71], v[48:51]\n" \
"v_mfma_f32_16x16x32_bf16 v[52:55], v[44:47], v[68:71], v[52:55]\n" \
"v_mfma_f32_16x16x32_bf16 v[56:59], v[68:71], v[68:71], v[56:59]\n" \
"v_mov_b32 v2, 0\n" \
"v_mov_b32 v3, 0\n" \
"v_max3_f32 v76, v48, v49, v50\n" \
"s_nop 0\n" \
"v_max3_f32 v76, v76, v51, v52\n" \
"v_max3_f32 v76, v76, v53, v54\n" \
"s_nop 1\n" \
"v_max3_f32 v77, v55, v56, v57\n" \
"v_max3_f32 v77, v77, v58, v59\n" \
"v_max_f32 v76, v76, v77\n" \
"v_max_f32 v77, v76, v77\n" \
"s_nop 1\n" \
"v_permlane16_swap_b32 v76, v77\n" \
"v_max_f32 v78, v76, v77\n" \
"v_max_f32 v79, v76, v77\n" \
"s_nop 1\n" \
"v_permlane32_swap_b32 v78, v79\n" \
"v_max_f32 v76, v78, v79\n" \
"v_mul_f32 v76, s90, v76\n" \
"v_pk_fma_f32 v[48:49], v[48:49], s[90:91], v[76:77] op_sel_hi:[1,1,0] neg_lo:[0,0,1] neg_hi:[0,0,1]\n" \
"v_pk_fma_f32 v[50:51], v[50:51], s[90:91], v[76:77] op_sel_hi:[1,1,0] neg_lo:[0,0,1] neg_hi:[0,0,1]\n" \
"v_pk_fma_f32 v[52:53], v[52:53], s[90:91], v[76:77] op_sel_hi:[1,1,0] neg_lo:[0,0,1] neg_hi:[0,0,1]\n" \
"v_pk_fma_f32 v[54:55], v[54:55], s[90:91], v[76:77] op_sel_hi:[1,1,0] neg_lo:[0,0,1] neg_hi:[0,0,1]\n" \
"v_pk_fma_f32 v[56:57], v[56:57], s[90:91], v[76:77] op_sel_hi:[1,1,0] neg_lo:[0,0,1] neg_hi:[0,0,1]\n" \
"v_pk_fma_f32 v[58:59], v[58:59], s[90:91], v[76:77] op_sel_hi:[1,1,0] neg_lo:[0,0,1] neg_hi:[0,0,1]\n" \
"v_exp_f32 v48, v48\n" \
"v_exp_f32 v49, v49\n" \
"v_exp_f32 v50, v50\n" \
"v_exp_f32 v51, v51\n" \
"v_exp_f32 v52, v52\n" \
"v_exp_f32 v53, v53\n" \
"v_exp_f32 v54, v54\n" \
"v_exp_f32 v55, v55\n" \
"v_cvt_pk_bf16_f32 v60, v48, v49\n" \
"v_cvt_pk_bf16_f32 v61, v50, v51\n" \
"v_cvt_pk_bf16_f32 v62, v52, v53\n" \
"v_cvt_pk_bf16_f32 v63, v54, v55\n" \
"v_exp_f32 v56, v56\n" \
"v_exp_f32 v57, v57\n" \
"v_mfma_f32_16x16x32_bf16 v[64:67], v[72:75], v[60:63], 0\n" \
"v_exp_f32 v58, v58\n" \
"v_mfma_f32_16x16x32_bf16 v[4:7], v[96:99], v[60:63], 0\n" \
"v_exp_f32 v59, v59\n" \
"v_mfma_f32_16x16x32_bf16 v[12:15], v[100:103], v[60:63], 0\n" \
"v_cvt_pk_bf16_f32 v0, v56, v57\n" \
"v_cvt_pk_bf16_f32 v1, v58, v59\n" \
"s_waitcnt vmcnt(0)\n" \
"v_max3_u32 v78, v104, v105, v106\n" \
"v_max_u32 v78, v78, v107\n" \
"v_mfma_f32_16x16x32_bf16 v[64:67], v[72:75], v[0:3], v[64:67]\n" \
"v_cmp_eq_u32 vcc, -1, v78\n" \
"s_cbranch_vccnz Lqk_vmretry_%=\n" \
"Lqk_vmok_%=:\n" \
"v_mfma_f32_16x16x32_bf16 v[4:7], v[104:107], v[0:3], v[4:7]\n" \
"v_mfma_f32_16x16x32_bf16 v[12:15], v[106:109], v[0:3], v[12:15]\n" \
"s_mov_b64 exec, s[60:61]\n" \
"v_rcp_f32 v20, v64\n" \
"s_nop 1\n" \
"v_pk_mul_f32 v[4:5], v[4:5], v[20:21] op_sel_hi:[1,0]\n" \
"v_pk_mul_f32 v[6:7], v[6:7], v[20:21] op_sel_hi:[1,0]\n" \
"v_cvt_pk_bf16_f32 v16, v4, v5\n" \
"v_cvt_pk_bf16_f32 v17, v6, v7\n" \
"v_pk_mul_f32 v[12:13], v[12:13], v[20:21] op_sel_hi:[1,0]\n" \
"v_pk_mul_f32 v[14:15], v[14:15], v[20:21] op_sel_hi:[1,0]\n" \
"v_cvt_pk_bf16_f32 v18, v12, v13\n" \
"v_cvt_pk_bf16_f32 v19, v14, v15\n" \
"buffer_store_dwordx4 v[16:19], v125, s[44:47], s87 offen sc1\n" \
"s_memrealtime s[72:73]\n" \
"s_mov_b64 exec, s[62:63]\n" \
"v_lshrrev_b32 v21, 8, v124\n" \
"v_and_b32 v22, 112, v124\n" \
"v_lshl_add_u32 v21, v21, 7, v22\n" \
"buffer_store_dwordx4 v[8:11], v21, s[44:47], s52 offen sc1\n" \
"s_mov_b64 exec, -1\n" \
"s_add_u32 s49, s49, 32768\n" \
"s_add_u32 s99, s99, 32768\n" \
"s_add_u32 s50, s50, 131072\n" \
"s_add_u32 s51, s51, 131072\n" \
"s_add_u32 s52, s52, 65536\n" \
"s_add_u32 s53, s53, 262144\n" \
"s_add_u32 s54, s54, 262144\n" \
"s_add_u32 s48, s48, 1\n" \
"s_cmp_lt_u32 s48, 64\n" \
"s_cbranch_scc1 Lqk_loop_%=\n" \
"v_mov_b32 v22, s48\n" \
"s_waitcnt vmcnt(0)\n" \
"buffer_store_dword v22, off, s[44:47], s57 sc1\n" \
"s_branch Lqk_end_%=\n" \
"Lqk_redo_%=:\n" \
"s_mov_b32 s59, s49\n" \
"s_mov_b32 s49, s99\n" \
"s_mov_b32 s99, s59\n" \
"buffer_load_dwordx4 v[0:3], v122, s[44:47], s49 offen offset:0 sc1\n" \
"buffer_load_dwordx4 v[4:7], v122, s[44:47], s49 offen offset:1024 sc1\n" \
"buffer_load_dwordx4 v[8:11], v122, s[44:47], s49 offen offset:2048 sc1\n" \
"buffer_load_dwordx4 v[12:15], v122, s[44:47], s49 offen offset:3072 sc1\n" \
"s_nop 7\n" \
"v_mov_b32 v80, 0\n" \
"v_mov_b32 v81, 0\n" \
"v_mov_b32 v82, 0\n" \
"v_mov_b32 v83, 0\n" \
"v_mov_b32 v84, 0\n" \
"v_mov_b32 v85, 0\n" \
"v_mov_b32 v86, 0\n" \
"v_mov_b32 v87, 0\n" \
"v_mov_b32 v88, 0\n" \
"v_mov_b32 v89, 0\n" \
"v_mov_b32 v90, 0\n" \
"v_mov_b32 v91, 0\n" \
"v_mov_b32 v92, 0\n" \
"v_mov_b32 v93, 0\n" \
"v_mov_b32 v94, 0\n" \
"v_mov_b32 v95, 0\n" \
"s_waitcnt vmcnt(0) lgkmcnt(0)\n" \
"v_max3_u32 v16, v0, v1, v2\n" \
"v_max3_u32 v17, v8, v9, v10\n" \
"v_max3_u32 v16, v16, v3, v4\n" \
"v_max3_u32 v17, v17, v11, v12\n" \
"v_max3_u32 v16, v16, v5, v6\n" \
"v_max3_u32 v17, v17, v13, v14\n" \
"v_max3_u32 v16, v16, v7, v15\n" \
"v_max_u32 v16, v16, v17\n" \
"v_cmp_eq_u32 vcc, -1, v16\n" \
"s_cbranch_vccz Lqk_pollok_%=\n" \
"s_sub_u32 s58, s58, 1\n" \
"s_cmp_lg_u32 s58, 0\n" \
"s_cbranch_scc1 Lqk_redo_%=\n" \
"s_branch Lqk_abort_%=\n" \
"Lqk_redo2_%=:\n" \
"s_mov_b32 s59, s49\n" \
"s_mov_b32 s49, s99\n" \
"s_mov_b32 s99, s59\n" \
"buffer_load_dwordx4 v[8:11], v122, s[44:47], s49 offen offset:2048 sc1\n" \
"buffer_load_dwordx4 v[12:15], v122, s[44:47], s49 offen offset:3072 sc1\n" \
"s_nop 7\n" \
"v_mov_b32 v80, 0\n" \
"v_mov_b32 v81, 0\n" \
"v_mov_b32 v82, 0\n" \
"v_mov_b32 v83, 0\n" \
"v_mov_b32 v84, 0\n" \
"v_mov_b32 v85, 0\n" \
"v_mov_b32 v86, 0\n" \
"v_mov_b32 v87, 0\n" \
"v_mov_b32 v88, 0\n" \
"v_mov_b32 v89, 0\n" \
"v_mov_b32 v90, 0\n" \
"v_mov_b32 v91, 0\n" \
"v_mov_b32 v92, 0\n" \
"v_mov_b32 v93, 0\n" \
"v_mov_b32 v94, 0\n" \
"v_mov_b32 v95, 0\n" \
"s_waitcnt vmcnt(0) lgkmcnt(0)\n" \
"v_max3_u32 v28, v8, v9, v10\n" \
"v_max3_u32 v28, v28, v11, v12\n" \
"v_max3_u32 v28, v28, v13, v14\n" \
"v_max_u32 v28, v28, v15\n" \
"v_cmp_eq_u32 vcc, -1, v28\n" \
"s_cbranch_vccz Lqk_redo2b_%=\n" \
"s_sub_u32 s58, s58, 1\n" \
"s_cmp_lg_u32 s58, 0\n" \
"s_cbranch_scc1 Lqk_redo2_%=\n" \
"s_branch Lqk_abort_%=\n" \
"Lqk_redo2b_%=:\n" \
"ds_write_b128 v123, v[8:11] offset:2048\n" \
"ds_write_b128 v123, v[12:15] offset:3072\n" \
"v_smfmac_f32_16x16x64_bf16 v[80:83], v[0:3], a[0:7], v127\n" \
"v_smfmac_f32_16x16x64_bf16 v[84:87], v[0:3], a[8:15], v127\n" \
"v_smfmac_f32_16x16x64_bf16 v[88:91], v[0:3], a[16:23], v127\n" \
"v_smfmac_f32_16x16x64_bf16 v[92:95], v[0:3], a[24:31], v127\n" \
"v_smfmac_f32_16x16x64_bf16 v[80:83], v[4:7], a[32:39], v127\n" \
"v_smfmac_f32_16x16x64_bf16 v[84:87], v[4:7], a[40:47], v127\n" \
"v_smfmac_f32_16x16x64_bf16 v[88:91], v[4:7], a[48:55], v127\n" \
"v_smfmac_f32_16x16x64_bf16 v[92:95], v[4:7], a[56:63], v127\n" \
"v_smfmac_f32_16x16x64_bf16 v[80:83], v[8:11], a[64:71], v127\n" \
"v_smfmac_f32_16x16x64_bf16 v[84:87], v[8:11], a[72:79], v127\n" \
"v_smfmac_f32_16x16x64_bf16 v[88:91], v[8:11], a[80:87], v127\n" \
"v_smfmac_f32_16x16x64_bf16 v[92:95], v[8:11], a[88:95], v127\n" \
"v_smfmac_f32_16x16x64_bf16 v[80:83], v[12:15], v[32:39], v127\n" \
"v_smfmac_f32_16x16x64_bf16 v[84:87], v[12:15], v[40:47], v127\n" \
"v_smfmac_f32_16x16x64_bf16 v[88:91], v[12:15], v[48:55], v127\n" \
"v_smfmac_f32_16x16x64_bf16 v[92:95], v[12:15], v[56:63], v127\n" \
"v_smfmac_f32_16x16x64_bf16 v[80:83], v[16:19], a[96:103], v127\n" \
"s_memrealtime s[74:75]\n" \
"s_branch Lqk_back2_%=\n" \
"Lqk_vmretry_%=:\n" \
"buffer_load_dwordx2 v[104:105], v126, s[44:47], s96 offen sc1\n" \
"buffer_load_dwordx2 v[106:107], v126, s[44:47], s96 offen offset:256 sc1\n" \
"s_waitcnt vmcnt(0)\n" \
"v_max3_u32 v78, v104, v105, v106\n" \
"v_max_u32 v78, v78, v107\n" \
"v_cmp_eq_u32 vcc, -1, v78\n" \
"s_cbranch_vccz Lqk_vmok_%=\n" \
"s_sub_u32 s58, s58, 1\n" \
"s_cmp_lg_u32 s58, 0\n" \
"s_cbranch_scc1 Lqk_vmretry_%=\n" \
"s_branch Lqk_abort_%=\n" \
"Lqk_abort_%=:\n" \
"s_mov_b32 s65, 1\n" \
"Lqk_end_%=:\n" \
"s_waitcnt vmcnt(0) lgkmcnt(0)\n"
#define ASM_B \
"s_mov_b32 s65, 0\n" \
"s_add_u32 m0, s55, 0\n" \
"s_xor_b32 s64, s66, 0\n" \
"s_lshl_b32 s64, s64, 2\n" \
"s_add_u32 s64, s64, 0\n" \
"s_lshl_b32 s64, s64, 19\n" \
"buffer_load_dwordx4 v4, s[40:43], s64 offen offset:0 lds\n" \
"buffer_load_dwordx4 v5, s[40:43], s64 offen offset:1024 lds\n" \
"buffer_load_dwordx4 v6, s[40:43], s64 offen offset:2048 lds\n" \
"buffer_load_dwordx4 v7, s[40:43], s64 offen offset:3072 lds\n" \
"s_add_u32 m0, s55, 4096\n" \
"s_xor_b32 s64, s66, 0\n" \
"s_lshl_b32 s64, s64, 2\n" \
"s_add_u32 s64, s64, 2\n" \
"s_lshl_b32 s64, s64, 19\n" \
"buffer_load_dwordx4 v4, s[40:43], s64 offen offset:0 lds\n" \
"buffer_load_dwordx4 v5, s[40:43], s64 offen offset:1024 lds\n" \
"buffer_load_dwordx4 v6, s[40:43], s64 offen offset:2048 lds\n" \
"buffer_load_dwordx4 v7, s[40:43], s64 offen offset:3072 lds\n" \
"s_add_u32 m0, s55, 8192\n" \
"s_xor_b32 s64, s66, 1\n" \
"s_lshl_b32 s64, s64, 2\n" \
"s_add_u32 s64, s64, 0\n" \
"s_lshl_b32 s64, s64, 19\n" \
"buffer_load_dwordx4 v4, s[40:43], s64 offen offset:0 lds\n" \
"buffer_load_dwordx4 v5, s[40:43], s64 offen offset:1024 lds\n" \
"buffer_load_dwordx4 v6, s[40:43], s64 offen offset:2048 lds\n" \
"buffer_load_dwordx4 v7, s[40:43], s64 offen offset:3072 lds\n" \
"s_add_u32 m0, s55, 12288\n" \
"s_xor_b32 s64, s66, 1\n" \
"s_lshl_b32 s64, s64, 2\n" \
"s_add_u32 s64, s64, 2\n" \
"s_lshl_b32 s64, s64, 19\n" \
"buffer_load_dwordx4 v4, s[40:43], s64 offen offset:0 lds\n" \
"buffer_load_dwordx4 v5, s[40:43], s64 offen offset:1024 lds\n" \
"buffer_load_dwordx4 v6, s[40:43], s64 offen offset:2048 lds\n" \
"buffer_load_dwordx4 v7, s[40:43], s64 offen offset:3072 lds\n" \
"s_add_u32 m0, s55, 16384\n" \
"s_xor_b32 s64, s66, 2\n" \
"s_lshl_b32 s64, s64, 2\n" \
"s_add_u32 s64, s64, 0\n" \
"s_lshl_b32 s64, s64, 19\n" \
"buffer_load_dwordx4 v4, s[40:43], s64 offen offset:0 lds\n" \
"buffer_load_dwordx4 v5, s[40:43], s64 offen offset:1024 lds\n" \
"buffer_load_dwordx4 v6, s[40:43], s64 offen offset:2048 lds\n" \
"buffer_load_dwordx4 v7, s[40:43], s64 offen offset:3072 lds\n" \
"s_add_u32 m0, s55, 20480\n" \
"s_xor_b32 s64, s66, 2\n" \
"s_lshl_b32 s64, s64, 2\n" \
"s_add_u32 s64, s64, 2\n" \
"s_lshl_b32 s64, s64, 19\n" \
"buffer_load_dwordx4 v4, s[40:43], s64 offen offset:0 lds\n" \
"buffer_load_dwordx4 v5, s[40:43], s64 offen offset:1024 lds\n" \
"buffer_load_dwordx4 v6, s[40:43], s64 offen offset:2048 lds\n" \
"buffer_load_dwordx4 v7, s[40:43], s64 offen offset:3072 lds\n" \
"s_add_u32 m0, s55, 24576\n" \
"s_xor_b32 s64, s66, 3\n" \
"s_lshl_b32 s64, s64, 2\n" \
"s_add_u32 s64, s64, 0\n" \
"s_lshl_b32 s64, s64, 19\n" \
"buffer_load_dwordx4 v4, s[40:43], s64 offen offset:0 lds\n" \
"buffer_load_dwordx4 v5, s[40:43], s64 offen offset:1024 lds\n" \
"buffer_load_dwordx4 v6, s[40:43], s64 offen offset:2048 lds\n" \
"buffer_load_dwordx4 v7, s[40:43], s64 offen offset:3072 lds\n" \
"s_add_u32 m0, s55, 28672\n" \
"s_xor_b32 s64, s66, 3\n" \
"s_lshl_b32 s64, s64, 2\n" \
"s_add_u32 s64, s64, 2\n" \
"s_lshl_b32 s64, s64, 19\n" \
"buffer_load_dwordx4 v4, s[40:43], s64 offen offset:0 lds\n" \
"buffer_load_dwordx4 v5, s[40:43], s64 offen offset:1024 lds\n" \
"buffer_load_dwordx4 v6, s[40:43], s64 offen offset:2048 lds\n" \
"buffer_load_dwordx4 v7, s[40:43], s64 offen offset:3072 lds\n" \
"s_waitcnt vmcnt(28)\n" \
"ds_read_b64_tr_b16 v[128:129], v10 offset:0\n" \
"ds_read_b64_tr_b16 v[130:131], v10 offset:256\n" \
"ds_read_b64_tr_b16 v[132:133], v10 offset:1024\n" \
"ds_read_b64_tr_b16 v[134:135], v10 offset:1280\n" \
"ds_read_b64_tr_b16 v[136:137], v10 offset:2048\n" \
"ds_read_b64_tr_b16 v[138:139], v10 offset:2304\n" \
"ds_read_b64_tr_b16 v[140:141], v10 offset:3072\n" \
"ds_read_b64_tr_b16 v[142:143], v10 offset:3328\n" \
"s_waitcnt lgkmcnt(0)\n" \
"s_add_u32 m0, s55, 0\n" \
"s_mov_b32 s64, 0\n" \
"buffer_load_dwordx4 v0, s[36:39], s64 offen offset:0 lds\n" \
"buffer_load_dwordx4 v1, s[36:39], s64 offen offset:1024 lds\n" \
"buffer_load_dwordx4 v2, s[36:39], s64 offen offset:2048 lds\n" \
"buffer_load_dwordx4 v3, s[36:39], s64 offen offset:3072 lds\n" \
"s_waitcnt vmcnt(28)\n" \
"ds_read_b64_tr_b16 v[144:145], v10 offset:4096\n" \
"ds_read_b64_tr_b16 v[146:147], v10 offset:4352\n" \
"ds_read_b64_tr_b16 v[148:149], v10 offset:5120\n" \
"ds_read_b64_tr_b16 v[150:151], v10 offset:5376\n" \
"ds_read_b64_tr_b16 v[152:153], v10 offset:6144\n" \
"ds_read_b64_tr_b16 v[154:155], v10 offset:6400\n" \
"ds_read_b64_tr_b16 v[156:157], v10 offset:7168\n" \
"ds_read_b64_tr_b16 v[158:159], v10 offset:7424\n" \
"s_waitcnt lgkmcnt(0)\n" \
"s_add_u32 m0, s55, 4096\n" \
"s_mov_b32 s64, 131072\n" \
"buffer_load_dwordx4 v0, s[36:39], s64 offen offset:0 lds\n" \
"buffer_load_dwordx4 v1, s[36:39], s64 offen offset:1024 lds\n" \
"buffer_load_dwordx4 v2, s[36:39], s64 offen offset:2048 lds\n" \
"buffer_load_dwordx4 v3, s[36:39], s64 offen offset:3072 lds\n" \
"s_waitcnt vmcnt(28)\n" \
"ds_read_b64_tr_b16 v[160:161], v10 offset:8192\n" \
"ds_read_b64_tr_b16 v[162:163], v10 offset:8448\n" \
"ds_read_b64_tr_b16 v[164:165], v10 offset:9216\n" \
"ds_read_b64_tr_b16 v[166:167], v10 offset:9472\n" \
"ds_read_b64_tr_b16 v[168:169], v10 offset:10240\n" \
"ds_read_b64_tr_b16 v[170:171], v10 offset:10496\n" \
"ds_read_b64_tr_b16 v[172:173], v10 offset:11264\n" \
"ds_read_b64_tr_b16 v[174:175], v10 offset:11520\n" \
"s_waitcnt lgkmcnt(0)\n" \
"s_add_u32 m0, s55, 8192\n" \
"s_mov_b32 s64, 262144\n" \
"buffer_load_dwordx4 v0, s[36:39], s64 offen offset:0 lds\n" \
"buffer_load_dwordx4 v1, s[36:39], s64 offen offset:1024 lds\n" \
"buffer_load_dwordx4 v2, s[36:39], s64 offen offset:2048 lds\n" \
"buffer_load_dwordx4 v3, s[36:39], s64 offen offset:3072 lds\n" \
"s_waitcnt vmcnt(28)\n" \
"ds_read_b64_tr_b16 v[176:177], v10 offset:12288\n" \
"ds_read_b64_tr_b16 v[178:179], v10 offset:12544\n" \
"ds_read_b64_tr_b16 v[180:181], v10 offset:13312\n" \
"ds_read_b64_tr_b16 v[182:183], v10 offset:13568\n" \
"ds_read_b64_tr_b16 v[184:185], v10 offset:14336\n" \
"ds_read_b64_tr_b16 v[186:187], v10 offset:14592\n" \
"ds_read_b64_tr_b16 v[188:189], v10 offset:15360\n" \
"ds_read_b64_tr_b16 v[190:191], v10 offset:15616\n" \
"s_waitcnt lgkmcnt(0)\n" \
"s_add_u32 m0, s55, 12288\n" \
"s_mov_b32 s64, 393216\n" \
"buffer_load_dwordx4 v0, s[36:39], s64 offen offset:0 lds\n" \
"buffer_load_dwordx4 v1, s[36:39], s64 offen offset:1024 lds\n" \
"buffer_load_dwordx4 v2, s[36:39], s64 offen offset:2048 lds\n" \
"buffer_load_dwordx4 v3, s[36:39], s64 offen offset:3072 lds\n" \
"s_waitcnt vmcnt(28)\n" \
"ds_read_b64_tr_b16 v[192:193], v10 offset:16384\n" \
"ds_read_b64_tr_b16 v[194:195], v10 offset:16640\n" \
"ds_read_b64_tr_b16 v[196:197], v10 offset:17408\n" \
"ds_read_b64_tr_b16 v[198:199], v10 offset:17664\n" \
"ds_read_b64_tr_b16 v[200:201], v10 offset:18432\n" \
"ds_read_b64_tr_b16 v[202:203], v10 offset:18688\n" \
"ds_read_b64_tr_b16 v[204:205], v10 offset:19456\n" \
"ds_read_b64_tr_b16 v[206:207], v10 offset:19712\n" \
"s_waitcnt lgkmcnt(0)\n" \
"s_add_u32 m0, s55, 16384\n" \
"s_mov_b32 s64, 524288\n" \
"buffer_load_dwordx4 v0, s[36:39], s64 offen offset:0 lds\n" \
"buffer_load_dwordx4 v1, s[36:39], s64 offen offset:1024 lds\n" \
"buffer_load_dwordx4 v2, s[36:39], s64 offen offset:2048 lds\n" \
"buffer_load_dwordx4 v3, s[36:39], s64 offen offset:3072 lds\n" \
"s_waitcnt vmcnt(28)\n" \
"ds_read_b64_tr_b16 v[208:209], v10 offset:20480\n" \
"ds_read_b64_tr_b16 v[210:211], v10 offset:20736\n" \
"ds_read_b64_tr_b16 v[212:213], v10 offset:21504\n" \
"ds_read_b64_tr_b16 v[214:215], v10 offset:21760\n" \
"ds_read_b64_tr_b16 v[216:217], v10 offset:22528\n" \
"ds_read_b64_tr_b16 v[218:219], v10 offset:22784\n" \
"ds_read_b64_tr_b16 v[220:221], v10 offset:23552\n" \
"ds_read_b64_tr_b16 v[222:223], v10 offset:23808\n" \
"s_waitcnt lgkmcnt(0)\n" \
"s_add_u32 m0, s55, 20480\n" \
"s_mov_b32 s64, 655360\n" \
"buffer_load_dwordx4 v0, s[36:39], s64 offen offset:0 lds\n" \
"buffer_load_dwordx4 v1, s[36:39], s64 offen offset:1024 lds\n" \
"buffer_load_dwordx4 v2, s[36:39], s64 offen offset:2048 lds\n" \
"buffer_load_dwordx4 v3, s[36:39], s64 offen offset:3072 lds\n" \
"s_waitcnt vmcnt(28)\n" \
"ds_read_b64_tr_b16 v[224:225], v10 offset:24576\n" \
"ds_read_b64_tr_b16 v[226:227], v10 offset:24832\n" \
"ds_read_b64_tr_b16 v[228:229], v10 offset:25600\n" \
"ds_read_b64_tr_b16 v[230:231], v10 offset:25856\n" \
"ds_read_b64_tr_b16 v[232:233], v10 offset:26624\n" \
"ds_read_b64_tr_b16 v[234:235], v10 offset:26880\n" \
"ds_read_b64_tr_b16 v[236:237], v10 offset:27648\n" \
"ds_read_b64_tr_b16 v[238:239], v10 offset:27904\n" \
"s_waitcnt lgkmcnt(0)\n" \
"s_add_u32 m0, s55, 24576\n" \
"s_mov_b32 s64, 786432\n" \
"buffer_load_dwordx4 v0, s[36:39], s64 offen offset:0 lds\n" \
"buffer_load_dwordx4 v1, s[36:39], s64 offen offset:1024 lds\n" \
"buffer_load_dwordx4 v2, s[36:39], s64 offen offset:2048 lds\n" \
"buffer_load_dwordx4 v3, s[36:39], s64 offen offset:3072 lds\n" \
"s_waitcnt vmcnt(28)\n" \
"ds_read_b64_tr_b16 v[240:241], v10 offset:28672\n" \
"ds_read_b64_tr_b16 v[242:243], v10 offset:28928\n" \
"ds_read_b64_tr_b16 v[244:245], v10 offset:29696\n" \
"ds_read_b64_tr_b16 v[246:247], v10 offset:29952\n" \
"ds_read_b64_tr_b16 v[248:249], v10 offset:30720\n" \
"ds_read_b64_tr_b16 v[250:251], v10 offset:30976\n" \
"ds_read_b64_tr_b16 v[252:253], v10 offset:31744\n" \
"ds_read_b64_tr_b16 v[254:255], v10 offset:32000\n" \
"s_waitcnt lgkmcnt(0)\n" \
"s_add_u32 m0, s55, 28672\n" \
"s_mov_b32 s64, 917504\n" \
"buffer_load_dwordx4 v0, s[36:39], s64 offen offset:0 lds\n" \
"buffer_load_dwordx4 v1, s[36:39], s64 offen offset:1024 lds\n" \
"buffer_load_dwordx4 v2, s[36:39], s64 offen offset:2048 lds\n" \
"buffer_load_dwordx4 v3, s[36:39], s64 offen offset:3072 lds\n" \
"s_waitcnt vmcnt(28)\n" \
"ds_read_b64_tr_b16 a[0:1], v8 offset:0\n" \
"ds_read_b64_tr_b16 a[2:3], v8 offset:512\n" \
"ds_read_b64_tr_b16 a[4:5], v8 offset:2048\n" \
"ds_read_b64_tr_b16 a[6:7], v8 offset:2560\n" \
"ds_read_b64_tr_b16 a[8:9], v9 offset:0\n" \
"ds_read_b64_tr_b16 a[10:11], v9 offset:512\n" \
"ds_read_b64_tr_b16 a[12:13], v9 offset:2048\n" \
"ds_read_b64_tr_b16 a[14:15], v9 offset:2560\n" \
"s_waitcnt lgkmcnt(0)\n" \
"s_add_u32 m0, s55, 0\n" \
"s_mov_b32 s64, 1048576\n" \
"buffer_load_dwordx4 v0, s[36:39], s64 offen offset:0 lds\n" \
"buffer_load_dwordx4 v1, s[36:39], s64 offen offset:1024 lds\n" \
"buffer_load_dwordx4 v2, s[36:39], s64 offen offset:2048 lds\n" \
"buffer_load_dwordx4 v3, s[36:39], s64 offen offset:3072 lds\n" \
"s_waitcnt vmcnt(28)\n" \
"ds_read_b64_tr_b16 a[16:17], v8 offset:4096\n" \
"ds_read_b64_tr_b16 a[18:19], v8 offset:4608\n" \
"ds_read_b64_tr_b16 a[20:21], v8 offset:6144\n" \
"ds_read_b64_tr_b16 a[22:23], v8 offset:6656\n" \
"ds_read_b64_tr_b16 a[24:25], v9 offset:4096\n" \
"ds_read_b64_tr_b16 a[26:27], v9 offset:4608\n" \
"ds_read_b64_tr_b16 a[28:29], v9 offset:6144\n" \
"ds_read_b64_tr_b16 a[30:31], v9 offset:6656\n" \
"s_waitcnt lgkmcnt(0)\n" \
"s_add_u32 m0, s55, 4096\n" \
"s_mov_b32 s64, 1179648\n" \
"buffer_load_dwordx4 v0, s[36:39], s64 offen offset:0 lds\n" \
"buffer_load_dwordx4 v1, s[36:39], s64 offen offset:1024 lds\n" \
"buffer_load_dwordx4 v2, s[36:39], s64 offen offset:2048 lds\n" \
"buffer_load_dwordx4 v3, s[36:39], s64 offen offset:3072 lds\n" \
"s_waitcnt vmcnt(28)\n" \
"ds_read_b64_tr_b16 a[32:33], v8 offset:8192\n" \
"ds_read_b64_tr_b16 a[34:35], v8 offset:8704\n" \
"ds_read_b64_tr_b16 a[36:37], v8 offset:10240\n" \
"ds_read_b64_tr_b16 a[38:39], v8 offset:10752\n" \
"ds_read_b64_tr_b16 a[40:41], v9 offset:8192\n" \
"ds_read_b64_tr_b16 a[42:43], v9 offset:8704\n" \
"ds_read_b64_tr_b16 a[44:45], v9 offset:10240\n" \
"ds_read_b64_tr_b16 a[46:47], v9 offset:10752\n" \
"s_waitcnt lgkmcnt(0)\n" \
"s_add_u32 m0, s55, 8192\n" \
"s_mov_b32 s64, 1310720\n" \
"buffer_load_dwordx4 v0, s[36:39], s64 offen offset:0 lds\n" \
"buffer_load_dwordx4 v1, s[36:39], s64 offen offset:1024 lds\n" \
"buffer_load_dwordx4 v2, s[36:39], s64 offen offset:2048 lds\n" \
"buffer_load_dwordx4 v3, s[36:39], s64 offen offset:3072 lds\n" \
"s_waitcnt vmcnt(28)\n" \
"ds_read_b64_tr_b16 a[48:49], v8 offset:12288\n" \
"ds_read_b64_tr_b16 a[50:51], v8 offset:12800\n" \
"ds_read_b64_tr_b16 a[52:53], v8 offset:14336\n" \
"ds_read_b64_tr_b16 a[54:55], v8 offset:14848\n" \
"ds_read_b64_tr_b16 a[56:57], v9 offset:12288\n" \
"ds_read_b64_tr_b16 a[58:59], v9 offset:12800\n" \
"ds_read_b64_tr_b16 a[60:61], v9 offset:14336\n" \
"ds_read_b64_tr_b16 a[62:63], v9 offset:14848\n" \
"s_waitcnt lgkmcnt(0)\n" \
"s_add_u32 m0, s55, 12288\n" \
"s_mov_b32 s64, 1441792\n" \
"buffer_load_dwordx4 v0, s[36:39], s64 offen offset:0 lds\n" \
"buffer_load_dwordx4 v1, s[36:39], s64 offen offset:1024 lds\n" \
"buffer_load_dwordx4 v2, s[36:39], s64 offen offset:2048 lds\n" \
"buffer_load_dwordx4 v3, s[36:39], s64 offen offset:3072 lds\n" \
"s_waitcnt vmcnt(28)\n" \
"ds_read_b64_tr_b16 a[64:65], v8 offset:16384\n" \
"ds_read_b64_tr_b16 a[66:67], v8 offset:16896\n" \
"ds_read_b64_tr_b16 a[68:69], v8 offset:18432\n" \
"ds_read_b64_tr_b16 a[70:71], v8 offset:18944\n" \
"ds_read_b64_tr_b16 a[72:73], v9 offset:16384\n" \
"ds_read_b64_tr_b16 a[74:75], v9 offset:16896\n" \
"ds_read_b64_tr_b16 a[76:77], v9 offset:18432\n" \
"ds_read_b64_tr_b16 a[78:79], v9 offset:18944\n" \
"s_waitcnt lgkmcnt(0)\n" \
"s_add_u32 m0, s55, 16384\n" \
"s_mov_b32 s64, 1572864\n" \
"buffer_load_dwordx4 v0, s[36:39], s64 offen offset:0 lds\n" \
"buffer_load_dwordx4 v1, s[36:39], s64 offen offset:1024 lds\n" \
"buffer_load_dwordx4 v2, s[36:39], s64 offen offset:2048 lds\n" \
"buffer_load_dwordx4 v3, s[36:39], s64 offen offset:3072 lds\n" \
"s_waitcnt vmcnt(28)\n" \
"ds_read_b64_tr_b16 a[80:81], v8 offset:20480\n" \
"ds_read_b64_tr_b16 a[82:83], v8 offset:20992\n" \
"ds_read_b64_tr_b16 a[84:85], v8 offset:22528\n" \
"ds_read_b64_tr_b16 a[86:87], v8 offset:23040\n" \
"ds_read_b64_tr_b16 a[88:89], v9 offset:20480\n" \
"ds_read_b64_tr_b16 a[90:91], v9 offset:20992\n" \
"ds_read_b64_tr_b16 a[92:93], v9 offset:22528\n" \
"ds_read_b64_tr_b16 a[94:95], v9 offset:23040\n" \
"s_waitcnt lgkmcnt(0)\n" \
"s_add_u32 m0, s55, 20480\n" \
"s_mov_b32 s64, 1703936\n" \
"buffer_load_dwordx4 v0, s[36:39], s64 offen offset:0 lds\n" \
"buffer_load_dwordx4 v1, s[36:39], s64 offen offset:1024 lds\n" \
"buffer_load_dwordx4 v2, s[36:39], s64 offen offset:2048 lds\n" \
"buffer_load_dwordx4 v3, s[36:39], s64 offen offset:3072 lds\n" \
"s_waitcnt vmcnt(28)\n" \
"ds_read_b64_tr_b16 a[96:97], v8 offset:24576\n" \
"ds_read_b64_tr_b16 a[98:99], v8 offset:25088\n" \
"ds_read_b64_tr_b16 a[100:101], v8 offset:26624\n" \
"ds_read_b64_tr_b16 a[102:103], v8 offset:27136\n" \
"ds_read_b64_tr_b16 a[104:105], v9 offset:24576\n" \
"ds_read_b64_tr_b16 a[106:107], v9 offset:25088\n" \
"ds_read_b64_tr_b16 a[108:109], v9 offset:26624\n" \
"ds_read_b64_tr_b16 a[110:111], v9 offset:27136\n" \
"s_waitcnt lgkmcnt(0)\n" \
"s_add_u32 m0, s55, 24576\n" \
"s_mov_b32 s64, 1835008\n" \
"buffer_load_dwordx4 v0, s[36:39], s64 offen offset:0 lds\n" \
"buffer_load_dwordx4 v1, s[36:39], s64 offen offset:1024 lds\n" \
"buffer_load_dwordx4 v2, s[36:39], s64 offen offset:2048 lds\n" \
"buffer_load_dwordx4 v3, s[36:39], s64 offen offset:3072 lds\n" \
"s_waitcnt vmcnt(28)\n" \
"ds_read_b64_tr_b16 a[112:113], v8 offset:28672\n" \
"ds_read_b64_tr_b16 a[114:115], v8 offset:29184\n" \
"ds_read_b64_tr_b16 a[116:117], v8 offset:30720\n" \
"ds_read_b64_tr_b16 a[118:119], v8 offset:31232\n" \
"ds_read_b64_tr_b16 a[120:121], v9 offset:28672\n" \
"ds_read_b64_tr_b16 a[122:123], v9 offset:29184\n" \
"ds_read_b64_tr_b16 a[124:125], v9 offset:30720\n" \
"ds_read_b64_tr_b16 a[126:127], v9 offset:31232\n" \
"s_waitcnt lgkmcnt(0)\n" \
"s_add_u32 m0, s55, 28672\n" \
"s_mov_b32 s64, 1966080\n" \
"buffer_load_dwordx4 v0, s[36:39], s64 offen offset:0 lds\n" \
"buffer_load_dwordx4 v1, s[36:39], s64 offen offset:1024 lds\n" \
"buffer_load_dwordx4 v2, s[36:39], s64 offen offset:2048 lds\n" \
"buffer_load_dwordx4 v3, s[36:39], s64 offen offset:3072 lds\n" \
"s_waitcnt vmcnt(28)\n" \
"ds_read_b64_tr_b16 a[128:129], v8 offset:0\n" \
"ds_read_b64_tr_b16 a[130:131], v8 offset:512\n" \
"ds_read_b64_tr_b16 a[132:133], v8 offset:2048\n" \
"ds_read_b64_tr_b16 a[134:135], v8 offset:2560\n" \
"ds_read_b64_tr_b16 a[136:137], v9 offset:0\n" \
"ds_read_b64_tr_b16 a[138:139], v9 offset:512\n" \
"ds_read_b64_tr_b16 a[140:141], v9 offset:2048\n" \
"ds_read_b64_tr_b16 a[142:143], v9 offset:2560\n" \
"s_waitcnt lgkmcnt(0)\n" \
"s_waitcnt vmcnt(24)\n" \
"ds_read_b64_tr_b16 a[144:145], v8 offset:4096\n" \
"ds_read_b64_tr_b16 a[146:147], v8 offset:4608\n" \
"ds_read_b64_tr_b16 a[148:149], v8 offset:6144\n" \
"ds_read_b64_tr_b16 a[150:151], v8 offset:6656\n" \
"ds_read_b64_tr_b16 a[152:153], v9 offset:4096\n" \
"ds_read_b64_tr_b16 a[154:155], v9 offset:4608\n" \
"ds_read_b64_tr_b16 a[156:157], v9 offset:6144\n" \
"ds_read_b64_tr_b16 a[158:159], v9 offset:6656\n" \
"s_waitcnt lgkmcnt(0)\n" \
"s_waitcnt vmcnt(20)\n" \
"ds_read_b64_tr_b16 a[160:161], v8 offset:8192\n" \
"ds_read_b64_tr_b16 a[162:163], v8 offset:8704\n" \
"ds_read_b64_tr_b16 a[164:165], v8 offset:10240\n" \
"ds_read_b64_tr_b16 a[166:167], v8 offset:10752\n" \
"ds_read_b64_tr_b16 a[168:169], v9 offset:8192\n" \
"ds_read_b64_tr_b16 a[170:171], v9 offset:8704\n" \
"ds_read_b64_tr_b16 a[172:173], v9 offset:10240\n" \
"ds_read_b64_tr_b16 a[174:175], v9 offset:10752\n" \
"s_waitcnt lgkmcnt(0)\n" \
"s_waitcnt vmcnt(16)\n" \
"ds_read_b64_tr_b16 a[176:177], v8 offset:12288\n" \
"ds_read_b64_tr_b16 a[178:179], v8 offset:12800\n" \
"ds_read_b64_tr_b16 a[180:181], v8 offset:14336\n" \
"ds_read_b64_tr_b16 a[182:183], v8 offset:14848\n" \
"ds_read_b64_tr_b16 a[184:185], v9 offset:12288\n" \
"ds_read_b64_tr_b16 a[186:187], v9 offset:12800\n" \
"ds_read_b64_tr_b16 a[188:189], v9 offset:14336\n" \
"ds_read_b64_tr_b16 a[190:191], v9 offset:14848\n" \
"s_waitcnt lgkmcnt(0)\n" \
"s_waitcnt vmcnt(12)\n" \
"ds_read_b64_tr_b16 a[192:193], v8 offset:16384\n" \
"ds_read_b64_tr_b16 a[194:195], v8 offset:16896\n" \
"ds_read_b64_tr_b16 a[196:197], v8 offset:18432\n" \
"ds_read_b64_tr_b16 a[198:199], v8 offset:18944\n" \
"ds_read_b64_tr_b16 a[200:201], v9 offset:16384\n" \
"ds_read_b64_tr_b16 a[202:203], v9 offset:16896\n" \
"ds_read_b64_tr_b16 a[204:205], v9 offset:18432\n" \
"ds_read_b64_tr_b16 a[206:207], v9 offset:18944\n" \
"s_waitcnt lgkmcnt(0)\n" \
"s_waitcnt vmcnt(8)\n" \
"ds_read_b64_tr_b16 a[208:209], v8 offset:20480\n" \
"ds_read_b64_tr_b16 a[210:211], v8 offset:20992\n" \
"ds_read_b64_tr_b16 a[212:213], v8 offset:22528\n" \
"ds_read_b64_tr_b16 a[214:215], v8 offset:23040\n" \
"ds_read_b64_tr_b16 a[216:217], v9 offset:20480\n" \
"ds_read_b64_tr_b16 a[218:219], v9 offset:20992\n" \
"ds_read_b64_tr_b16 a[220:221], v9 offset:22528\n" \
"ds_read_b64_tr_b16 a[222:223], v9 offset:23040\n" \
"s_waitcnt lgkmcnt(0)\n" \
"s_waitcnt vmcnt(4)\n" \
"ds_read_b64_tr_b16 a[224:225], v8 offset:24576\n" \
"ds_read_b64_tr_b16 a[226:227], v8 offset:25088\n" \
"ds_read_b64_tr_b16 a[228:229], v8 offset:26624\n" \
"ds_read_b64_tr_b16 a[230:231], v8 offset:27136\n" \
"ds_read_b64_tr_b16 a[232:233], v9 offset:24576\n" \
"ds_read_b64_tr_b16 a[234:235], v9 offset:25088\n" \
"ds_read_b64_tr_b16 a[236:237], v9 offset:26624\n" \
"ds_read_b64_tr_b16 a[238:239], v9 offset:27136\n" \
"s_waitcnt lgkmcnt(0)\n" \
"s_waitcnt vmcnt(0)\n" \
"ds_read_b64_tr_b16 a[240:241], v8 offset:28672\n" \
"ds_read_b64_tr_b16 a[242:243], v8 offset:29184\n" \
"ds_read_b64_tr_b16 a[244:245], v8 offset:30720\n" \
"ds_read_b64_tr_b16 a[246:247], v8 offset:31232\n" \
"ds_read_b64_tr_b16 a[248:249], v9 offset:28672\n" \
"ds_read_b64_tr_b16 a[250:251], v9 offset:29184\n" \
"ds_read_b64_tr_b16 a[252:253], v9 offset:30720\n" \
"ds_read_b64_tr_b16 a[254:255], v9 offset:31232\n" \
"s_waitcnt lgkmcnt(0)\n" \
"v_xor_b32 v125, 4096, v119\n" \
"v_xor_b32 v126, 8192, v119\n" \
"v_xor_b32 v127, 12288, v119\n" \
"Lb_loop_%=:\n" \
"v_mov_b32 v96, 0\n" \
"v_mov_b32 v97, 0\n" \
"v_mov_b32 v98, 0\n" \
"v_mov_b32 v99, 0\n" \
"v_mov_b32 v100, 0\n" \
"v_mov_b32 v101, 0\n" \
"v_mov_b32 v102, 0\n" \
"v_mov_b32 v103, 0\n" \
"v_mov_b32 v104, 0\n" \
"v_mov_b32 v105, 0\n" \
"v_mov_b32 v106, 0\n" \
"v_mov_b32 v107, 0\n" \
"v_mov_b32 v108, 0\n" \
"v_mov_b32 v109, 0\n" \
"v_mov_b32 v110, 0\n" \
"v_mov_b32 v111, 0\n" \
"v_mov_b32 v112, 0\n" \
"v_mov_b32 v113, 0\n" \
"v_mov_b32 v114, 0\n" \
"v_mov_b32 v115, 0\n" \
"s_mov_b32 s58, 400000\n" \
"s_lshr_b32 s64, s48, 4\n" \
"s_lshr_b32 s64, s86, s64\n" \
"s_and_b32 s59, s64, 1\n" \
"s_lshl_b32 s59, s59, 13\n" \
"s_add_u32 s87, s50, s59\n" \
"s_and_b32 s59, s64, 16\n" \
"s_lshl_b32 s59, s59, 9\n" \
"s_add_u32 s96, s51, s59\n" \
"s_add_u32 s53, s87, 4096\n" \
"s_add_u32 s54, s87, 16384\n" \
"s_add_u32 s56, s87, 20480\n" \
"s_add_u32 s93, s48, 1\n" \
"s_cmp_eq_u32 s48, 0\n" \
"s_cbranch_scc1 Lb_first_%=\n" \
"v_add_u32 v16, s94, v95\n" \
"Lb_dly1_%=:\n" \
"s_memrealtime s[74:75]\n" \
"s_waitcnt lgkmcnt(0)\n" \
"s_lshl_b32 s74, s74, 6\n" \
"v_subrev_u32 v17, s74, v16\n" \
"v_cmp_lt_i32 vcc, 0, v17\n" \
"s_cbranch_vccnz Lb_dly1_%=\n" \
"Lb_first_%=:\n" \
"buffer_load_dwordx4 v[0:3], v118, s[44:47], s49 offen offset:0 sc1\n" \
"s_nop 11\n" \
"buffer_load_dwordx4 v[4:7], v118, s[44:47], s49 offen offset:1024 sc1\n" \
"s_nop 11\n" \
"buffer_load_dwordx4 v[8:11], v118, s[44:47], s49 offen offset:2048 sc1\n" \
"s_nop 11\n" \
"buffer_load_dwordx4 v[12:15], v118, s[44:47], s49 offen offset:3072 sc1\n" \
"s_waitcnt vmcnt(3)\n" \
"Lb_pollok_%=:\n" \
"ds_write_b128 v119, v[0:3] offset:0\n" \
"v_smfmac_f32_16x16x64_bf16 v[112:115], v[0:3], v[128:135], v116\n" \
"v_max3_u32 v16, v0, v1, v0\n" \
"v_max3_u32 v16, v2, v3, v16\n" \
"s_waitcnt vmcnt(2)\n" \
"ds_write_b128 v119, v[4:7] offset:1024\n" \
"v_smfmac_f32_16x16x64_bf16 v[112:115], v[4:7], v[136:143], v116\n" \
"v_max3_u32 v16, v4, v5, v16\n" \
"v_max3_u32 v16, v6, v7, v16\n" \
"s_waitcnt vmcnt(1)\n" \
"ds_write_b128 v119, v[8:11] offset:2048\n" \
"v_smfmac_f32_16x16x64_bf16 v[112:115], v[8:11], v[144:151], v116\n" \
"v_max3_u32 v16, v8, v9, v16\n" \
"v_max3_u32 v16, v10, v11, v16\n" \
"s_waitcnt vmcnt(0)\n" \
"ds_write_b128 v119, v[12:15] offset:3072\n" \
"v_smfmac_f32_16x16x64_bf16 v[112:115], v[12:15], v[152:159], v116\n" \
"v_max3_u32 v16, v12, v13, v16\n" \
"v_max3_u32 v16, v14, v15, v16\n" \
"v_cmp_eq_u32 vcc, -1, v16\n" \
"s_waitcnt lgkmcnt(0)\n" \
"s_cbranch_vccnz Lb_poll_%=\n" \
"s_barrier\n" \
"ds_read_b128 v[16:19], v125 offset:0\n" \
"ds_read_b128 v[20:23], v125 offset:1024\n" \
"ds_read_b128 v[24:27], v125 offset:2048\n" \
"ds_read_b128 v[28:31], v125 offset:3072\n" \
"ds_read_b128 v[32:35], v126 offset:0\n" \
"ds_read_b128 v[36:39], v126 offset:1024\n" \
"ds_read_b128 v[40:43], v126 offset:2048\n" \
"ds_read_b128 v[44:47], v126 offset:3072\n" \
"ds_read_b128 v[48:51], v127 offset:0\n" \
"ds_read_b128 v[52:55], v127 offset:1024\n" \
"ds_read_b128 v[56:59], v127 offset:2048\n" \
"ds_read_b128 v[60:63], v127 offset:3072\n" \
"s_waitcnt lgkmcnt(11)\n" \
"v_smfmac_f32_16x16x64_bf16 v[112:115], v[16:19], v[160:167], v116\n" \
"s_waitcnt lgkmcnt(10)\n" \
"v_smfmac_f32_16x16x64_bf16 v[112:115], v[20:23], v[168:175], v116\n" \
"s_waitcnt lgkmcnt(9)\n" \
"v_smfmac_f32_16x16x64_bf16 v[112:115], v[24:27], v[176:183], v116\n" \
"s_waitcnt lgkmcnt(8)\n" \
"v_smfmac_f32_16x16x64_bf16 v[112:115], v[28:31], v[184:191], v116\n" \
"s_waitcnt lgkmcnt(7)\n" \
"v_smfmac_f32_16x16x64_bf16 v[112:115], v[32:35], v[192:199], v116\n" \
"s_waitcnt lgkmcnt(6)\n" \
"v_smfmac_f32_16x16x64_bf16 v[112:115], v[36:39], v[200:207], v116\n" \
"s_waitcnt lgkmcnt(5)\n" \
"v_smfmac_f32_16x16x64_bf16 v[112:115], v[40:43], v[208:215], v116\n" \
"s_waitcnt lgkmcnt(4)\n" \
"v_smfmac_f32_16x16x64_bf16 v[112:115], v[44:47], v[216:223], v116\n" \
"s_waitcnt lgkmcnt(3)\n" \
"v_smfmac_f32_16x16x64_bf16 v[112:115], v[48:51], v[224:231], v116\n" \
"s_waitcnt lgkmcnt(2)\n" \
"v_smfmac_f32_16x16x64_bf16 v[112:115], v[52:55], v[232:239], v116\n" \
"s_waitcnt lgkmcnt(1)\n" \
"v_smfmac_f32_16x16x64_bf16 v[112:115], v[56:59], v[240:247], v116\n" \
"s_waitcnt lgkmcnt(0)\n" \
"v_smfmac_f32_16x16x64_bf16 v[112:115], v[60:63], v[248:255], v116\n" \
"s_nop 7\n" \
"v_permlane32_swap_b32 v112, v114\n" \
"v_permlane32_swap_b32 v113, v115\n" \
"v_pk_add_f32 v[112:113], v[112:113], v[114:115]\n" \
"v_cvt_pk_bf16_f32 v112, v112, v113\n" \
"buffer_store_dword v112, v121, s[44:47], s96 offen sc1\n" \
"s_cmp_eq_u32 s48, 63\n" \
"s_cbranch_scc1 Lb_end_%=\n" \
"v_lshlrev_b32 v25, 3, v119\n" \
"v_and_b32 v25, 0x1f80, v25\n" \
"buffer_load_dword v24, v25, s[44:47], s101 offen sc1\n" \
"v_bfe_u32 v21, v119, 4, 5\n" \
"v_lshlrev_b32 v21, 7, v21\n" \
"Lb_hint2_%=:\n" \
"buffer_load_dword v20, v21, s[44:47], s92 offen sc1\n" \
"s_waitcnt vmcnt(0)\n" \
"v_subrev_u32 v22, s93, v20\n" \
"v_and_b32 v22, 63, v22\n" \
"v_cmp_lt_u32 vcc, 31, v22\n" \
"s_cbranch_vccz Lb_hint2_ok_%=\n" \
"s_sub_u32 s58, s58, 1\n" \
"s_cmp_lg_u32 s58, 0\n" \
"s_cbranch_scc1 Lb_hint2_%=\n" \
"s_branch Lb_abort_%=\n" \
"Lb_hint2_ok_%=:\n" \
"v_lshlrev_b32 v22, 20, v22\n" \
"v_sub_u32 v20, v20, v22\n" \
"v_max_u32_dpp v24, v24, v24 row_ror:8 row_mask:0xf bank_mask:0xf\n" \
"s_nop 1\n" \
"v_max_u32_dpp v24, v24, v24 row_ror:4 row_mask:0xf bank_mask:0xf\n" \
"s_nop 1\n" \
"v_max_u32_dpp v24, v24, v24 row_ror:2 row_mask:0xf bank_mask:0xf\n" \
"s_nop 1\n" \
"v_max_u32_dpp v24, v24, v24 row_ror:1 row_mask:0xf bank_mask:0xf\n" \
"v_mov_b32 v25, v24\n" \
"s_nop 1\n" \
"v_permlane16_swap_b32 v24, v25\n" \
"v_max_u32 v24, v24, v25\n" \
"v_mov_b32 v25, v24\n" \
"s_nop 1\n" \
"v_permlane32_swap_b32 v24, v25\n" \
"v_max_u32 v24, v24, v25\n" \
"s_nop 0\n" \
"v_readfirstlane_b32 s94, v24\n" \
"s_add_u32 s94, s94, s68\n" \
"v_add_u32 v16, s67, v20\n" \
"Lb_dly2_%=:\n" \
"s_memrealtime s[74:75]\n" \
"s_waitcnt lgkmcnt(0)\n" \
"s_lshl_b32 s74, s74, 6\n" \
"v_subrev_u32 v17, s74, v16\n" \
"v_cmp_lt_i32 vcc, 0, v17\n" \
"s_cbranch_vccnz Lb_dly2_%=\n" \
"s_mov_b32 s89, s74\n" \
"buffer_load_dwordx4 v[64:67], v120, s[44:47], s87 offen offset:0 sc1\n" \
"s_nop 11\n" \
"buffer_load_dwordx4 v[68:71], v120, s[44:47], s87 offen offset:2048 sc1\n" \
"s_nop 11\n" \
"buffer_load_dwordx4 v[72:75], v120, s[44:47], s53 offen offset:0 sc1\n" \
"s_nop 11\n" \
"buffer_load_dwordx4 v[76:79], v120, s[44:47], s53 offen offset:2048 sc1\n" \
"s_nop 11\n" \
"buffer_load_dwordx4 v[80:83], v120, s[44:47], s54 offen offset:0 sc1\n" \
"s_nop 11\n" \
"buffer_load_dwordx4 v[84:87], v120, s[44:47], s54 offen offset:2048 sc1\n" \
"s_nop 11\n" \
"buffer_load_dwordx4 v[88:91], v120, s[44:47], s56 offen offset:0 sc1\n" \
"s_nop 11\n" \
"buffer_load_dwordx4 v[92:95], v120, s[44:47], s56 offen offset:2048 sc1\n" \
"s_waitcnt vmcnt(7)\n" \
"v_smfmac_f32_16x16x64_bf16 v[96:99], v[64:67], a[0:7], v117\n" \
"v_max3_u32 v16, v64, v65, v64\n" \
"v_smfmac_f32_16x16x64_bf16 v[104:107], v[64:67], a[16:23], v117\n" \
"v_max3_u32 v16, v66, v67, v16\n" \
"v_smfmac_f32_16x16x64_bf16 v[100:103], v[64:67], a[8:15], v117\n" \
"v_cmp_eq_u32 vcc, -1, v16\n" \
"v_smfmac_f32_16x16x64_bf16 v[108:111], v[64:67], a[24:31], v117\n" \
"s_waitcnt vmcnt(6)\n" \
"v_smfmac_f32_16x16x64_bf16 v[96:99], v[68:71], a[32:39], v117\n" \
"v_max3_u32 v16, v68, v69, v16\n" \
"s_cbranch_vccnz Lb_redo_%=\n" \
"v_smfmac_f32_16x16x64_bf16 v[104:107], v[68:71], a[48:55], v117\n" \
"v_max3_u32 v16, v70, v71, v16\n" \
"v_smfmac_f32_16x16x64_bf16 v[100:103], v[68:71], a[40:47], v117\n" \
"v_smfmac_f32_16x16x64_bf16 v[108:111], v[68:71], a[56:63], v117\n" \
"s_waitcnt vmcnt(5)\n" \
"v_smfmac_f32_16x16x64_bf16 v[96:99], v[72:75], a[64:71], v117\n" \
"v_max3_u32 v16, v72, v73, v16\n" \
"v_smfmac_f32_16x16x64_bf16 v[104:107], v[72:75], a[80:87], v117\n" \
"v_max3_u32 v16, v74, v75, v16\n" \
"v_smfmac_f32_16x16x64_bf16 v[100:103], v[72:75], a[72:79], v117\n" \
"v_smfmac_f32_16x16x64_bf16 v[108:111], v[72:75], a[88:95], v117\n" \
"s_waitcnt vmcnt(4)\n" \
"v_smfmac_f32_16x16x64_bf16 v[96:99], v[76:79], a[96:103], v117\n" \
"v_max3_u32 v16, v76, v77, v16\n" \
"v_smfmac_f32_16x16x64_bf16 v[104:107], v[76:79], a[112:119], v117\n" \
"v_max3_u32 v16, v78, v79, v16\n" \
"v_smfmac_f32_16x16x64_bf16 v[100:103], v[76:79], a[104:111], v117\n" \
"v_smfmac_f32_16x16x64_bf16 v[108:111], v[76:79], a[120:127], v117\n" \
"s_waitcnt vmcnt(3)\n" \
"v_smfmac_f32_16x16x64_bf16 v[96:99], v[80:83], a[128:135], v117\n" \
"v_max3_u32 v16, v80, v81, v16\n" \
"v_smfmac_f32_16x16x64_bf16 v[104:107], v[80:83], a[144:151], v117\n" \
"v_max3_u32 v16, v82, v83, v16\n" \
"v_smfmac_f32_16x16x64_bf16 v[100:103], v[80:83], a[136:143], v117\n" \
"v_smfmac_f32_16x16x64_bf16 v[108:111], v[80:83], a[152:159], v117\n" \
"s_waitcnt vmcnt(2)\n" \
"v_smfmac_f32_16x16x64_bf16 v[96:99], v[84:87], a[160:167], v117\n" \
"v_max3_u32 v16, v84, v85, v16\n" \
"v_smfmac_f32_16x16x64_bf16 v[104:107], v[84:87], a[176:183], v117\n" \
"v_max3_u32 v16, v86, v87, v16\n" \
"v_smfmac_f32_16x16x64_bf16 v[100:103], v[84:87], a[168:175], v117\n" \
"v_smfmac_f32_16x16x64_bf16 v[108:111], v[84:87], a[184:191], v117\n" \
"s_waitcnt vmcnt(1)\n" \
"v_smfmac_f32_16x16x64_bf16 v[96:99], v[88:91], a[192:199], v117\n" \
"v_max3_u32 v16, v88, v89, v16\n" \
"v_smfmac_f32_16x16x64_bf16 v[104:107], v[88:91], a[208:215], v117\n" \
"v_max3_u32 v16, v90, v91, v16\n" \
"v_smfmac_f32_16x16x64_bf16 v[100:103], v[88:91], a[200:207], v117\n" \
"v_smfmac_f32_16x16x64_bf16 v[108:111], v[88:91], a[216:223], v117\n" \
"s_waitcnt vmcnt(0)\n" \
"v_smfmac_f32_16x16x64_bf16 v[96:99], v[92:95], a[224:231], v117\n" \
"v_max3_u32 v16, v92, v93, v16\n" \
"v_smfmac_f32_16x16x64_bf16 v[104:107], v[92:95], a[240:247], v117\n" \
"v_max3_u32 v16, v94, v95, v16\n" \
"v_smfmac_f32_16x16x64_bf16 v[100:103], v[92:95], a[232:239], v117\n" \
"v_cmp_eq_u32 vcc, -1, v16\n" \
"v_smfmac_f32_16x16x64_bf16 v[108:111], v[92:95], a[248:255], v117\n" \
"s_cbranch_vccnz Lb_redo_%=\n" \
"Lb_odone_%=:\n" \
"v_mov_b32 v95, v20\n" \
"v_cndmask_b32 v96, v96, v104, s[60:61]\n" \
"v_cndmask_b32 v97, v97, v105, s[60:61]\n" \
"v_cndmask_b32 v98, v98, v106, s[60:61]\n" \
"v_cndmask_b32 v99, v99, v107, s[60:61]\n" \
"s_nop 1\n" \
"v_cndmask_b32 v100, v100, v108, s[60:61]\n" \
"v_cndmask_b32 v101, v101, v109, s[60:61]\n" \
"v_cndmask_b32 v102, v102, v110, s[60:61]\n" \
"v_cndmask_b32 v103, v103, v111, s[60:61]\n" \
"v_permlane32_swap_b32 v96, v100\n" \
"v_permlane32_swap_b32 v97, v101\n" \
"v_permlane32_swap_b32 v98, v102\n" \
"v_permlane32_swap_b32 v99, v103\n" \
"v_pk_add_f32 v[96:97], v[96:97], v[100:101]\n" \
"v_pk_add_f32 v[98:99], v[98:99], v[102:103]\n" \
"s_nop 1\n" \
"v_permlane16_swap_b32 v96, v98\n" \
"v_permlane16_swap_b32 v97, v99\n" \
"v_pk_add_f32 v[96:97], v[96:97], v[98:99]\n" \
"ds_write2_b32 v122, v96, v97 offset0:0 offset1:32\n" \
"s_waitcnt lgkmcnt(0)\n" \
"s_barrier\n" \
"s_cmp_lg_u32 s66, 0\n" \
"s_cbranch_scc1 Lb_skip_%=\n" \
"ds_read_b64 v[96:97], v123 offset:0\n" \
"ds_read_b64 v[98:99], v123 offset:512\n" \
"ds_read_b64 v[100:101], v123 offset:1024\n" \
"ds_read_b64 v[102:103], v123 offset:1536\n" \
"s_waitcnt lgkmcnt(2)\n" \
"v_pk_add_f32 v[96:97], v[96:97], v[98:99]\n" \
"s_waitcnt lgkmcnt(0)\n" \
"v_pk_add_f32 v[100:101], v[100:101], v[102:103]\n" \
"v_pk_add_f32 v[96:97], v[96:97], v[100:101]\n" \
"v_cvt_pk_bf16_f32 v96, v96, v97\n" \
"buffer_store_dword v96, v124, s[44:47], s52 offen sc1\n" \
"buffer_store_dword v96, v124, s[44:47], s100 offen sc1\n" \
"s_memrealtime s[72:73]\n" \
"s_waitcnt lgkmcnt(0)\n" \
"s_lshl_b32 s59, s72, 6\n" \
"s_sub_u32 s59, s59, s89\n" \
"s_sub_u32 s59, s59, s88\n" \
"s_min_i32 s59, s59, 768\n" \
"s_ashr_i32 s59, s59, 2\n" \
"s_add_u32 s88, s88, s59\n" \
"v_mov_b32 v96, s88\n" \
"buffer_store_dword v96, off, s[44:47], s97 sc1\n" \
"Lb_skip_%=:\n" \
"s_add_u32 s49, s49, 32768\n" \
"s_add_u32 s99, s99, 32768\n" \
"s_add_u32 s50, s50, 131072\n" \
"s_add_u32 s51, s51, 131072\n" \
"s_add_u32 s52, s52, 32768\n" \
"s_add_u32 s100, s100, 32768\n" \
"s_add_u32 s48, s48, 1\n" \
"s_branch Lb_loop_%=\n" \
"Lb_poll_%=:\n" \
"s_mov_b32 s59, s49\n" \
"s_mov_b32 s49, s99\n" \
"s_mov_b32 s99, s59\n" \
"buffer_load_dwordx4 v[0:3], v118, s[44:47], s49 offen offset:0 sc1\n" \
"buffer_load_dwordx4 v[4:7], v118, s[44:47], s49 offen offset:1024 sc1\n" \
"buffer_load_dwordx4 v[8:11], v118, s[44:47], s49 offen offset:2048 sc1\n" \
"buffer_load_dwordx4 v[12:15], v118, s[44:47], s49 offen offset:3072 sc1\n" \
"s_nop 7\n" \
"v_mov_b32 v112, 0\n" \
"v_mov_b32 v113, 0\n" \
"v_mov_b32 v114, 0\n" \
"v_mov_b32 v115, 0\n" \
"s_waitcnt vmcnt(0)\n" \
"v_max3_u32 v16, v0, v1, v2\n" \
"v_max3_u32 v17, v8, v9, v10\n" \
"v_max3_u32 v16, v16, v3, v4\n" \
"v_max3_u32 v17, v17, v11, v12\n" \
"v_max3_u32 v16, v16, v5, v6\n" \
"v_max3_u32 v17, v17, v13, v14\n" \
"v_max3_u32 v16, v16, v7, v15\n" \
"v_max_u32 v16, v16, v17\n" \
"v_cmp_eq_u32 vcc, -1, v16\n" \
"s_cbranch_vccz Lb_pollok_%=\n" \
"s_sub_u32 s58, s58, 1\n" \
"s_cmp_lg_u32 s58, 0\n" \
"s_cbranch_scc1 Lb_poll_%=\n" \
"s_branch Lb_abort_%=\n" \
"Lb_redo_%=:\n" \
"buffer_load_dwordx4 v[64:67], v120, s[44:47], s87 offen offset:0 sc1\n" \
"buffer_load_dwordx4 v[68:71], v120, s[44:47], s87 offen offset:2048 sc1\n" \
"buffer_load_dwordx4 v[72:75], v120, s[44:47], s53 offen offset:0 sc1\n" \
"buffer_load_dwordx4 v[76:79], v120, s[44:47], s53 offen offset:2048 sc1\n" \
"buffer_load_dwordx4 v[80:83], v120, s[44:47], s54 offen offset:0 sc1\n" \
"buffer_load_dwordx4 v[84:87], v120, s[44:47], s54 offen offset:2048 sc1\n" \
"buffer_load_dwordx4 v[88:91], v120, s[44:47], s56 offen offset:0 sc1\n" \
"buffer_load_dwordx4 v[92:95], v120, s[44:47], s56 offen offset:2048 sc1\n" \
"s_nop 7\n" \
"v_mov_b32 v96, 0\n" \
"v_mov_b32 v97, 0\n" \
"v_mov_b32 v98, 0\n" \
"v_mov_b32 v99, 0\n" \
"v_mov_b32 v100, 0\n" \
"v_mov_b32 v101, 0\n" \
"v_mov_b32 v102, 0\n" \
"v_mov_b32 v103, 0\n" \
"v_mov_b32 v104, 0\n" \
"v_mov_b32 v105, 0\n" \
"v_mov_b32 v106, 0\n" \
"v_mov_b32 v107, 0\n" \
"v_mov_b32 v108, 0\n" \
"v_mov_b32 v109, 0\n" \
"v_mov_b32 v110, 0\n" \
"v_mov_b32 v111, 0\n" \
"s_waitcnt vmcnt(0)\n" \
"v_max3_u32 v16, v64, v65, v66\n" \
"v_max3_u32 v17, v80, v81, v82\n" \
"v_max3_u32 v16, v16, v67, v68\n" \
"v_max3_u32 v17, v17, v83, v84\n" \
"v_max3_u32 v16, v16, v69, v70\n" \
"v_max3_u32 v17, v17, v85, v86\n" \
"v_max3_u32 v16, v16, v71, v72\n" \
"v_max3_u32 v17, v17, v87, v88\n" \
"v_max3_u32 v16, v16, v73, v74\n" \
"v_max3_u32 v17, v17, v89, v90\n" \
"v_max3_u32 v16, v16, v75, v76\n" \
"v_max3_u32 v17, v17, v91, v92\n" \
"v_max3_u32 v16, v16, v77, v78\n" \
"v_max3_u32 v17, v17, v93, v94\n" \
"v_max3_u32 v16, v16, v79, v95\n" \
"v_max_u32 v16, v16, v17\n" \
"v_cmp_eq_u32 vcc, -1, v16\n" \
"s_cbranch_vccz Lb_redo2_%=\n" \
"s_sub_u32 s58, s58, 1\n" \
"s_cmp_lg_u32 s58, 0\n" \
"s_cbranch_scc1 Lb_redo_%=\n" \
"s_branch Lb_abort_%=\n" \
"Lb_redo2_%=:\n" \
"v_smfmac_f32_16x16x64_bf16 v[96:99], v[64:67], a[0:7], v117\n" \
"v_smfmac_f32_16x16x64_bf16 v[104:107], v[64:67], a[16:23], v117\n" \
"v_smfmac_f32_16x16x64_bf16 v[100:103], v[64:67], a[8:15], v117\n" \
"v_smfmac_f32_16x16x64_bf16 v[108:111], v[64:67], a[24:31], v117\n" \
"v_smfmac_f32_16x16x64_bf16 v[96:99], v[68:71], a[32:39], v117\n" \
"v_smfmac_f32_16x16x64_bf16 v[104:107], v[68:71], a[48:55], v117\n" \
"v_smfmac_f32_16x16x64_bf16 v[100:103], v[68:71], a[40:47], v117\n" \
"v_smfmac_f32_16x16x64_bf16 v[108:111], v[68:71], a[56:63], v117\n" \
"v_smfmac_f32_16x16x64_bf16 v[96:99], v[72:75], a[64:71], v117\n" \
"v_smfmac_f32_16x16x64_bf16 v[104:107], v[72:75], a[80:87], v117\n" \
"v_smfmac_f32_16x16x64_bf16 v[100:103], v[72:75], a[72:79], v117\n" \
"v_smfmac_f32_16x16x64_bf16 v[108:111], v[72:75], a[88:95], v117\n" \
"v_smfmac_f32_16x16x64_bf16 v[96:99], v[76:79], a[96:103], v117\n" \
"v_smfmac_f32_16x16x64_bf16 v[104:107], v[76:79], a[112:119], v117\n" \
"v_smfmac_f32_16x16x64_bf16 v[100:103], v[76:79], a[104:111], v117\n" \
"v_smfmac_f32_16x16x64_bf16 v[108:111], v[76:79], a[120:127], v117\n" \
"v_smfmac_f32_16x16x64_bf16 v[96:99], v[80:83], a[128:135], v117\n" \
"v_smfmac_f32_16x16x64_bf16 v[104:107], v[80:83], a[144:151], v117\n" \
"v_smfmac_f32_16x16x64_bf16 v[100:103], v[80:83], a[136:143], v117\n" \
"v_smfmac_f32_16x16x64_bf16 v[108:111], v[80:83], a[152:159], v117\n" \
"v_smfmac_f32_16x16x64_bf16 v[96:99], v[84:87], a[160:167], v117\n" \
"v_smfmac_f32_16x16x64_bf16 v[104:107], v[84:87], a[176:183], v117\n" \
"v_smfmac_f32_16x16x64_bf16 v[100:103], v[84:87], a[168:175], v117\n" \
"v_smfmac_f32_16x16x64_bf16 v[108:111], v[84:87], a[184:191], v117\n" \
"v_smfmac_f32_16x16x64_bf16 v[96:99], v[88:91], a[192:199], v117\n" \
"v_smfmac_f32_16x16x64_bf16 v[104:107], v[88:91], a[208:215], v117\n" \
"v_smfmac_f32_16x16x64_bf16 v[100:103], v[88:91], a[200:207], v117\n" \
"v_smfmac_f32_16x16x64_bf16 v[108:111], v[88:91], a[216:223], v117\n" \
"v_smfmac_f32_16x16x64_bf16 v[96:99], v[92:95], a[224:231], v117\n" \
"v_smfmac_f32_16x16x64_bf16 v[104:107], v[92:95], a[240:247], v117\n" \
"v_smfmac_f32_16x16x64_bf16 v[100:103], v[92:95], a[232:239], v117\n" \
"v_smfmac_f32_16x16x64_bf16 v[108:111], v[92:95], a[248:255], v117\n" \
"s_branch Lb_odone_%=\n" \
"Lb_abort_%=:\n" \
"s_mov_b32 s65, 1\n" \
"Lb_end_%=:\n" \
"s_waitcnt vmcnt(0) lgkmcnt(0)\n"
#define ACLOB "a0","a1","a2","a3","a4","a5","a6","a7","a8","a9","a10","a11","a12","a13","a14","a15","a16","a17","a18","a19","a20","a21","a22","a23","a24","a25","a26","a27","a28","a29","a30","a31","a32","a33","a34","a35","a36","a37","a38","a39","a40","a41","a42","a43","a44","a45","a46","a47","a48","a49","a50","a51","a52","a53","a54","a55","a56","a57","a58","a59","a60","a61","a62","a63","a64","a65","a66","a67","a68","a69","a70","a71","a72","a73","a74","a75","a76","a77","a78","a79","a80","a81","a82","a83","a84","a85","a86","a87","a88","a89","a90","a91","a92","a93","a94","a95","a96","a97","a98","a99","a100","a101","a102","a103","a104","a105","a106","a107","a108","a109","a110","a111","a112","a113","a114","a115","a116","a117","a118","a119","a120","a121","a122","a123","a124","a125","a126","a127","a128","a129","a130","a131","a132","a133","a134","a135","a136","a137","a138","a139","a140","a141","a142","a143","a144","a145","a146","a147","a148","a149","a150","a151","a152","a153","a154","a155","a156","a157","a158","a159","a160","a161","a162","a163","a164","a165","a166","a167","a168","a169","a170","a171","a172","a173","a174","a175","a176","a177","a178","a179","a180","a181","a182","a183","a184","a185","a186","a187","a188","a189","a190","a191","a192","a193","a194","a195","a196","a197","a198","a199","a200","a201","a202","a203","a204","a205","a206","a207","a208","a209","a210","a211","a212","a213","a214","a215","a216","a217","a218","a219","a220","a221","a222","a223","a224","a225","a226","a227","a228","a229","a230","a231","a232","a233","a234","a235","a236","a237","a238","a239","a240","a241","a242","a243","a244","a245","a246","a247","a248","a249","a250","a251","a252","a253","a254","a255"
#define VCLOB_G "v0","v1","v2","v3","v4","v5","v6","v7","v8","v9","v10","v11","v12","v13","v14","v15","v16","v17","v18","v19","v20","v21","v22","v23","v24","v25","v26","v27","v28","v29","v30","v31","v32","v33","v34","v35","v36","v37","v38","v39","v40","v41","v42","v43","v44","v45","v46","v47","v48","v49","v50","v51","v52","v53","v54","v55","v56","v57","v58","v59","v60","v61","v62","v63","v64","v65","v66","v67","v68","v69","v70","v71","v72","v73","v74","v75","v76","v77","v78","v79","v80","v81","v82","v83","v84","v85","v86","v87","v88","v89","v90","v91","v92","v93","v94","v95","v96","v97","v98","v99","v100","v101","v102","v103","v104","v105","v106","v107","v108","v109","v110","v111","v112","v113","v114","v115","v116","v117","v118","v119","v120","v121","v122","v123","v124","v125","v126","v127"
#define VCLOB_S "v8","v9","v10","v11","v12","v13","v14","v15","v16","v17","v18","v19","v20","v21","v22","v23","v24","v25","v26","v27","v28","v29","v30","v31","v32","v33","v34","v35","v36","v37","v38","v39","v40","v41","v42","v43","v44","v45","v46","v47","v48","v49","v50","v51","v52","v53","v54","v55","v56","v57","v58","v59","v60","v61","v62","v63","v64","v65","v66","v67","v68","v69","v70","v71","v72","v73","v74","v75","v76","v77","v78","v79","v80","v81","v82","v83","v84","v85","v86","v87","v88","v89","v90","v91","v92","v93","v94","v95","v96","v97","v98","v99","v100","v101","v102","v103","v104","v105","v106","v107","v108","v109","v110","v111","v112","v113","v114","v115","v116","v117","v118","v119","v2","v3","v124","v125","v126","v127"
#define VCLOB_QK "v1","v2","v3","v4","v5","v6","v7","v8","v9","v10","v11","v12","v13","v14","v15","v16","v17","v18","v19","v20","v21","v22","v23","v24","v25","v26","v27","v28","v29","v30","v31","v32","v33","v34","v35","v36","v37","v38","v39","v40","v41","v42","v43","v44","v45","v46","v47","v48","v49","v50","v51","v52","v53","v54","v55","v56","v57","v58","v59","v60","v61","v62","v63","v64","v65","v66","v67","v68","v69","v70","v71","v72","v73","v74","v75","v76","v77","v78","v79","v80","v81","v82","v83","v84","v85","v86","v87","v88","v89","v90","v91","v92","v93","v94","v95","v96","v97","v98","v99","v100","v101","v102","v103","v104","v105","v106","v107","v128","v129","v130","v131","v132","v133","v134","v135","v136","v137","v138","v139","v140","v141","v142","v143","v144","v145","v146","v147","v148","v149","v150","v151","v152","v153","v154","v155","v156","v157","v158","v159","v160","v161","v162","v163","v164","v165","v166","v167","v168","v169","v170","v171","v172","v173","v174","v175","v176","v177","v178","v179","v180","v181","v182","v183","v184","v185","v186","v187","v188","v189","v190","v191","v192","v193","v194","v195","v196","v197","v198","v199","v200","v201","v202","v203","v204","v205","v206","v207","v208","v209","v210","v211","v212","v213","v214","v215","v216","v217","v218","v219","v220","v221","v222","v223","v224","v225","v226","v227","v228","v229","v230","v231","v232","v233","v234","v235","v236","v237","v238","v239","v240","v241","v242","v243","v244","v245","v246","v247","v248","v249","v250","v251","v252","v253","v254","v255"
#define VCLOB_B "v11","v12","v13","v14","v15","v16","v17","v18","v19","v20","v21","v22","v23","v24","v25","v26","v27","v28","v29","v30","v31","v32","v33","v34","v35","v36","v37","v38","v39","v40","v41","v42","v43","v44","v45","v46","v47","v48","v49","v50","v51","v52","v53","v54","v55","v56","v57","v58","v59","v60","v61","v62","v63","v64","v65","v66","v67","v68","v69","v70","v71","v72","v73","v74","v75","v76","v77","v78","v79","v80","v81","v82","v83","v84","v85","v86","v87","v88","v89","v90","v91","v92","v93","v94","v95","v96","v97","v98","v99","v100","v101","v102","v103","v104","v105","v106","v107","v108","v109","v110","v111","v112","v113","v114","v115","v125","v126","v127","v128","v129","v130","v131","v132","v133","v134","v135","v136","v137","v138","v139","v140","v141","v142","v143","v144","v145","v146","v147","v148","v149","v150","v151","v152","v153","v154","v155","v156","v157","v158","v159","v160","v161","v162","v163","v164","v165","v166","v167","v168","v169","v170","v171","v172","v173","v174","v175","v176","v177","v178","v179","v180","v181","v182","v183","v184","v185","v186","v187","v188","v189","v190","v191","v192","v193","v194","v195","v196","v197","v198","v199","v200","v201","v202","v203","v204","v205","v206","v207","v208","v209","v210","v211","v212","v213","v214","v215","v216","v217","v218","v219","v220","v221","v222","v223","v224","v225","v226","v227","v228","v229","v230","v231","v232","v233","v234","v235","v236","v237","v238","v239","v240","v241","v242","v243","v244","v245","v246","v247","v248","v249","v250","v251","v252","v253","v254","v255"
#define KSN 2

constexpr u32 NTASKS = 432;
__device__ const u32 TASKS[NTASKS * 6] = {
258,26214400,55574528,65568,0,0,
515,26214400,72351744,65568,0,0,
65794,26214400,55582720,65568,0,0,
66051,26214400,72368128,65568,0,0,
131330,26214400,55590912,65568,0,0,
131587,26214400,72384512,65568,0,0,
196866,26214400,55599104,65568,0,0,
197123,26214400,72400896,65568,0,0,
262402,26214400,55607296,65568,0,0,
262659,26214400,72417280,65568,0,0,
327938,26214400,55615488,65568,0,0,
328195,26214400,72433664,65568,0,0,
393474,26214400,55623680,65568,0,0,
393731,26214400,72450048,65568,0,0,
459010,26214400,55631872,65568,0,0,
459267,26214400,72466432,65568,0,0,
524546,26214400,55640064,65568,0,0,
524803,26214400,72482816,65568,0,0,
590082,26214400,55648256,65568,0,0,
590339,26214400,72499200,65568,0,0,
655618,26214400,55656448,65568,0,0,
655875,26214400,72515584,65568,0,0,
721154,26214400,55664640,65568,0,0,
721411,26214400,72531968,65568,0,0,
786690,26214400,55672832,65568,0,0,
786947,26214400,72548352,65568,0,0,
852226,26214400,55681024,65568,0,0,
852483,26214400,72564736,65568,0,0,
917762,26214400,55689216,65568,0,0,
918019,26214400,72581120,65568,0,0,
983298,26214400,55697408,65568,0,0,
983555,26214400,72597504,65568,0,0,
258,26476544,56623104,131104,0,0,
515,26476544,73400320,131104,0,0,
65794,26476544,56631296,131104,0,0,
66051,26476544,73416704,131104,0,0,
131330,26476544,56639488,131104,0,0,
131587,26476544,73433088,131104,0,0,
196866,26476544,56647680,131104,0,0,
197123,26476544,73449472,131104,0,0,
262402,26476544,56655872,131104,0,0,
262659,26476544,73465856,131104,0,0,
327938,26476544,56664064,131104,0,0,
328195,26476544,73482240,131104,0,0,
393474,26476544,56672256,131104,0,0,
393731,26476544,73498624,131104,0,0,
459010,26476544,56680448,131104,0,0,
459267,26476544,73515008,131104,0,0,
524546,26476544,56688640,131104,0,0,
524803,26476544,73531392,131104,0,0,
590082,26476544,56696832,131104,0,0,
590339,26476544,73547776,131104,0,0,
655618,26476544,56705024,131104,0,0,
655875,26476544,73564160,131104,0,0,
721154,26476544,56713216,131104,0,0,
721411,26476544,73580544,131104,0,0,
786690,26476544,56721408,131104,0,0,
786947,26476544,73596928,131104,0,0,
852226,26476544,56729600,131104,0,0,
852483,26476544,73613312,131104,0,0,
917762,26476544,56737792,131104,0,0,
918019,26476544,73629696,131104,0,0,
983298,26476544,56745984,131104,0,0,
983555,26476544,73646080,131104,0,0,
2,26214400,38797312,1114128,0,0,
65538,26214400,38805504,1114128,0,0,
131074,26214400,38813696,1114128,0,0,
196610,26214400,38821888,1114128,0,0,
262146,26214400,38830080,1114128,0,0,
327682,26214400,38838272,1114128,0,0,
393218,26214400,38846464,1114128,0,0,
458754,26214400,38854656,1114128,0,0,
524290,26214400,38862848,1114128,0,0,
589826,26214400,38871040,1114128,0,0,
655362,26214400,38879232,1114128,0,0,
720898,26214400,38887424,1114128,0,0,
786434,26214400,38895616,1114128,0,0,
851970,26214400,38903808,1114128,0,0,
917506,26214400,38912000,1114128,0,0,
983042,26214400,38920192,1114128,0,0,
2,26476544,39845888,1179664,0,0,
65538,26476544,39854080,1179664,0,0,
131074,26476544,39862272,1179664,0,0,
196610,26476544,39870464,1179664,0,0,
262146,26476544,39878656,1179664,0,0,
327682,26476544,39886848,1179664,0,0,
393218,26476544,39895040,1179664,0,0,
458754,26476544,39903232,1179664,0,0,
524290,26476544,39911424,1179664,0,0,
589826,26476544,39919616,1179664,0,0,
655362,26476544,39927808,1179664,0,0,
720898,26476544,39936000,1179664,0,0,
786434,26476544,39944192,1179664,0,0,
851970,26476544,39952384,1179664,0,0,
917506,26476544,39960576,1179664,0,0,
983042,26476544,39968768,1179664,0,0,
256,26738688,57671680,196640,262176,0,
513,26738688,74448896,196640,262176,0,
65792,26738688,57679872,196640,262176,0,
66049,26738688,74465280,196640,262176,0,
131328,26738688,57688064,196640,262176,0,
131585,26738688,74481664,196640,262176,0,
196864,26738688,57696256,196640,262176,0,
197121,26738688,74498048,196640,262176,0,
262400,26738688,57704448,196640,262176,0,
262657,26738688,74514432,196640,262176,0,
327936,26738688,57712640,196640,262176,0,
328193,26738688,74530816,196640,262176,0,
393472,26738688,57720832,196640,262176,0,
393729,26738688,74547200,196640,262176,0,
459008,26738688,57729024,196640,262176,0,
459265,26738688,74563584,196640,262176,0,
524544,26738688,57737216,196640,262176,0,
524801,26738688,74579968,196640,262176,0,
590080,26738688,57745408,196640,262176,0,
590337,26738688,74596352,196640,262176,0,
655616,26738688,57753600,196640,262176,0,
655873,26738688,74612736,196640,262176,0,
721152,26738688,57761792,196640,262176,0,
721409,26738688,74629120,196640,262176,0,
786688,26738688,57769984,196640,262176,0,
786945,26738688,74645504,196640,262176,0,
852224,26738688,57778176,196640,262176,0,
852481,26738688,74661888,196640,262176,0,
917760,26738688,57786368,196640,262176,0,
918017,26738688,74678272,196640,262176,0,
983296,26738688,57794560,196640,262176,0,
983553,26738688,74694656,196640,262176,0,
256,27262976,59768832,327712,393248,0,
513,27262976,76546048,327712,393248,0,
65792,27262976,59777024,327712,393248,0,
66049,27262976,76562432,327712,393248,0,
131328,27262976,59785216,327712,393248,0,
131585,27262976,76578816,327712,393248,0,
196864,27262976,59793408,327712,393248,0,
197121,27262976,76595200,327712,393248,0,
262400,27262976,59801600,327712,393248,0,
262657,27262976,76611584,327712,393248,0,
327936,27262976,59809792,327712,393248,0,
328193,27262976,76627968,327712,393248,0,
393472,27262976,59817984,327712,393248,0,
393729,27262976,76644352,327712,393248,0,
459008,27262976,59826176,327712,393248,0,
459265,27262976,76660736,327712,393248,0,
524544,27262976,59834368,327712,393248,0,
524801,27262976,76677120,327712,393248,0,
590080,27262976,59842560,327712,393248,0,
590337,27262976,76693504,327712,393248,0,
655616,27262976,59850752,327712,393248,0,
655873,27262976,76709888,327712,393248,0,
721152,27262976,59858944,327712,393248,0,
721409,27262976,76726272,327712,393248,0,
786688,27262976,59867136,327712,393248,0,
786945,27262976,76742656,327712,393248,0,
852224,27262976,59875328,327712,393248,0,
852481,27262976,76759040,327712,393248,0,
917760,27262976,59883520,327712,393248,0,
918017,27262976,76775424,327712,393248,0,
983296,27262976,59891712,327712,393248,0,
983553,27262976,76791808,327712,393248,0,
0,26738688,40894464,1245200,1310736,0,
65536,26738688,40902656,1245200,1310736,0,
131072,26738688,40910848,1245200,1310736,0,
196608,26738688,40919040,1245200,1310736,0,
262144,26738688,40927232,1245200,1310736,0,
327680,26738688,40935424,1245200,1310736,0,
393216,26738688,40943616,1245200,1310736,0,
458752,26738688,40951808,1245200,1310736,0,
524288,26738688,40960000,1245200,1310736,0,
589824,26738688,40968192,1245200,1310736,0,
655360,26738688,40976384,1245200,1310736,0,
720896,26738688,40984576,1245200,1310736,0,
786432,26738688,40992768,1245200,1310736,0,
851968,26738688,41000960,1245200,1310736,0,
917504,26738688,41009152,1245200,1310736,0,
983040,26738688,41017344,1245200,1310736,0,
256,27787264,61865984,458784,524320,0,
513,27787264,78643200,458784,524320,0,
65792,27787264,61874176,458784,524320,0,
66049,27787264,78659584,458784,524320,0,
131328,27787264,61882368,458784,524320,0,
131585,27787264,78675968,458784,524320,0,
196864,27787264,61890560,458784,524320,0,
197121,27787264,78692352,458784,524320,0,
262400,27787264,61898752,458784,524320,0,
262657,27787264,78708736,458784,524320,0,
327936,27787264,61906944,458784,524320,0,
328193,27787264,78725120,458784,524320,0,
393472,27787264,61915136,458784,524320,0,
393729,27787264,78741504,458784,524320,0,
459008,27787264,61923328,458784,524320,0,
459265,27787264,78757888,458784,524320,0,
524544,27787264,61931520,458784,524320,0,
524801,27787264,78774272,458784,524320,0,
590080,27787264,61939712,458784,524320,0,
590337,27787264,78790656,458784,524320,0,
655616,27787264,61947904,458784,524320,0,
655873,27787264,78807040,458784,524320,0,
721152,27787264,61956096,458784,524320,0,
721409,27787264,78823424,458784,524320,0,
786688,27787264,61964288,458784,524320,0,
786945,27787264,78839808,458784,524320,0,
852224,27787264,61972480,458784,524320,0,
852481,27787264,78856192,458784,524320,0,
917760,27787264,61980672,458784,524320,0,
918017,27787264,78872576,458784,524320,0,
983296,27787264,61988864,458784,524320,0,
983553,27787264,78888960,458784,524320,0,
0,27262976,42991616,1376272,1441808,0,
65536,27262976,42999808,1376272,1441808,0,
131072,27262976,43008000,1376272,1441808,0,
196608,27262976,43016192,1376272,1441808,0,
262144,27262976,43024384,1376272,1441808,0,
327680,27262976,43032576,1376272,1441808,0,
393216,27262976,43040768,1376272,1441808,0,
458752,27262976,43048960,1376272,1441808,0,
524288,27262976,43057152,1376272,1441808,0,
589824,27262976,43065344,1376272,1441808,0,
655360,27262976,43073536,1376272,1441808,0,
720896,27262976,43081728,1376272,1441808,0,
786432,27262976,43089920,1376272,1441808,0,
851968,27262976,43098112,1376272,1441808,0,
917504,27262976,43106304,1376272,1441808,0,
983040,27262976,43114496,1376272,1441808,0,
256,28311552,63963136,589856,655392,0,
513,28311552,80740352,589856,655392,0,
65792,28311552,63971328,589856,655392,0,
66049,28311552,80756736,589856,655392,0,
131328,28311552,63979520,589856,655392,0,
131585,28311552,80773120,589856,655392,0,
196864,28311552,63987712,589856,655392,0,
197121,28311552,80789504,589856,655392,0,
262400,28311552,63995904,589856,655392,0,
262657,28311552,80805888,589856,655392,0,
327936,28311552,64004096,589856,655392,0,
328193,28311552,80822272,589856,655392,0,
393472,28311552,64012288,589856,655392,0,
393729,28311552,80838656,589856,655392,0,
459008,28311552,64020480,589856,655392,0,
459265,28311552,80855040,589856,655392,0,
524544,28311552,64028672,589856,655392,0,
524801,28311552,80871424,589856,655392,0,
590080,28311552,64036864,589856,655392,0,
590337,28311552,80887808,589856,655392,0,
655616,28311552,64045056,589856,655392,0,
655873,28311552,80904192,589856,655392,0,
721152,28311552,64053248,589856,655392,0,
721409,28311552,80920576,589856,655392,0,
786688,28311552,64061440,589856,655392,0,
786945,28311552,80936960,589856,655392,0,
852224,28311552,64069632,589856,655392,0,
852481,28311552,80953344,589856,655392,0,
917760,28311552,64077824,589856,655392,0,
918017,28311552,80969728,589856,655392,0,
983296,28311552,64086016,589856,655392,0,
983553,28311552,80986112,589856,655392,0,
0,27787264,45088768,1507344,1572880,0,
65536,27787264,45096960,1507344,1572880,0,
131072,27787264,45105152,1507344,1572880,0,
196608,27787264,45113344,1507344,1572880,0,
262144,27787264,45121536,1507344,1572880,0,
327680,27787264,45129728,1507344,1572880,0,
393216,27787264,45137920,1507344,1572880,0,
458752,27787264,45146112,1507344,1572880,0,
524288,27787264,45154304,1507344,1572880,0,
589824,27787264,45162496,1507344,1572880,0,
655360,27787264,45170688,1507344,1572880,0,
720896,27787264,45178880,1507344,1572880,0,
786432,27787264,45187072,1507344,1572880,0,
851968,27787264,45195264,1507344,1572880,0,
917504,27787264,45203456,1507344,1572880,0,
983040,27787264,45211648,1507344,1572880,0,
256,28835840,66060288,720928,786464,0,
513,28835840,82837504,720928,786464,0,
65792,28835840,66068480,720928,786464,0,
66049,28835840,82853888,720928,786464,0,
131328,28835840,66076672,720928,786464,0,
131585,28835840,82870272,720928,786464,0,
196864,28835840,66084864,720928,786464,0,
197121,28835840,82886656,720928,786464,0,
262400,28835840,66093056,720928,786464,0,
262657,28835840,82903040,720928,786464,0,
327936,28835840,66101248,720928,786464,0,
328193,28835840,82919424,720928,786464,0,
393472,28835840,66109440,720928,786464,0,
393729,28835840,82935808,720928,786464,0,
459008,28835840,66117632,720928,786464,0,
459265,28835840,82952192,720928,786464,0,
524544,28835840,66125824,720928,786464,0,
524801,28835840,82968576,720928,786464,0,
590080,28835840,66134016,720928,786464,0,
590337,28835840,82984960,720928,786464,0,
655616,28835840,66142208,720928,786464,0,
655873,28835840,83001344,720928,786464,0,
721152,28835840,66150400,720928,786464,0,
721409,28835840,83017728,720928,786464,0,
786688,28835840,66158592,720928,786464,0,
786945,28835840,83034112,720928,786464,0,
852224,28835840,66166784,720928,786464,0,
852481,28835840,83050496,720928,786464,0,
917760,28835840,66174976,720928,786464,0,
918017,28835840,83066880,720928,786464,0,
983296,28835840,66183168,720928,786464,0,
983553,28835840,83083264,720928,786464,0,
0,28311552,47185920,1638416,1703952,0,
65536,28311552,47194112,1638416,1703952,0,
131072,28311552,47202304,1638416,1703952,0,
196608,28311552,47210496,1638416,1703952,0,
262144,28311552,47218688,1638416,1703952,0,
327680,28311552,47226880,1638416,1703952,0,
393216,28311552,47235072,1638416,1703952,0,
458752,28311552,47243264,1638416,1703952,0,
524288,28311552,47251456,1638416,1703952,0,
589824,28311552,47259648,1638416,1703952,0,
655360,28311552,47267840,1638416,1703952,0,
720896,28311552,47276032,1638416,1703952,0,
786432,28311552,47284224,1638416,1703952,0,
851968,28311552,47292416,1638416,1703952,0,
917504,28311552,47300608,1638416,1703952,0,
983040,28311552,47308800,1638416,1703952,0,
256,29360128,68157440,852000,917536,0,
513,29360128,84934656,852000,917536,0,
65792,29360128,68165632,852000,917536,0,
66049,29360128,84951040,852000,917536,0,
131328,29360128,68173824,852000,917536,0,
131585,29360128,84967424,852000,917536,0,
196864,29360128,68182016,852000,917536,0,
197121,29360128,84983808,852000,917536,0,
262400,29360128,68190208,852000,917536,0,
262657,29360128,85000192,852000,917536,0,
327936,29360128,68198400,852000,917536,0,
328193,29360128,85016576,852000,917536,0,
393472,29360128,68206592,852000,917536,0,
393729,29360128,85032960,852000,917536,0,
459008,29360128,68214784,852000,917536,0,
459265,29360128,85049344,852000,917536,0,
524544,29360128,68222976,852000,917536,0,
524801,29360128,85065728,852000,917536,0,
590080,29360128,68231168,852000,917536,0,
590337,29360128,85082112,852000,917536,0,
655616,29360128,68239360,852000,917536,0,
655873,29360128,85098496,852000,917536,0,
721152,29360128,68247552,852000,917536,0,
721409,29360128,85114880,852000,917536,0,
786688,29360128,68255744,852000,917536,0,
786945,29360128,85131264,852000,917536,0,
852224,29360128,68263936,852000,917536,0,
852481,29360128,85147648,852000,917536,0,
917760,29360128,68272128,852000,917536,0,
918017,29360128,85164032,852000,917536,0,
983296,29360128,68280320,852000,917536,0,
983553,29360128,85180416,852000,917536,0,
0,28835840,49283072,1769488,1835024,0,
65536,28835840,49291264,1769488,1835024,0,
131072,28835840,49299456,1769488,1835024,0,
196608,28835840,49307648,1769488,1835024,0,
262144,28835840,49315840,1769488,1835024,0,
327680,28835840,49324032,1769488,1835024,0,
393216,28835840,49332224,1769488,1835024,0,
458752,28835840,49340416,1769488,1835024,0,
524288,28835840,49348608,1769488,1835024,0,
589824,28835840,49356800,1769488,1835024,0,
655360,28835840,49364992,1769488,1835024,0,
720896,28835840,49373184,1769488,1835024,0,
786432,28835840,49381376,1769488,1835024,0,
851968,28835840,49389568,1769488,1835024,0,
917504,28835840,49397760,1769488,1835024,0,
983040,28835840,49405952,1769488,1835024,0,
256,29884416,70254592,983072,1048608,0,
513,29884416,87031808,983072,1048608,0,
65792,29884416,70262784,983072,1048608,0,
66049,29884416,87048192,983072,1048608,0,
131328,29884416,70270976,983072,1048608,0,
131585,29884416,87064576,983072,1048608,0,
196864,29884416,70279168,983072,1048608,0,
197121,29884416,87080960,983072,1048608,0,
262400,29884416,70287360,983072,1048608,0,
262657,29884416,87097344,983072,1048608,0,
327936,29884416,70295552,983072,1048608,0,
328193,29884416,87113728,983072,1048608,0,
393472,29884416,70303744,983072,1048608,0,
393729,29884416,87130112,983072,1048608,0,
459008,29884416,70311936,983072,1048608,0,
459265,29884416,87146496,983072,1048608,0,
524544,29884416,70320128,983072,1048608,0,
524801,29884416,87162880,983072,1048608,0,
590080,29884416,70328320,983072,1048608,0,
590337,29884416,87179264,983072,1048608,0,
655616,29884416,70336512,983072,1048608,0,
655873,29884416,87195648,983072,1048608,0,
721152,29884416,70344704,983072,1048608,0,
721409,29884416,87212032,983072,1048608,0,
786688,29884416,70352896,983072,1048608,0,
786945,29884416,87228416,983072,1048608,0,
852224,29884416,70361088,983072,1048608,0,
852481,29884416,87244800,983072,1048608,0,
917760,29884416,70369280,983072,1048608,0,
918017,29884416,87261184,983072,1048608,0,
983296,29884416,70377472,983072,1048608,0,
983553,29884416,87277568,983072,1048608,0,
0,29360128,51380224,1900560,1966096,0,
65536,29360128,51388416,1900560,1966096,0,
131072,29360128,51396608,1900560,1966096,0,
196608,29360128,51404800,1900560,1966096,0,
262144,29360128,51412992,1900560,1966096,0,
327680,29360128,51421184,1900560,1966096,0,
393216,29360128,51429376,1900560,1966096,0,
458752,29360128,51437568,1900560,1966096,0,
524288,29360128,51445760,1900560,1966096,0,
589824,29360128,51453952,1900560,1966096,0,
655360,29360128,51462144,1900560,1966096,0,
720896,29360128,51470336,1900560,1966096,0,
786432,29360128,51478528,1900560,1966096,0,
851968,29360128,51486720,1900560,1966096,0,
917504,29360128,51494912,1900560,1966096,0,
983040,29360128,51503104,1900560,1966096,0,
0,29884416,53477376,2031632,2097168,0,
65536,29884416,53485568,2031632,2097168,0,
131072,29884416,53493760,2031632,2097168,0,
196608,29884416,53501952,2031632,2097168,0,
262144,29884416,53510144,2031632,2097168,0,
327680,29884416,53518336,2031632,2097168,0,
393216,29884416,53526528,2031632,2097168,0,
458752,29884416,53534720,2031632,2097168,0,
524288,29884416,53542912,2031632,2097168,0,
589824,29884416,53551104,2031632,2097168,0,
655360,29884416,53559296,2031632,2097168,0,
720896,29884416,53567488,2031632,2097168,0,
786432,29884416,53575680,2031632,2097168,0,
851968,29884416,53583872,2031632,2097168,0,
917504,29884416,53592064,2031632,2097168,0,
983040,29884416,53600256,2031632,2097168,0};

constexpr u32 MB = 1u << 20;
constexpr u32 O_MEMX = 0;
constexpr u32 O_OM = 2 * MB;
constexpr u32 O_VM = 10 * MB;
constexpr u32 O_HINT = 18 * MB;
constexpr u32 O_MEMY = 20 * MB;
constexpr u32 O_KM = 180 * MB;
constexpr u32 O_CNT = 24 * MB;
constexpr u32 O_DBG = O_CNT + 256 * 1024;
constexpr u32 O_PAR = O_CNT + 512 * 1024;
constexpr u32 O_XN = 25 * MB;
constexpr u32 O_H = 29 * MB;
constexpr u32 O_HN = 33 * MB;
constexpr u32 O_Q = 37 * MB;
constexpr u32 O_KX = 53 * MB;
constexpr u32 O_VT = 69 * MB;
constexpr u32 O_OX = 85 * MB;
constexpr u32 O_ACT = 101 * MB;
constexpr u32 O_P = 115 * MB;
constexpr u32 O_P2 = 147 * MB;
constexpr u32 WS_END = 179 * MB;
constexpr u32 C_FLAG = 1024, C_TICK = 1152, C_CNT = 2048, F_KV = 1, F_Q = 17;
constexpr u32 C_PA = 4096, C_PW = C_PA + 16 * 32, C_P1 = C_PW + 32 * 32, C_PG = C_P1 + 16 * 32, C_PD = C_PG + 56 * 32;
constexpr u32 DUR_Q = 84, DUR_B = 72;
constexpr int LDS_BYTES = 160 * 1024;

struct Params {
  const u16 *x, *mem0, *wq, *wk, *wv, *wo, *wg, *wu, *wd;
  u16* out;
  char* ws;
  int phase, d1, d2, d3, flip, pad;
};

DI u32 rfl(u32 x) { return __builtin_amdgcn_readfirstlane(x); }
DI u32 cvtpk(float lo, float hi) { f32x2 v = {lo, hi}; bf16x2 b = __builtin_convertvector(v, bf16x2); return __builtin_bit_cast(u32, b); }
DI float blo(u32 v) { return __uint_as_float(v << 16); }
DI float bhi(u32 v) { return __uint_as_float(v & 0xFFFF0000u); }
DI u32x4 mksrd(const void* p, long long off) { u64 b = (u64)p + (u64)off; u32x4 r; r.x = rfl((u32)b); r.y = rfl((u32)(b >> 32)); r.z = 0xFFFFFFFFu; r.w = 0x00020000u; return r; }
DI __amdgpu_buffer_rsrc_t mkrs(const void* p) { return __builtin_amdgcn_make_buffer_rsrc((void*)p, 0, 0xFFFFFFFFu, 0x00020000); }
DI u32x4 ld128(__amdgpu_buffer_rsrc_t r, u32 v, u32 s) { return __builtin_amdgcn_raw_buffer_load_b128(r, v, s, 16); }
DI u32x2 ld64(__amdgpu_buffer_rsrc_t r, u32 v, u32 s) { return __builtin_amdgcn_raw_buffer_load_b64(r, v, s, 16); }
DI void st128(u32x4 d, __amdgpu_buffer_rsrc_t r, u32 v, u32 s) { __builtin_amdgcn_raw_buffer_store_b128(d, r, v + s, 0, 16); }
DI int lane_id() { int l; asm volatile("v_mbcnt_lo_u32_b32 %0, -1, 0\n\tv_mbcnt_hi_u32_b32 %0, -1, %0" : "=v"(l)); return l; }
DI void swap16(u32& a, u32& b) { auto r = __builtin_amdgcn_permlane16_swap(a, b, false, false); a = r[0]; b = r[1]; }
DI void swap32(u32& a, u32& b) { auto r = __builtin_amdgcn_permlane32_swap(a, b, false, false); a = r[0]; b = r[1]; }
DI float gsum(float v) { u32 a = __float_as_uint(v), b = a; swap16(a, b); v = __uint_as_float(a) + __uint_as_float(b); a = __float_as_uint(v); b = a; swap32(a, b); return __uint_as_float(a) + __uint_as_float(b); }
DI float gmax(float v) { u32 a = __float_as_uint(v), b = a; swap16(a, b); v = fmaxf(__uint_as_float(a), __uint_as_float(b)); a = __float_as_uint(v); b = a; swap32(a, b); return fmaxf(__uint_as_float(a), __uint_as_float(b)); }
DI f32x4 mfma32(u32x4 a, u32x4 b, f32x4 c) { return __builtin_amdgcn_mfma_f32_16x16x32_bf16(__builtin_bit_cast(bf16x8, a), __builtin_bit_cast(bf16x8, b), c, 0, 0, 0); }

DI void ln_store(float (&v)[64], float* red, int w, int l, __amdgpu_buffer_rsrc_t ws, u32 soff) {
  float s = 0.f;
#pragma unroll
  for (int i = 0; i < 64; i++) s += v[i];
  s = gsum(s);
  if (l < 16) red[w * 16 + l] = s;
  __syncthreads();
  const int r = l & 15;
  const float mean = (red[r] + red[16 + r] + red[32 + r] + red[48 + r]) * (1.f / 1024.f);
  float q = 0.f;
#pragma unroll
  for (int i = 0; i < 64; i++) { v[i] -= mean; q += v[i] * v[i]; }
  q = gsum(q);
  if (l < 16) red[64 + w * 16 + l] = q;
  __syncthreads();
  const float var = (red[64 + r] + red[80 + r] + red[96 + r] + red[112 + r]) * (1.f / 1024.f);
  const float rs = 1.f / sqrtf(var + 1e-5f);
#pragma unroll
  for (int k = 0; k < 8; k++) {
    u32x4 o;
#pragma unroll
    for (int j = 0; j < 4; j++) o[j] = cvtpk(v[8 * k + 2 * j] * rs, v[8 * k + 2 * j + 1] * rs);
    st128(o, ws, l * 16 + k * 1024, soff);
  }
}
DI u32x4 ld_rowfrag(const u16* base, int mt, int kt, int l) {
  const int r = l & 15, g = l >> 4;
  u32x4 d = *(const u32x4*)(base + (size_t)(mt * 16 + r) * 1024 + kt * 32 + (g & 1) * 16 + (g >> 1) * 8);
  u32 a = d.x, b = d.y, c = d.z, e = d.w;
  swap16(a, c); swap16(b, e);
  return u32x4{a, b, c, e};
}

__global__ void __launch_bounds__(256) prep_kernel(Params p) {
  __shared__ float red[128];
  const int b = blockIdx.x, tid = threadIdx.x, l = tid & 63, w = rfl(tid >> 6);
  if (b < 128) {
    float v[64];
#pragma unroll
    for (int k = 0; k < 8; k++) {
      u32x4 d = ld_rowfrag(p.x, b, 8 * w + k, l);
#pragma unroll
      for (int j = 0; j < 4; j++) { v[8 * k + 2 * j] = blo(d[j]); v[8 * k + 2 * j + 1] = bhi(d[j]); }
    }
    ln_store(v, red, w, l, mkrs(p.ws), O_XN + (b * 32 + 8 * w) * 1024);
  } else {
    const int c = b - 128;
    if (c == 0) {
      for (int i = tid; i < 2048; i += 256) {
        const int cg = (i >> 5) & 31, rh = (i >> 4) & 1, c4 = (i >> 2) & 3, rr = i & 3;
        const u32x4 v = *(const u32x4*)(p.mem0 + (rh * 4 + rr) * 1024 + cg * 32 + c4 * 8);
        *(u32x4*)(p.ws + O_MEMX + (cg >> 4) * 16384 + (i >> 10) * 8192 + (i & 511) * 16) = v;
        *(u32x4*)(p.ws + O_MEMY + (cg >> 4) * 16384 + (i >> 10) * 8192 + (i & 511) * 16) = v;
      }
      for (int i = tid; i < 4096; i += 256) { *(u32x4*)(p.ws + O_CNT + i * 16) = u32x4{0, 0, 0, 0}; }
      if (tid < 64) *(u32*)(p.ws + O_HINT + (tid >> 5) * 8192 + (tid & 31) * 128) = 0;
      if (tid < 128) *(u32*)(p.ws + O_HINT + 32768 + tid * 128) = DUR_B << 6;
    } else if (c <= 11 && w == 0) {
      char* a = p.ws + c * 2 * MB - 16384;
      u32 best0 = ~0u, best1 = ~0u;
      for (int r = 0; r < 8; r++) {
        u64 c0, c1; u32 v = ~0u;
        asm volatile("s_memtime %0\n\ts_waitcnt lgkmcnt(0)\n\tglobal_store_dword %3, %2, off sc1\n\ts_waitcnt vmcnt(0)\n\ts_memtime %1\n\ts_waitcnt lgkmcnt(0)" : "=&s"(c0), "=&s"(c1) : "v"(v), "v"(a + (r & 1) * 8192) : "memory");
        const u32 d = (u32)(c1 - c0);
        if (r & 1) best1 = min(best1, d); else best0 = min(best0, d);
      }
      const u32 side = (__builtin_amdgcn_s_getreg(20 | (3 << 11)) >> 2) & 1;
      if (l == 0) { ((u32*)(p.ws + O_PAR))[c - 1] = (best1 < best0 ? 1u : 0u) ^ side; ((u32*)(p.ws + O_PAR))[15 + c] = best0 | (best1 << 16); }
    }
    const u32 lo = O_MEMX + 32768, n = (O_HINT - lo) / 16;
    const u32x4 ones = {~0u, ~0u, ~0u, ~0u};
    for (u32 i = c * 256 + tid; i < n; i += 384 * 256) *(u32x4*)(p.ws + lo + (size_t)i * 16) = ones;
    for (u32 i = c * 256 + tid; i < (2 * MB - 32768) / 16; i += 384 * 256) *(u32x4*)(p.ws + O_MEMY + 32768 + (size_t)i * 16) = ones;
  }
}

enum { E_F = 0, E_V = 1, E_P = 2, E_G = 3 };
template <int VAR>
DI void gemm_unit(char* ws, u32 w, u32 epi, u32 xoff, u32 xkt, const u16* w0, const u16* w1, u32 ldw, u32 nst, u32 ooff) {
  constexpr u32 MT = VAR < 2 ? 8 : VAR < 5 ? 4 : 2, NT = VAR < 4 ? 8 : 4, NX = MT / 2, NW = NT / 2;
  const int l = lane_id();
  const u32 wm = w >> 1, wn = w & 1;
  u32x4 sX = mksrd(ws, (long long)xoff - 4096), sW0 = mksrd(w0, -4096), sW1 = mksrd(w1, -4096), sO = mksrd(ws, 0);
  u32x4 vXd, vWd, vWr;
#pragma unroll
  for (int i = 0; i < 4; i++) vXd[i] = 4096 + l * 16 + (NX * w + i) * xkt * 1024 - i * 1024;
  { const int rr = l >> 3, pp = l & 7;
#pragma unroll
    for (int i = 0; i < 4; i++) vWd[i] = 4096 + ((8 * w + rr) * ldw + 8 * (pp ^ (rr & 6))) * 2 + (epi == E_G ? (i >> 1) : i) * 128 - i * 1024; }
  u32 vXr = l * 16 + wm * MT * 1024;
  { const int g = l >> 4, i = l & 15, q = i >> 2, pq = i & 3, rr = 4 * (g & 1) + q;
#pragma unroll
    for (int t = 0; t < 4; t++) { const int tt = NT == 8 ? t : 2 * wn + (t & 1);
      vWr[t] = 65536 + (NT == 8 ? wn * 2048 : 0) + (g >> 1) * NW * 1024 + rr * 128 + (((2 * tt + (pq >> 1)) ^ (rr & 6)) * 16) + (pq & 1) * 8; } }
  u32 vOut = epi == E_P ? ((l >> 2) & 3) * 16384 + (l >> 4) * 64 + (l & 3) * 16 : l * 16;
  const u32 ostride = epi == E_F ? 131072 : epi == E_V ? 262144 : epi == E_P ? 65536 : 114688;
  const u32 wns = (epi == E_F ? 4096 : epi == E_V ? 8192 : 2048) / (NT == 8 ? 1 : epi == E_G ? 2 : 4);
  u32x4 sA = {0u, 0u, rfl(64 * ldw), rfl(nst / (NT == 8 ? 4 : 4 * KSN) - 1)};
  u32x4 sB = {w * NX * 1024, 65536 + w * NW * 1024, rfl(epi), 0u};
  u32x4 sD = {rfl(ooff + wm * (epi == E_V ? MT / 2 : MT) * ostride + wn * wns), rfl(ostride), 0u, 0u};
#define GEMM_ASM(BODY) asm volatile("s_nop 4\n" BODY : "+{s[52:55]}"(sA), "+{s[64:67]}"(sD) \
    : "{s[40:43]}"(sX), "{s[44:47]}"(sW0), "{s[48:51]}"(sW1), "{s[56:59]}"(sB), "{s[60:63]}"(sO), \
      "{v[128:131]}"(vXd), "{v[132:135]}"(vWd), "{v136}"(vXr), "{v137}"(vWr.x), "{v138}"(vWr.y), "{v139}"(vWr.z), "{v140}"(vWr.w), "{v141}"(vOut) \
    : "memory", "scc", ACLOB, VCLOB_G)
  if (VAR == 0) GEMM_ASM(ASM_G88);
  if (VAR == 1) GEMM_ASM(ASM_G88S);
  if (VAR == 2) GEMM_ASM(ASM_G48);
  if (VAR == 3) GEMM_ASM(ASM_G48S);
}

typedef __attribute__((address_space(1))) u32 gu32;
#define RLX __ATOMIC_RELAXED, __HIP_MEMORY_SCOPE_AGENT
DI void wait_ge(gu32* word, u32 target) {
  for (u32 spins = 0; spins < (1u << 18); spins++) {
    if (__all(__hip_atomic_load(word, RLX) >= target)) break;
    __builtin_amdgcn_s_sleep(1);
  }
  __builtin_amdgcn_fence(__ATOMIC_ACQUIRE, "wavefront");
}

#define TOUCH 4
DI void attn_pair(char* wsp, int seg, int h) {
  const u32 svm = O_VM + seg * 131072 + (h >> 2) * 16384 + (h & 3) * 2048 + (((const u32*)(wsp + O_PAR))[5 + (seg >> 4)] & 1) * 8192;
  const int l = lane_id(), ii = l & 15, g = l >> 4;
  __amdgpu_buffer_rsrc_t ws = mkrs(wsp);
  const u32 so = (seg * 2 * 128 + 4 * h) * 1024, vo = l * 16, sm = (seg * 32 + h) * 2048;
  u32x4 q[2][4], k[2][4], km[4], v[8]; u32x2 vm[8];
#pragma unroll
  for (int a = 0; a < 2; a++)
#pragma unroll
    for (int t = 0; t < 4; t++) { q[a][t] = ld128(ws, vo + t * 1024, O_Q + so + a * 131072); k[a][t] = ld128(ws, vo + t * 1024, O_KX + so + a * 131072); }
#pragma unroll
  for (int e = 0; e < 8; e++) v[e] = ld128(ws, vo + (e & 3) * 1024, O_VT + (seg * 256 + 8 * h) * 1024 + (e >> 2) * 4096);
  const u32 sn = seg + TOUCH < 64 ? seg + TOUCH : 63;
  u32 t0 = __builtin_amdgcn_raw_buffer_load_b32(ws, (l & 31) * 128 + (l >> 5) * 131072, O_KX + (sn * 256 + 4 * h) * 1024, 16);
  u32 t1 = __builtin_amdgcn_raw_buffer_load_b32(ws, l * 128, O_VT + (sn * 256 + 8 * h) * 1024, 16);
  asm volatile("" :: "v"(t0), "v"(t1));
  wait_ge((gu32*)(wsp + O_CNT) + h * 32 + (l & 3), seg + 1);
#pragma unroll
  for (int t = 0; t < 4; t++) km[t] = ld128(ws, g * 128 + (ii & 7) * 16 + t * 512, O_KM + sm);
#pragma unroll
  for (int e = 0; e < 8; e++) vm[e] = ld64(ws, (g & 1) * 128 + ii * 8 + e * 256, svm);
  const float c = 0.08838834764831845f * 1.4426950408889634f, NEG = -3.0e38f;
  const u32x4 ones = {0x3f803f80u, 0x3f803f80u, 0x3f803f80u, 0x3f803f80u};
#pragma unroll
  for (int b = 0; b < 2; b++) {
    f32x4 s0 = {0, 0, 0, 0}, s1 = {0, 0, 0, 0}, s2 = {0, 0, 0, 0};
#pragma unroll
    for (int t = 0; t < 4; t++) { s0 = mfma32(k[0][t], q[b][t], s0); if (b == 1) s1 = mfma32(k[1][t], q[b][t], s1); s2 = mfma32(km[t], q[b][t], s2); }
    float m = NEG;
#pragma unroll
    for (int j = 0; j < 4; j++) {
      const bool diag = 4 * g + j <= ii;
      if (b == 0) { s0[j] = diag ? s0[j] : NEG; s1[j] = NEG; } else s1[j] = diag ? s1[j] : NEG;
      s2[j] = g < 2 ? s2[j] : NEG;
      m = fmaxf(m, fmaxf(s0[j], fmaxf(s1[j], s2[j])));
    }
    m = gmax(m);
    const float mc = m * c;
    float p0[4], p1[4], p2[4];
#pragma unroll
    for (int j = 0; j < 4; j++) { p0[j] = __builtin_amdgcn_exp2f(s0[j] * c - mc); p1[j] = b == 0 ? 0.f : __builtin_amdgcn_exp2f(s1[j] * c - mc); p2[j] = __builtin_amdgcn_exp2f(s2[j] * c - mc); }
    const u32x4 px = {cvtpk(p0[0], p0[1]), cvtpk(p0[2], p0[3]), cvtpk(p1[0], p1[1]), cvtpk(p1[2], p1[3])};
    const u32x4 pm = {cvtpk(p2[0], p2[1]), cvtpk(p2[2], p2[3]), 0u, 0u};
    f32x4 ls = mfma32(ones, px, f32x4{0, 0, 0, 0});
    ls = mfma32(ones, pm, ls);
    const float r = __builtin_amdgcn_rcpf(ls[0]);
#pragma unroll
    for (int t = 0; t < 4; t++) {
      f32x4 o0 = mfma32(v[2 * t], px, f32x4{0, 0, 0, 0}), o1 = mfma32(v[2 * t + 1], px, f32x4{0, 0, 0, 0});
      o0 = mfma32(u32x4{vm[2 * t].x, vm[2 * t].y, 0u, 0u}, pm, o0); o1 = mfma32(u32x4{vm[2 * t + 1].x, vm[2 * t + 1].y, 0u, 0u}, pm, o1);
      const u32x4 o = {cvtpk(o0[0] * r, o0[1] * r), cvtpk(o0[2] * r, o0[3] * r), cvtpk(o1[0] * r, o1[1] * r), cvtpk(o1[2] * r, o1[3] * r)};
      st128(o, ws, vo + t * 1024, O_OX + so + b * 131072);
    }
  }
}

template <int KS>
DI void sum_partials(__amdgpu_buffer_rsrc_t ws, u32 pbase, u32 rg, u32 w, int l, f32x4 (&s)[4]) {
  const u32 so = pbase + rg * 16384 + w * 4096, vo = l * 16;
  u32x4 t[KS][4];
#pragma unroll
  for (int k = 0; k < KS; k++)
#pragma unroll
    for (int i = 0; i < 4; i++) t[k][i] = ld128(ws, vo + i * 1024, so + k * 8 * MB);
#pragma unroll
  for (int i = 0; i < 4; i++) {
    s[i] = __builtin_bit_cast(f32x4, t[0][i]);
#pragma unroll
    for (int k = 1; k < KS; k++) s[i] += __builtin_bit_cast(f32x4, t[k][i]);
  }
}
DI float rsum(float v) {
  v += __builtin_bit_cast(float, __builtin_amdgcn_update_dpp(0, __builtin_bit_cast(int, v), 0x124, 0xF, 0xF, false));
  v += __builtin_bit_cast(float, __builtin_amdgcn_update_dpp(0, __builtin_bit_cast(int, v), 0x128, 0xF, 0xF, false));
  return gsum(v);
}
DI void st_frag(__amdgpu_buffer_rsrc_t ws, u32 base, u32 rg, u32 w, int l, u32x2 (&h)[4]) {
#pragma unroll
  for (int i = 0; i < 4; i += 2) {
    u32 a = h[i].x, b = h[i].y, c = h[i + 1].x, d = h[i + 1].y;
    swap16(a, c); swap16(b, d);
    const int q = l >> 4, kt = 8 * w + 2 * i + 2 * (q & 1) + (q >> 1);
    st128(u32x4{a, b, c, d}, ws, kt * 1024 + ((l >> 2) & 3) * 256 + (l & 3) * 16, base + (rg >> 2) * 32768 + (rg & 3) * 64);
  }
}
template <int KS>
DI void reduce1(const Params& p, u32 rg, u32 w, float* red) {
  const int l = lane_id();
  __amdgpu_buffer_rsrc_t ws = mkrs(p.ws);
  u32x2 h[4]; f32x4 s[4];
#pragma unroll
  for (int i = 0; i < 4; i++) h[i] = *(const u32x2*)(p.x + (size_t)(rg * 4 + (l & 3)) * 1024 + w * 256 + i * 64 + (l >> 2) * 4);
  sum_partials<KS>(ws, O_P, rg, w, l, s);
  float sum = 0.f;
#pragma unroll
  for (int i = 0; i < 4; i++) {
    const u32 a = cvtpk(s[i][0], s[i][1]), b = cvtpk(s[i][2], s[i][3]);
    h[i] = u32x2{cvtpk(blo(h[i].x) + blo(a), bhi(h[i].x) + bhi(a)), cvtpk(blo(h[i].y) + blo(b), bhi(h[i].y) + bhi(b))};
    s[i] = f32x4{blo(h[i].x), bhi(h[i].x), blo(h[i].y), bhi(h[i].y)};
    sum += (s[i][0] + s[i][1]) + (s[i][2] + s[i][3]);
  }
  sum = rsum(sum);
  if (l < 4) red[w * 4 + l] = sum;
  st_frag(ws, O_H, rg, w, l, h);
  asm volatile("s_waitcnt lgkmcnt(0)\n\ts_barrier" ::: "memory");
  const int r = l & 3;
  const float mean = (red[r] + red[4 + r] + red[8 + r] + red[12 + r]) * (1.f / 1024.f);
  float q = 0.f;
#pragma unroll
  for (int i = 0; i < 4; i++) { s[i] -= mean; q += (s[i][0] * s[i][0] + s[i][1] * s[i][1]) + (s[i][2] * s[i][2] + s[i][3] * s[i][3]); }
  q = rsum(q);
  if (l < 4) red[16 + w * 4 + l] = q;
  asm volatile("s_waitcnt lgkmcnt(0)\n\ts_barrier" ::: "memory");
  const float rs = 1.f / sqrtf((red[16 + r] + red[20 + r] + red[24 + r] + red[28 + r]) * (1.f / 1024.f) + 1e-5f);
#pragma unroll
  for (int i = 0; i < 4; i++) h[i] = u32x2{cvtpk(s[i][0] * rs, s[i][1] * rs), cvtpk(s[i][2] * rs, s[i][3] * rs)};
  st_frag(ws, O_HN, rg, w, l, h);
}
template <int KS>
DI void reduce2(const Params& p, u32 rg, u32 w) {
  const int l = lane_id();
  __amdgpu_buffer_rsrc_t ws = mkrs(p.ws);
  u32x2 h[4]; f32x4 s[4];
#pragma unroll
  for (int i = 0; i < 4; i++) h[i] = ld64(ws, (2 * i + (l >> 5)) * 1024 + ((l >> 2) & 3) * 256 + (l & 3) * 16 + ((l >> 4) & 1) * 8, O_H + (rg >> 2) * 32768 + (rg & 3) * 64 + w * 8192);
  sum_partials<KS>(ws, O_P2, rg, w, l, s);
#pragma unroll
  for (int i = 0; i < 4; i++)
    *(u32x2*)(p.out + (size_t)(rg * 4 + (l & 3)) * 1024 + w * 256 + i * 64 + (l >> 2) * 4) =
        u32x2{cvtpk(blo(h[i].x) + s[i][0], bhi(h[i].x) + s[i][1]), cvtpk(blo(h[i].y) + s[i][2], bhi(h[i].y) + s[i][3])};
}

#define STAMP_OUT
#define STAMP_CLOB
#define STAMP_DECL(id)
#define STAMP_WRITE(id)

DI void role_qk(const Params& p, u32 h, u32 w, u32 par) {
  const int l = lane_id(), g = l >> 4, ii = l & 15;
  u32x4 sWq = mksrd(p.wq, (long long)(h * 128 + 32 * w) * 2 - 4096), sWk = mksrd(p.wk, (long long)(h * 128 + 32 * w) * 2 - 4096), sWS = mksrd(p.ws, 0);
  u32x4 vWd, vB0, vB1, vA, vC;
  { const int rho = l >> 2, pos = l & 3, r0 = rho & 1, r1 = (rho >> 1) & 1, r2 = (rho >> 2) & 1, r3 = rho >> 3;
    const int rr = 8 * r2 + 4 * r1 + 2 * r3 + r0, ch = pos ^ (2 * r2);
#pragma unroll
    for (int b = 0; b < 4; b++) vWd[b] = 4096 + (16 * b + rr) * 8192 + ch * 16 - b * 1024; }
  { const int q = ii >> 2, pq = ii & 3, q0 = q & 1, q1 = q >> 1, g0 = g & 1, g1 = g >> 1;
    const int base = w * 32768 + g1 * 1024 + ((q1 << 2) | (g0 << 1) | q0) * 64 + (pq & 1) * 8;
    vA.x = base + (((pq >> 1)) ^ (2 * q1)) * 16; vA.y = base + ((2 + (pq >> 1)) ^ (2 * q1)) * 16; }
  { const int hi = ii >> 3, r = ii & 7;
    vA.z = (w >> 1) * 16384 + (w & 1) * 4096 + (g >> 1) * 512 + (r >> 2) * 256 + (2 * (g & 1) + hi) * 64 + (r & 3) * 16;
    vA.w = 131072 + w * 4096 + l * 16;
    vC.x = l * 16;
    vC.y = (r >> 2) * 1024 + w * 256 + g * 64 + (r & 3) * 16;
    vC.z = w * 512 + (g & 1) * 128 + ii * 8;
    vC.w = hi ? 0xEEEEu : 0x4444u; }
  { auto sel = [&](int k, int col) { return (u32)(k == col ? 0x3f80u : 0u); };
    const int k0 = 4 * g;
    const int c0 = (g < 2) ? ii : -1, c1 = (g >= 2) ? ii + 8 : -1, c2 = (g < 2) ? ii - 8 : -1, c3 = (g >= 2) ? ii : -1;
    vB0.x = sel(k0, c0) | sel(k0 + 1, c0) << 16; vB0.y = sel(k0 + 2, c0) | sel(k0 + 3, c0) << 16;
    vB0.z = sel(k0, c1) | sel(k0 + 1, c1) << 16; vB0.w = sel(k0 + 2, c1) | sel(k0 + 3, c1) << 16;
    vB1.x = sel(k0, c2) | sel(k0 + 1, c2) << 16; vB1.y = sel(k0 + 2, c2) | sel(k0 + 3, c2) << 16;
    vB1.z = sel(k0, c3) | sel(k0 + 1, c3) << 16; vB1.w = sel(k0 + 2, c3) | sel(k0 + 3, c3) << 16; }
  u32 cneg = g < 2 ? __float_as_uint(-3.0e38f) : 0u;
  u32x8 sA = {0u, rfl(O_MEMX + (par & 1) * 8192), rfl(O_OM + (h >> 2) * 16384 + (h & 3) * 2048), rfl(O_VM + (h >> 2) * 16384 + (h & 3) * 2048), rfl(O_KM + h * 2048 + w * 512), rfl(O_KX + (4 * h + w) * 1024), rfl(O_VT + (8 * h + 2 * w) * 1024), w * 32768};
  u32x4 sB = {147456 + w * 2048, rfl(O_CNT + h * 128 + w * 4), 0u, 0u}, sM = {0x00FF00FFu, 0x00FF00FFu, 0xFF00FF00u, 0xFF00FF00u}, sC = {0u, 0u, w, 155648 + w * 1024};
  u32 dly = rfl(p.d1 << 6), pm = rfl(par >> 1);
  const u32 cs = __float_as_uint(0.08838834764831845f * 1.4426950408889634f);
  u32x2 cs2 = {cs, cs};
  u32 my = rfl(O_MEMY + ((par >> 10) & 1) * 8192), est = DUR_Q << 6, hdur = rfl(O_HINT + ((par >> 9) & 1) * 8192 + 32768);
  const u32 hb = O_HINT + ((par >> 9) & 1) * 8192;
  u32 hpoll = rfl(hb), hown = rfl(w == 3 ? hb + h * 128 : hb + 16384 + (h * 4 + w) * 64), kvf = O_CNT + (C_FLAG + F_KV) * 4;
  STAMP_DECL(h)
#define TUNE_OUT
  asm volatile("s_nop 4\n" ASM_QK
    : "+{s[48:55]}"(sA), "+{s[56:59]}"(sB), "+{s[64:67]}"(sC), "+{s99}"(my), "+{s88}"(est), "+{v0}"(cneg), "+{v[116:119]}"(vWd), "+{v[120:123]}"(vA) STAMP_OUT TUNE_OUT
    : "{s[36:39]}"(sWq), "{s[40:43]}"(sWk), "{s[44:47]}"(sWS), "{s[60:63]}"(sM), "{s68}"(dly), "{s86}"(pm), "{s[90:91]}"(cs2), "{s92}"(hpoll), "{s97}"(hown), "{s94}"(kvf), "{s101}"(hdur),
      "{v[108:111]}"(vB0), "{v[112:115]}"(vB1), "{v[124:127]}"(vC)
    : "memory", "scc", "vcc", "s72", "s73", "s74", "s75", "s87", "s89", "s93", "s95", "s96", "s98" STAMP_CLOB, ACLOB, VCLOB_QK);
  STAMP_WRITE(h)
}
DI void role_b(const Params& p, u32 b, u32 w, u32 par) {
  const int l = lane_id(), g = l >> 4, ii = l & 15;
  const u32 cg = b >> 1, rh = b & 1, h = b >> 1, half = b & 1;
  u32x4 sWo = mksrd(p.wo, (long long)w * 1024 * 2048 + cg * 64 - 4096), sWv = mksrd(p.wv, (long long)(h * 128 + half * 64 + 16 * w) * 2 - 4096), sWS = mksrd(p.ws, 0);
  u32x4 vOd, vVd, vC, vD; u32x2 vTo; u32 vTv, vMst;
  { const int rho = l >> 2, pos = l & 3, r0 = rho & 1, r1 = (rho >> 1) & 1, r2 = (rho >> 2) & 1, r3 = rho >> 3, ch = pos ^ (2 * r2);
#pragma unroll
    for (int k = 0; k < 4; k++) vOd[k] = 4096 + (32 * (k >> 1) + 16 * r1 + 8 * (k & 1) + 4 * r2 + 2 * r3 + r0) * 2048 + ch * 16 - k * 1024; }
  { const int rho = l >> 1, pos = l & 1, q0 = rho & 1, g0 = (rho >> 1) & 1, q1 = (rho >> 2) & 1, rd0 = (rho >> 3) & 1, g1 = rho >> 4;
#pragma unroll
    for (int k = 0; k < 4; k++) vVd[k] = 4096 + (64 * (k >> 1) + 32 * (k & 1) + 16 * g1 + 8 * q1 + 4 * g0 + 2 * rd0 + q0) * 8192 + pos * 16 - k * 1024; }
  { const int q = ii >> 2, pq = ii & 3, q0 = q & 1, q1 = q >> 1, g0 = g & 1, g1 = g >> 1;
    const int base = w * 32768 + g1 * 1024 + ((q1 << 2) | (g0 << 1) | q0) * 64 + (pq & 1) * 8;
    vTo.x = base + (((pq >> 1)) ^ (2 * q1)) * 16; vTo.y = base + ((2 + (pq >> 1)) ^ (2 * q1)) * 16;
    vTv = w * 32768 + ((g1 << 4) | (q1 << 2) | (g0 << 1) | q0) * 32 + pq * 8; }
  { const int hi = ii >> 3, r = ii & 7;
    vC.x = hi ? 0xEEEEu : 0x4444u;
    vC.y = ((ii >> 2) & 1) ? 0xEEEEu : 0x4444u;
    vC.z = (w >> 1) * 16384 + (w & 1) * 4096 + (g >> 1) * 512 + (r >> 2) * 256 + (2 * (g & 1) + hi) * 64 + (r & 3) * 16;
    vC.w = 131072 + w * 4096 + l * 16;
    vD.x = (2 * (ii >> 3) + (g >> 1)) * 256 + (2 * (g & 1) + ((ii >> 2) & 1)) * 64 + (ii & 3) * 16;
    vD.y = (g & 1) * 128 + ii * 8 + (g >> 1) * 4;
    vD.z = 147456 + w * 512 + (g & 1) * 256 + (16 * (g >> 1) + ii) * 4;
    vD.w = 147456 + g * 128 + ii * 8;
    vMst = (ii >> 2) * 64 + g * 16 + (ii & 3) * 4; }
  u32x8 sA = {0u, rfl(O_MEMX + (par & 1) * 8192), rfl(O_OM + w * 32768 + rh * 1024), rfl(O_VM + (h >> 2) * 16384 + (h & 3) * 2048 + (half * 4 + w) * 256), rfl(O_MEMX + 32768 + (par & 1) * 8192 + (cg >> 4) * 16384 + (cg & 15) * 512 + rh * 256), 0u, 0u, w * 32768};
  u32x4 sC = {0u, 0u, w, rfl(p.d3 << 6)};
  u32 d2 = rfl(p.d2 << 6), pm = rfl(par >> 1);
  const u32 hb = O_HINT + ((par >> 9) & 1) * 8192;
  u32 hpoll = rfl(hb), my = rfl(O_MEMY + ((par >> 10) & 1) * 8192), myo = rfl(my + 32768 + (cg >> 4) * 16384 + (cg & 15) * 512 + rh * 256);
  u32 est = DUR_B << 6, hdur = rfl(hb + 32768), hown = rfl(hb + 32768 + b * 128);
  u64 mhi = 0xFFFFFFFF00000000ull;
  STAMP_DECL(32 + b)
  asm volatile("s_nop 4\n" ASM_B
    : "+{s[48:55]}"(sA), "+{s[64:67]}"(sC), "+{s99}"(my), "+{s100}"(myo), "+{s88}"(est), "+{v[0:3]}"(vOd), "+{v[4:7]}"(vVd), "+{v[8:9]}"(vTo), "+{v10}"(vTv) STAMP_OUT
    : "{s[36:39]}"(sWo), "{s[40:43]}"(sWv), "{s[44:47]}"(sWS), "{s[60:61]}"(mhi), "{s68}"(d2), "{s86}"(pm), "{s92}"(hpoll), "{s97}"(hown), "{s101}"(hdur),
      "{v[116:119]}"(vC), "{v[120:123]}"(vD), "{v124}"(vMst)
    : "memory", "scc", "vcc", "s56", "s58", "s59", "s72", "s73", "s74", "s75", "s87", "s89", "s93", "s94", "s96", "s98" STAMP_CLOB, ACLOB, VCLOB_B);
  STAMP_WRITE(32 + b)
}

template <int KIND>
DI void role_s(const Params& p, u32 id, u32 w) {
  const int l = lane_id();
  const u32 ks = KIND == 1 ? 0 : id >> 3, cg = KIND == 1 ? id : id & 7, ldw = KIND == 1 ? 3584 : 1024;
  const u16* w0 = KIND == 0 ? p.wo + (size_t)ks * 1024 * 1024 + cg * 128 : KIND == 1 ? p.wg + cg * 64 : p.wd + (size_t)ks * 896 * 1024 + cg * 128;
  const u16* w1 = KIND == 1 ? p.wu + cg * 64 : w0 + 64;
  u32x4 sW0 = mksrd(w0, -4096), sW1 = mksrd(w1, -4096), sWS = mksrd(p.ws, 0);
  u32x2 vWd; u32x4 vWr, vC;
  { const int rr = l >> 3, pp = l & 7;
    vWd.x = 4096 + ((8 * w + rr) * ldw + 8 * (pp ^ (rr & 6))) * 2; vWd.y = vWd.x - 1024; }
  { const int g = l >> 4, i = l & 15, q = i >> 2, pq = i & 3, rr = 4 * (g & 1) + q;
#pragma unroll
    for (int t = 0; t < 2; t++) { const int nb = KIND == 1 ? t : w >> 1, tt = KIND == 1 ? w : 2 * (w & 1) + t;
      vWr[t] = nb * 1024 + (g >> 1) * 2048 + rr * 128 + (((2 * tt + (pq >> 1)) ^ (rr & 6)) * 16) + (pq & 1) * 8; vWr[2 + t] = vWr[t] + 65536; } }
  constexpr u32 xrow = KIND == 0 ? 131072 : KIND == 1 ? 32768 : 114688, nfl = KIND == 0 ? 2 : KIND == 1 ? 8 : 14;
  vC.x = l * 16;
  vC.y = KIND == 1 ? l * 16 + (w & 1) * 8 : ((l >> 2) & 3) * 16384 + (l >> 4) * 64 + (l & 3) * 16;
  vC.z = (l < nfl ? l : 0) * 128;
  vC.w = 131072 + 2048 + w * 4;
  const u32 xo = (KIND == 0 ? O_OX + ks * 32768 : KIND == 1 ? O_HN : O_ACT + ks * 28672) + (w & 1) * xrow + (w >> 1) * 4096;
  const u32 own = KIND == 0 ? C_PW + id * 32 : KIND == 1 ? C_PG + id * 32 : C_PD + id * 32;
  u32x8 sA = {0u, 0u, rfl(xo), 2 * xrow, 0u, 0u, 64 * ldw, rfl(O_CNT + own * 4 + (w ? 20480 : 0))};
  u32x4 sB = {w * 2048, (w & 1) * 32768 + (w >> 1) * 4096, 0u, 0u};
  u32x2 sO = {rfl(KIND == 1 ? O_ACT + (cg * 2 + (w >> 1)) * 1024 : (KIND ? O_P2 : O_P) + ks * 8 * MB + (cg * 8 + 2 * w) * 256), KIND == 1 ? 2 * 114688u : 131072u};
  u32x4 sD = {0u, 0u, 0u, 0u}, sE = {0u, 0u, (w >> 1) ? 0u : 1024u, KIND == 2 ? 0u : 1u};
  u32x2 sF = {1024u, rfl(O_CNT + (KIND == 0 ? C_PA + ks * 64 : KIND == 1 ? C_P1 : C_PG + ks * 14 * 32) * 4)};
#define S_ASM(BODY) asm volatile("s_nop 4\n" BODY : "+{s[48:55]}"(sA), "+{s[56:59]}"(sB), "+{s[60:61]}"(sO), "+{s[64:67]}"(sD), "+{s[68:71]}"(sE), "+{v[0:1]}"(vWd), "+{v[4:7]}"(vWr) \
    : "{s[36:39]}"(sW0), "{s[40:43]}"(sW1), "{s[44:47]}"(sWS), "{s[72:73]}"(sF), "{v[120:123]}"(vC) : "memory", "scc", "vcc", ACLOB, VCLOB_S)
  if (KIND == 0) S_ASM(ASM_SP);
  if (KIND == 1) S_ASM(ASM_SG);
  if (KIND == 2) S_ASM(ASM_SD);
}

typedef const __attribute__((address_space(4))) Params* KP;
DI Params args() {
  KP k = (KP)__builtin_amdgcn_kernarg_segment_ptr();
  asm volatile("" : "+s"(k));
  Params p;
  p.x = k->x; p.mem0 = k->mem0; p.wq = k->wq; p.wk = k->wk; p.wv = k->wv; p.wo = k->wo; p.wg = k->wg; p.wu = k->wu; p.wd = k->wd;
  p.out = k->out; p.ws = k->ws; p.phase = k->phase; p.d1 = k->d1; p.d2 = k->d2; p.d3 = k->d3; p.flip = k->flip; p.pad = 0;
  return p;
}

__global__ void __launch_bounds__(256, 1) main_kernel(Params p_) {
  extern __shared__ __attribute__((aligned(16))) char lds[];
  u32 w = rfl(threadIdx.x >> 6);
  const u32 b = blockIdx.x;
  volatile u32* ctl = (volatile u32*)(lds + 131072 + 1024);
  u32 t;
#define WX 0
  const bool chain = WX == 4 ? (b & 7) < 4 && (b >> 3) < 24 : (b & 7) < 4 && (b & 7) != WX;
  if (chain) {
    const Params p = args();
    const u32 r = WX == 4 ? (b >> 3) * 4 + (b & 3) : (b >> 3) * 3 + (b & 3) - ((b & 3) > WX);
    const u32* pt = (const u32*)(p.ws + O_PAR);
    u32 par = 0;
    for (int i = 0; i < 11; i++) par |= (pt[i] & 1) << i;
    par = rfl(par);
    { if (r < 32) role_qk(p, r, w, par); else role_b(p, r - 32, w, par); }
    return;
  }
  const u32 x = b & 3, j = b >> 3;
  const bool lo = (b & 7) < 4;
  const u32 role = lo ? 0 : j < 8 ? 2 : j < 22 ? 1 : (j - 22) * 4 + x < 16 ? 3 : (j - 22) * 4 + x < 32 ? 4 : 5;
  const u32 id = lo ? (WX == 4 ? x * 8 + (j - 24) : j) : j < 8 ? x * 8 + j : j < 22 ? x * 14 + (j - 8) : ((j - 22) * 4 + x) & 15;
  t = role == 3 ? id : role == 0 ? 16 + id : role == 1 ? 48 + id : role == 2 ? 104 + id : role == 4 ? 136 + id : 152 + id;
  for (u32 n = role == 3 ? 1 : 3; n > 0 && t < NTASKS; n--) {
    asm volatile("" : "+s"(w));
    const Params p = args();
    gu32* cnt = (gu32*)(p.ws + O_CNT);
    const u32* e = TASKS + t * 6;
    const u32 e0 = e[0], e1 = e[1], e2 = e[2], e3 = e[3], e4 = e[4];
    const u32 wsel = (e0 >> 8) & 3, type = e0 & 3;
    const u16* W = (wsel == 0 ? p.wq : wsel == 1 ? p.wk : p.wv) + (e0 >> 16) * 256;
    u32 k = 0;
    if (n > 1 && w == 0 && lane_id() == 0) k = __hip_atomic_fetch_add(cnt + C_TICK, 1u, RLX);
    if (type == 0) gemm_unit<0>(p.ws, w, E_F, e1, 32, W, W, 4096, 32, e2);
    else if (type == 1) gemm_unit<1>(p.ws, w, E_V, e1, 32, W, W, 4096, 32, e2);
    else if (type == 2) gemm_unit<2>(p.ws, w, E_F, e1, 32, W, W, 4096, 32, e2);
    else gemm_unit<3>(p.ws, w, E_V, e1, 32, W, W, 4096, 32, e2);
    const int l = lane_id();
    if (w == 0 && l == 0) ctl[0] = 160 + k;
    asm volatile("s_waitcnt vmcnt(0) lgkmcnt(0)\n\ts_barrier" ::: "memory");
    t = rfl(ctl[0]);
    if (w == 0) {
      const u32 ss = l == 0 ? e3 : l == 1 ? e4 : 0;
      gu32* c2 = (gu32*)(args().ws + O_CNT);
      if (ss && __hip_atomic_fetch_add(c2 + C_CNT + (ss >> 16) * 32, 1u, RLX) + 1 == (ss & 0xFFFF)) __hip_atomic_store(c2 + C_FLAG + (ss >> 16), 1u, RLX);
    }
    asm volatile("s_barrier" ::: "memory");
  }
  asm volatile("" : "+s"(w));
  if (role == 0) role_s<0>(args(), id, w);
  else if (role == 1) role_s<1>(args(), id, w);
  else if (role == 2) role_s<2>(args(), id, w);
  else {
    for (u32 s = role == 5 ? 0 : id >> 3; s < 64; s += role == 5 ? 1 : 2) {
      asm volatile("" : "+s"(w));
      const Params p = args();
      gu32* cnt = (gu32*)(p.ws + O_CNT);
      const int l = lane_id();
      if (role == 3) {
        wait_ge(cnt + C_FLAG + ((l & 1) ? F_Q : F_KV) + (s >> 2), 1u);
        attn_pair(p.ws, s, (id & 7) * 4 + w);
      } else {
        wait_ge(cnt + (role == 4 ? C_PW : C_PD) + (l & 31) * 32, s + 1);
        if (role == 4) reduce1<4>(p, s * 8 + (id & 7), w, (float*)lds); else reduce2<4>(p, s * 8 + id, w);
      }
      if (role != 5) {
        asm volatile("s_waitcnt vmcnt(0)\n\ts_barrier" ::: "memory");
        if (w == 0 && l == 0) __hip_atomic_store(cnt + (role == 3 ? C_PA : C_P1) + id * 32, s + 1, RLX);
      }
    }
  }
}

#define D1 18
#define D2 18
#define D3 10
extern "C" void kernel_launch(void* const* d_in, const int* in_sizes, int n_in, void* d_out, int out_size, void* d_ws, size_t ws_size, hipStream_t stream) {
  Params p;
  p.x = (const u16*)d_in[0]; p.mem0 = (const u16*)d_in[1]; p.wq = (const u16*)d_in[2]; p.wk = (const u16*)d_in[3]; p.wv = (const u16*)d_in[4];
  p.wo = (const u16*)d_in[5]; p.wg = (const u16*)d_in[8]; p.wu = (const u16*)d_in[9]; p.wd = (const u16*)d_in[10];
  p.out = (u16*)d_out; p.ws = (char*)(((size_t)d_ws + 2 * MB - 1) & ~(size_t)(2 * MB - 1)); p.flip = 0; p.pad = 0; p.phase = 0; p.d1 = D1; p.d2 = D2; p.d3 = D3;
  hipFuncSetAttribute((const void*)main_kernel, hipFuncAttributeMaxDynamicSharedMemorySize, LDS_BYTES);
  prep_kernel<<<512, 256, 0, stream>>>(p);
  main_kernel<<<256, 256, LDS_BYTES, stream>>>(p);
}
```
